```python
import torch

def _softmax(x, axis=-1):
    return torch.softmax(x, dim=axis)
def _gelu(x):
    return torch.nn.functional.gelu(x, approximate="tanh")
def _silu(x):
    return torch.nn.functional.silu(x)
def _sigmoid(x):
    return torch.sigmoid(x)
def _layernorm(x, axis=-1, eps=1e-5):
    m = x.mean(dim=axis, keepdim=True)
    v = x.var(dim=axis, keepdim=True, unbiased=False)
    return (x - m) / torch.sqrt(v + eps)
def _scatter_add(tgt, idx, src, axis=0):
    return tgt.clone().index_add_(axis, idx, src)
def _segment_sum(src, seg, n):
    out = torch.zeros((n,) + src.shape[1:], dtype=src.dtype)
    return out.index_add_(0, seg, src)
def _pad(x, pw):
    flat = tuple(v for pair in reversed(pw) for v in pair)
    return torch.nn.functional.pad(x, flat)

kernel_name = 'interleave_layers__linear_attn_block__ssm_conv_s4__prompt'

def setup_inputs():

    _w = (lambda a: (lambda t: t.to(torch.bfloat16) if t.is_floating_point() else t)(torch.as_tensor(a)))
    def _A_setup():
        B, S, D = 1, 2048, 512
        return {
            "x": _w((lambda *s: torch.randn(*s, dtype=torch.bfloat16) * 0.05)(B, S, D)),
            "wqkv": _w((lambda *s: torch.randn(*s, dtype=torch.bfloat16) * 0.05)(D, 3 * D)),
            "wo": _w((lambda *s: torch.randn(*s, dtype=torch.bfloat16) * 0.05)(D, D)),
        }
    _A_in = _A_setup()

    def _B_setup():
        B, L, D, N = 1, 2048, 512, 128
        A_re = -(lambda _x: torch.exp(torch.as_tensor(_x)))((lambda *s: torch.randn(*s, dtype=torch.bfloat16) * 1.6)(D, N))
        A_im = torch.pi * (lambda *s: torch.randn(*s, dtype=torch.bfloat16) * 0.1)(D, N)
        B_re = (lambda *s: torch.randn(*s, dtype=torch.bfloat16) * 0.1)(D, N)
        B_im = (lambda *s: torch.randn(*s, dtype=torch.bfloat16) * 0.1)(D, N)
        C_re = (lambda *s: torch.randn(*s, dtype=torch.bfloat16) * 0.1)(D, N)
        C_im = (lambda *s: torch.randn(*s, dtype=torch.bfloat16) * 0.1)(D, N)
        log_dt = (lambda *s: torch.randn(*s, dtype=torch.bfloat16) * 0.1)(D) * 0.5 - 4.0
        Dskip = (lambda *s: torch.randn(*s, dtype=torch.bfloat16) * 0.1)(D)
        return {
            "u": _w((lambda *s: torch.randn(*s, dtype=torch.bfloat16) * 0.05)(B, L, D)),
            "A_re": _w(A_re),
            "A_im": _w(A_im),
            "B_re": _w(B_re),
            "B_im": _w(B_im),
            "C_re": _w(C_re),
            "C_im": _w(C_im),
            "log_dt": _w(log_dt),
            "Dskip": _w(Dskip),
        }
    _B_in = _B_setup()

    return {
        "A_x": _A_in["x"],
        "A_wqkv": _A_in["wqkv"],
        "A_wo": _A_in["wo"],
        "B_u": _B_in["u"],
        "B_A_re": _B_in["A_re"],
        "B_A_im": _B_in["A_im"],
        "B_B_re": _B_in["B_re"],
        "B_B_im": _B_in["B_im"],
        "B_C_re": _B_in["C_re"],
        "B_C_im": _B_in["C_im"],
        "B_log_dt": _B_in["log_dt"],
        "B_Dskip": _B_in["Dskip"],
    }

def reference(A_x, A_wqkv, A_wo, B_u, B_A_re, B_A_im, B_B_re, B_B_im, B_C_re, B_C_im, B_log_dt, B_Dskip):
    def _A_ref(x, wqkv, wo):
        B, S, D = x.shape
        H = 32; Dh = D // H
        qkv = (lambda x, *d: x.permute(*d))((x @ wqkv).reshape(B, S, 3, H, Dh), 2, 0, 3, 1, 4)
        q, k, v = qkv[0], qkv[1], qkv[2]
        phi = lambda t: torch.where(t > 0, t + 1, torch.exp(t)) + 1e-6
        pq, pk = phi(q), phi(k)
        kv = torch.einsum("bhsd,bhse->bhde", pk, v)
        z = torch.einsum("bhsd->bhd", pk)
        num = torch.einsum("bhqd,bhde->bhqe", pq, kv)
        den = torch.einsum("bhqd,bhd->bhq", pq, z)[..., None] + 1e-6
        o = num / den
        return (lambda x, *d: x.permute(*d))(o, 0, 2, 1, 3).reshape(B, S, D) @ wo

    def _B_ref(u, A_re, A_im, B_re, B_im, C_re, C_im, log_dt, Dskip):
        Bsz, L, D = u.shape
        dt = torch.exp(log_dt)[:, None]
        A = A_re + 1j * A_im
        Bc = B_re + 1j * B_im
        Cc = C_re + 1j * C_im
        logdA = dt * A
        dB = dt * Bc
        l = (lambda x, dt: x.to(dt))(torch.arange(L), A_re.dtype)
        P = torch.exp(logdA[:, :, None] * l[None, None, :])
        K = torch.einsum("dn,dnl,dn->dl", Cc, P, dB).real
        L2 = 2 * L
        Kp = _pad(K, ((0, 0), (0, L)))
        up = (lambda x, *d: x.permute(*d))(u, 0, 2, 1)
        up = _pad(up, ((0, 0), (0, 0), (0, L)))
        Kf = (lambda x, axes=(-2,-1): torch.fft.rfft2(x.float(), dim=axes))(Kp[:, None, :], axes=(-2, -1))
        Uf = (lambda x, axes=(-2,-1): torch.fft.rfft2(x.float(), dim=axes))(up[:, :, None, :], axes=(-2, -1))
        Yf = Uf * Kf[None, :, :, :]
        y = (lambda x, s, axes=(-2,-1): torch.fft.irfft2(x, s=s, dim=axes).to(torch.bfloat16))(Yf, (1, L2), axes=(-2, -1))[:, :, 0, :L]
        y = (lambda x, *d: x.permute(*d))(y, 0, 2, 1) + u * Dskip[None, None, :]
        return y

    x = A_x
    x = x + _A_ref(_layernorm(x, axis=-1), A_wqkv, A_wo)
    x = x + _B_ref(_layernorm(x, axis=-1), B_A_re, B_A_im, B_B_re, B_B_im, B_C_re, B_C_im, B_log_dt, B_Dskip)
    x = x + _A_ref(_layernorm(x, axis=-1), A_wqkv, A_wo)
    x = x + _B_ref(_layernorm(x, axis=-1), B_A_re, B_A_im, B_B_re, B_B_im, B_C_re, B_C_im, B_log_dt, B_Dskip)
    return x
```

```cpp
#include <hip/hip_runtime.h>
#include <hip/hip_bf16.h>
#include <stdint.h>

typedef unsigned short u16;
#define S_ 2048
#define D_ 512
#define H_ 32
#define DH 16
#define N_ 128

__device__ __forceinline__ float bf2f(u16 v){ return __uint_as_float(((unsigned)v)<<16); }
__device__ __forceinline__ u16 f2bf(float f){ __hip_bfloat16 b = __float2bfloat16(f); return *reinterpret_cast<u16*>(&b); }
__device__ __forceinline__ float rbf(float f){ return bf2f(f2bf(f)); }

__global__ void ln_k(const u16* x, u16* u){
  int t = blockIdx.x; int lane = threadIdx.x;
  float v[8]; float s=0;
  for(int i=0;i<8;i++){ v[i]=bf2f(x[t*D_+lane*8+i]); s+=v[i]; }
  for(int o=32;o;o>>=1) s+=__shfl_xor(s,o);
  float mean=s/D_; float q=0;
  for(int i=0;i<8;i++){ float d=v[i]-mean; q+=d*d; }
  for(int o=32;o;o>>=1) q+=__shfl_xor(q,o);
  float var=q/D_;
  float mb=rbf(mean), vb=rbf(var);
  float sd=rbf(sqrtf(rbf(vb+1e-5f)));
  for(int i=0;i<8;i++){ u[t*D_+lane*8+i]=f2bf(rbf(v[i]-mb)/sd); }
}
__global__ void gemm_k(const u16* A,const u16* B,u16* C,int M,int N,int K){
  int n=blockIdx.x*64+threadIdx.x; int m=blockIdx.y;
  float acc=0;
  for(int k=0;k<K;k++) acc+=bf2f(A[m*K+k])*bf2f(B[k*N+n]);
  C[m*N+n]=f2bf(acc);
}
__device__ __forceinline__ float phi(float t){ float r = t>0 ? rbf(t+1.0f) : rbf(expf(t)); return rbf(r+1e-6f); }
__global__ void kv_k(const u16* qkv,u16* kv,u16* z){
  int h=blockIdx.x; int d=threadIdx.x/16, e=threadIdx.x%16;
  float acc=0, zz=0;
  for(int s=0;s<S_;s++){ float pk=phi(bf2f(qkv[s*1536+512+h*16+d])); float v=bf2f(qkv[s*1536+1024+h*16+e]); acc+=pk*v; zz+=pk; }
  kv[h*256+d*16+e]=f2bf(acc); if(e==0) z[h*16+d]=f2bf(zz);
}
__global__ void o_k(const u16* qkv,const u16* kv,const u16* z,u16* o){
  int t=blockIdx.x; int c=threadIdx.x;
  int h=c/16,e=c%16; float num=0,den=0;
  for(int d=0;d<16;d++){ float pq=phi(bf2f(qkv[t*1536+h*16+d])); num+=pq*bf2f(kv[h*256+d*16+e]); den+=pq*bf2f(z[h*16+d]); }
  float nb=rbf(num), db=rbf(rbf(den)+1e-6f);
  o[t*D_+c]=f2bf(nb/db);
}
__global__ void add_k(const u16* a,const u16* b,u16* c,int n){ int i=blockIdx.x*256+threadIdx.x; if(i<n) c[i]=f2bf(bf2f(a[i])+bf2f(b[i])); }
__global__ void kgen_k(const u16* Are,const u16* Aim,const u16* Bre,const u16* Bim,const u16* Cre,const u16* Cim,const u16* logdt,float* K){
  int d=blockIdx.y; int l=blockIdx.x*256+threadIdx.x;
  float dt=rbf(expf(bf2f(logdt[d]))); float lf=rbf((float)l);
  float acc=0;
  for(int n=0;n<N_;n++){
    float ar=dt*bf2f(Are[d*N_+n]), ai=dt*bf2f(Aim[d*N_+n]);
    float br=dt*bf2f(Bre[d*N_+n]), bi=dt*bf2f(Bim[d*N_+n]);
    float cr=bf2f(Cre[d*N_+n]), ci=bf2f(Cim[d*N_+n]);
    float wr=cr*br-ci*bi, wi=cr*bi+ci*br;
    float e=expf(ar*lf); float s,c; sincosf(ai*lf,&s,&c);
    acc+=e*(wr*c-wi*s);
  }
  K[d*S_+l]=acc;
}
__global__ void conv_k(const u16* u,const float* K,const u16* Dskip,const u16* x,u16* out){
  int t=blockIdx.x; int d=threadIdx.x;
  float acc=0;
  for(int j=0;j<=t;j++) acc+=K[d*S_+j]*bf2f(u[(t-j)*D_+d]);
  float y=rbf(acc); float us=rbf(bf2f(u[t*D_+d])*bf2f(Dskip[d]));
  float yy=rbf(y+us);
  out[t*D_+d]=f2bf(bf2f(x[t*D_+d])+yy);
}

extern "C" void kernel_launch(void* const* d_in, const int* in_sizes, int n_in,
                              void* d_out, int out_size, void* d_ws, size_t ws_size,
                              hipStream_t stream) {
  const u16* x=(const u16*)d_in[0]; const u16* wqkv=(const u16*)d_in[1]; const u16* wo=(const u16*)d_in[2];
  const u16* Are=(const u16*)d_in[4]; const u16* Aim=(const u16*)d_in[5]; const u16* Bre=(const u16*)d_in[6]; const u16* Bim=(const u16*)d_in[7];
  const u16* Cre=(const u16*)d_in[8]; const u16* Cim=(const u16*)d_in[9]; const u16* logdt=(const u16*)d_in[10]; const u16* Dskip=(const u16*)d_in[11];
  char* ws=(char*)d_ws;
  u16* u=(u16*)(ws); u16* qkv=(u16*)(ws+(4<<20)); u16* kv=(u16*)(ws+(12<<20)); u16* z=(u16*)(ws+(13<<20));
  u16* o=(u16*)(ws+(14<<20)); u16* at=(u16*)(ws+(18<<20)); u16* xa=(u16*)(ws+(22<<20)); u16* xb=(u16*)(ws+(26<<20));
  float* K=(float*)(ws+(32<<20));
  kgen_k<<<dim3(S_/256,D_),256,0,stream>>>(Are,Aim,Bre,Bim,Cre,Cim,logdt,K);
  const u16* cur=x;
  for(int it=0;it<2;it++){
    ln_k<<<S_,64,0,stream>>>(cur,u);
    gemm_k<<<dim3(1536/64,S_),64,0,stream>>>(u,wqkv,qkv,S_,1536,512);
    kv_k<<<H_,256,0,stream>>>(qkv,kv,z);
    o_k<<<S_,512,0,stream>>>(qkv,kv,z,o);
    gemm_k<<<dim3(512/64,S_),64,0,stream>>>(o,wo,at,S_,512,512);
    add_k<<<S_*D_/256,256,0,stream>>>(cur,at,xa,S_*D_);
    ln_k<<<S_,64,0,stream>>>(xa,u);
    u16* dst = it==0 ? xb : (u16*)d_out;
    conv_k<<<S_,512,0,stream>>>(u,K,Dskip,xa,dst);
    cur=xb;
  }
}
```

```cpp
#include <hip/hip_runtime.h>
#include <stdint.h>
#pragma clang diagnostic ignored "-Winline-asm"

__global__ void __launch_bounds__(256, 1) __attribute__((aligned(4096)))
interleave_mega(const void* x0, const void* wqkv, const void* wo, const void* Are, const void* Aim, const void* Bre, const void* Bim,
     const void* Cre, const void* Cim, const void* logdt, const void* Dskip, void* out, void* ws)
{
  __shared__ __attribute__((aligned(1024))) char smem[163840];
  asm volatile(
    "s_mov_b64 s[4:5], %0\n"
    "s_mov_b32 s6, %1\n"
    "v_mov_b32 v0, %2\n"
    "s_getpc_b64 s[78:79]\n"
    "Lpc_%=:\n"
    "s_load_dwordx2 s[8:9], s[4:5], 0\n"
    "s_load_dwordx2 s[12:13], s[4:5], 8\n"
    "s_load_dwordx2 s[20:21], s[4:5], 0x60\n"
    "s_load_dwordx2 s[16:17], s[4:5], 16\n"
    "s_load_dwordx16 s[28:43], s[4:5], 24\n"
    "s_lshr_b32 s47, s6, 3\n"
    "s_and_b32 s55, s47, 3\n"
    "s_lshl_b32 s55, s55, 15\n"
    "s_mov_b32 s56, Lend_%=-Lpc_%=-4\n"
    "s_and_b32 s46, s6, 7\n"
    "s_mov_b32 s10, 0x200c00\n"
    "s_mov_b32 s11, 0x20000\n"
    "s_mov_b32 s14, 0x180c00\n"
    "s_mov_b32 s15, 0x20000\n"
    "v_lshlrev_b32 v190, 7, v0\n"
    "v_add_u32 v190, s55, v190\n"
    "s_lshr_b32 s55, s46, 1\n"
    "v_min_u32 v190, s56, v190\n"
    "global_load_dword v191, v190, s[78:79]\n"
    "s_lshr_b32 s56, s47, 2\n"
    "s_lshl_b32 s55, s55, 3\n"
    "s_add_u32 s48, s55, s56\n"
    "s_mov_b32 s66, s56\n"
    "s_and_b32 s66, s66, 3\n"
    "s_and_b32 s55, s46, 1\n"
    "s_lshl_b32 s55, s55, 2\n"
    "s_and_b32 s56, s47, 3\n"
    "s_add_u32 s49, s55, s56\n"
    "v_lshrrev_b32 v16, 6, v0\n"
    "v_and_b32 v8, 63, v0\n"
    "v_readfirstlane_b32 s7, v16\n"
    "v_and_b32 v9, 15, v0\n"
    "s_lshl_b32 s51, s7, 12\n"
    "s_lshl_b32 s52, s48, 16\n"
    "s_lshl_b32 s55, s7, 14\n"
    "s_add_u32 s52, s52, s55\n"
    "s_lshl_b32 s55, s7, 5\n"
    "s_and_b32 s56, s55, 63\n"
    "s_mul_i32 s56, s56, 0xc00\n"
    "s_lshr_b32 s57, s7, 1\n"
    "s_lshl_b32 s57, s57, 10\n"
    "s_add_u32 s56, s56, s57\n"
    "s_lshl_b32 s57, s49, 7\n"
    "s_add_u32 s56, s56, s57\n"
    "s_add_u32 s53, s56, 0x400\n"
    "s_mul_i32 s55, s55, 0xc00\n"
    "s_add_u32 s54, s55, s57\n"
    "s_add_u32 s67, s66, 0\n"
    "s_and_b32 s67, s67, 3\n"
    "s_lshl_b32 s67, s67, 8\n"
    "s_add_u32 s67, s67, s52\n"
    "s_lshl_b32 s59, s66, 1\n"
    "s_add_u32 s59, s59, 0\n"
    "s_and_b32 s59, s59, 7\n"
    "s_mul_i32 s59, s59, 0x30000\n"
    "s_add_u32 s59, s59, s53\n"
    "v_bfe_u32 v10, v0, 4, 2\n"
    "v_lshrrev_b32 v17, 3, v8\n"
    "v_and_b32 v18, 7, v8\n"
    "v_xor_b32 v19, v18, v10\n"
    "v_lshlrev_b32 v19, 4, v19\n"
    "v_lshl_add_u32 v19, v17, 10, v19\n"
    "v_add_u32 v20, 0xc00, v19\n"
    "v_add_u32 v22, 0x480, v19\n"
    "v_xor_b32 v19, 64, v19\n"
    "v_add_u32 v21, 0x2800, v19\n"
    "v_add_u32 v23, 0x2080, v19\n"
    "v_and_b32 v19, 1, v10\n"
    "v_lshlrev_b32 v19, 5, v19\n"
    "v_lshlrev_b32 v18, 4, v18\n"
    "v_xor_b32 v19, v19, v18\n"
    "v_mul_u32_u24 v17, 0xc00, v17\n"
    "v_add_u32 v19, v19, v17\n"
    "v_add_u32 v24, 0xc00, v19\n"
    "v_add_u32 v26, 0xc400, v19\n"
    "v_xor_b32 v19, 64, v19\n"
    "v_add_u32 v25, 0x6800, v19\n"
    "v_add_u32 v27, 0x12000, v19\n"
    "s_waitcnt lgkmcnt(0)\n"
    "s_sub_u32 s8, s8, 0xc00\n"
    "s_subb_u32 s9, s9, 0\n"
    "s_and_b32 s9, s9, 0xffff\n"
    "s_sub_u32 s12, s12, 0xc00\n"
    "s_subb_u32 s13, s13, 0\n"
    "s_and_b32 s13, s13, 0xffff\n"
    "s_add_u32 m0, s51, 0\n"
    "s_nop 0\n"
    "buffer_load_dwordx4 v20, s[8:11], s67 offen offset:0 lds\n"
    "buffer_load_dwordx4 v21, s[8:11], s67 offen offset:1024 lds\n"
    "buffer_load_dwordx4 v22, s[8:11], s67 offen offset:2048 lds\n"
    "buffer_load_dwordx4 v23, s[8:11], s67 offen offset:3072 lds\n"
    "s_add_u32 m0, s51, 0x10000\n"
    "s_nop 0\n"
    "buffer_load_dwordx4 v24, s[12:15], s59 offen offset:0 lds\n"
    "buffer_load_dwordx4 v25, s[12:15], s59 offen offset:1024 lds\n"
    "buffer_load_dwordx4 v26, s[12:15], s59 offen offset:2048 lds\n"
    "buffer_load_dwordx4 v27, s[12:15], s59 offen offset:3072 lds\n"
    "s_and_b32 s17, s17, 0xffff\n"
    "s_mov_b32 s18, 0x80000\n"
    "s_mov_b32 s19, 0x20000\n"
    "s_lshl_b32 s55, s49, 2\n"
    "s_add_u32 s50, s55, s7\n"
    "s_mov_b32 s68, 0x3f800008\n"
    "s_mov_b32 s69, 0x3f800008\n"
    "s_mov_b32 s70, 0x3fb8aa3b\n"
    "s_mov_b32 s71, 0xbfb8aa47\n"
    "s_mov_b32 s72, 0x358637bd\n"
    "s_mov_b32 s73, 0x358637bd\n"
    "s_add_u32 s55, s7, 0\n"
    "s_and_b32 s55, s55, 3\n"
    "s_lshl_b32 s55, s55, 12\n"
    "v_mov_b32 v12, 0x3f803f80\n"
    "v_mov_b32 v13, 0x3f803f80\n"
    "v_mov_b32 v14, 0x3f803f80\n"
    "v_mov_b32 v15, 0x3f803f80\n"
    "v_mov_b32 v1, 0xbfb8aa47\n"
    "v_lshrrev_b32 v36, 1, v9\n"
    "v_xor_b32 v36, v36, v10\n"
    "v_lshlrev_b32 v36, 4, v36\n"
    "v_lshl_add_u32 v36, v9, 7, v36\n"
    "v_add_u32 v28, s55, v36\n"
    "s_add_u32 s55, s7, 1\n"
    "s_and_b32 s55, s55, 3\n"
    "s_lshl_b32 s55, s55, 12\n"
    "v_xor_b32 v32, 64, v28\n"
    "v_add_u32 v29, s55, v36\n"
    "s_add_u32 s55, s7, 2\n"
    "s_and_b32 s55, s55, 3\n"
    "s_lshl_b32 s55, s55, 12\n"
    "v_xor_b32 v33, 64, v29\n"
    "v_add_u32 v30, s55, v36\n"
    "s_add_u32 s55, s7, 3\n"
    "s_and_b32 s55, s55, 3\n"
    "s_lshl_b32 s55, s55, 12\n"
    "s_lshl_b32 s24, s46, 3\n"
    "v_xor_b32 v34, 64, v30\n"
    "v_add_u32 v31, s55, v36\n"
    "s_lshr_b32 s55, s47, 2\n"
    "s_add_u32 s24, s24, s55\n"
    "s_and_b32 s45, s47, 3\n"
    "s_lshl_b32 s101, s7, 6\n"
    "s_lshl_b32 s55, s45, 8\n"
    "s_add_u32 s101, s101, s55\n"
    "s_add_u32 s55, s51, 0x10000\n"
    "v_xor_b32 v35, 64, v31\n"
    "v_lshrrev_b32 v36, 2, v9\n"
    "v_lshl_add_u32 v38, v10, 3, v36\n"
    "v_lshlrev_b32 v38, 7, v38\n"
    "v_lshrrev_b32 v36, 1, v36\n"
    "v_and_b32 v37, 1, v10\n"
    "v_lshl_or_b32 v36, v37, 1, v36\n"
    "v_xor_b32 v36, s7, v36\n"
    "v_lshl_add_u32 v38, v36, 5, v38\n"
    "v_and_b32 v36, 3, v8\n"
    "v_lshl_add_u32 v38, v36, 3, v38\n"
    "v_add_u32 v38, 0x10000, v38\n"
    "v_add_u32 v39, 0x10000, v38\n"
    "v_and_b32 v41, 1, v10\n"
    "v_lshlrev_b32 v41, 1, v41\n"
    "v_and_b32 v40, 3, v8\n"
    "v_xor_b32 v40, v40, v41\n"
    "v_lshlrev_b32 v40, 4, v40\n"
    "v_lshrrev_b32 v41, 2, v8\n"
    "v_lshl_add_u32 v42, v41, 10, v40\n"
    "v_add_u32 v43, 0x3c00, v42\n"
    "v_add_u32 v44, 0x7800, v42\n"
    "v_add_u32 v45, 0xb400, v42\n"
    "v_lshrrev_b32 v41, 2, v9\n"
    "v_and_b32 v40, 1, v10\n"
    "v_lshl_add_u32 v41, v40, 2, v41\n"
    "v_lshlrev_b32 v46, 5, v40\n"
    "v_lshrrev_b32 v40, 1, v10\n"
    "v_lshl_add_u32 v40, v40, 4, v41\n"
    "v_lshl_add_u32 v46, v40, 6, v46\n"
    "v_and_b32 v41, 3, v8\n"
    "v_lshl_add_u32 v46, v41, 3, v46\n"
    "v_add_u32 v46, s55, v46\n"
    "s_add_u32 s55, s7, 0\n"
    "s_and_b32 s55, s55, 3\n"
    "s_lshl_b32 s55, s55, 6\n"
    "s_add_u32 s55, s55, 0x27800\n"
    "v_add_u32 v47, 0x10000, v46\n"
    "v_xor_b32 v49, 32, v46\n"
    "v_add_u32 v48, 0x10000, v49\n"
    "v_lshl_add_u32 v50, v9, 2, s55\n"
    "s_add_u32 s55, s7, 1\n"
    "s_and_b32 s55, s55, 3\n"
    "s_lshl_b32 s55, s55, 6\n"
    "s_add_u32 s55, s55, 0x27800\n"
    "v_lshl_add_u32 v51, v9, 2, s55\n"
    "s_add_u32 s55, s7, 2\n"
    "s_and_b32 s55, s55, 3\n"
    "s_lshl_b32 s55, s55, 6\n"
    "s_add_u32 s55, s55, 0x27800\n"
    "v_lshl_add_u32 v52, v9, 2, s55\n"
    "s_add_u32 s55, s7, 3\n"
    "s_and_b32 s55, s55, 3\n"
    "s_lshl_b32 s55, s55, 6\n"
    "s_add_u32 s55, s55, 0x27800\n"
    "v_lshl_add_u32 v53, v9, 2, s55\n"
    "s_add_u32 s55, s7, 0\n"
    "v_lshlrev_b32 v54, 4, v9\n"
    "v_lshrrev_b32 v55, 1, v10\n"
    "v_lshl_add_u32 v54, v55, 9, v54\n"
    "v_and_b32 v56, 1, v10\n"
    "v_add_u32 v56, s55, v56\n"
    "s_add_u32 s55, s7, 2\n"
    "v_and_b32 v57, 1, v56\n"
    "v_lshl_add_u32 v57, v57, 8, v54\n"
    "v_bfe_u32 v56, v56, 1, 1\n"
    "v_lshl_add_u32 v57, v56, 15, v57\n"
    "v_and_b32 v58, 1, v10\n"
    "v_add_u32 v58, s55, v58\n"
    "s_add_u32 s55, s7, 0\n"
    "s_and_b32 s55, s55, 3\n"
    "s_lshl_b32 s55, s55, 6\n"
    "s_add_u32 s55, s55, 0x27800\n"
    "v_and_b32 v59, 1, v58\n"
    "v_lshl_add_u32 v59, v59, 8, v54\n"
    "v_bfe_u32 v58, v58, 1, 1\n"
    "v_lshl_add_u32 v59, v58, 15, v59\n"
    "v_lshl_add_u32 v60, v10, 4, s55\n"
    "s_add_u32 s55, s7, 1\n"
    "s_and_b32 s55, s55, 3\n"
    "s_lshl_b32 s55, s55, 6\n"
    "s_add_u32 s55, s55, 0x27800\n"
    "v_lshl_add_u32 v61, v10, 4, s55\n"
    "s_add_u32 s55, s7, 2\n"
    "s_and_b32 s55, s55, 3\n"
    "s_lshl_b32 s55, s55, 6\n"
    "s_add_u32 s55, s55, 0x27800\n"
    "v_lshl_add_u32 v62, v10, 4, s55\n"
    "s_add_u32 s55, s7, 3\n"
    "s_and_b32 s55, s55, 3\n"
    "s_lshl_b32 s55, s55, 6\n"
    "s_add_u32 s55, s55, 0x27800\n"
    "v_lshl_add_u32 v63, v10, 4, s55\n"
    "v_lshlrev_b32 v64, 2, v8\n"
    "v_cmp_gt_u32 vcc, 16, v8\n"
    "v_mov_b32 v66, 0x7ffffff0\n"
    "s_nop 0\n"
    "v_cndmask_b32 v65, v66, v64, vcc\n"
    "s_and_b32 s21, s21, 0xffff\n"
    "s_mov_b32 s22, 0x2000000\n"
    "s_mov_b32 s23, 0x20000\n"
    "buffer_load_dword v73, off, s[20:23], 0 sc1\n"
    "v_mov_b32 v68, 0\n"
    "v_mov_b32 v69, 0\n"
    "v_mov_b32 v70, 0\n"
    "v_mov_b32 v71, 0\n"
    "v_lshlrev_b32 v67, 4, v0\n"
    "s_cmp_lg_u32 s48, 0\n"
    "s_cbranch_scc1 Lnoinit_6_%=\n"
    "s_lshl_b32 s55, s49, 12\n"
    "s_cmp_eq_u32 s49, 1\n"
    "s_cselect_b32 s57, 0x8000, 0x40000000\n"
    "s_add_u32 s56, s55, 0x10000\n"
    "buffer_store_dwordx4 v[68:71], v67, s[20:23], s56 offen sc1\n"
    "s_add_u32 s56, s57, 0x10000\n"
    "buffer_store_dwordx4 v[68:71], v67, s[20:23], s56 offen sc1\n"
    "s_add_u32 s56, s55, 0x1a000\n"
    "buffer_store_dwordx4 v[68:71], v67, s[20:23], s56 offen sc1\n"
    "s_add_u32 s56, s57, 0x1a000\n"
    "s_cmp_eq_u32 s49, 0\n"
    "v_lshlrev_b32 v72, 8, v0\n"
    "v_add_u32 v72, 0x1000, v72\n"
    "v_cmp_eq_u32 vcc, 0x48, v0\n"
    "v_mov_b32 v74, 0x100\n"
    "v_mov_b32 v75, 0x60\n"
    "v_cndmask_b32 v72, v72, v74, vcc\n"
    "v_mov_b32 v74, 0x7ffffff0\n"
    "v_cmp_lt_u32 vcc, v75, v0\n"
    "s_nop 1\n"
    "v_cndmask_b32 v72, v72, v74, vcc\n"
    "buffer_store_dwordx4 v[68:71], v67, s[20:23], s56 offen sc1\n"
    "s_cselect_b32 s56, 0, 0x40000000\n"
    "buffer_store_dword v68, v72, s[20:23], s56 offen sc1\n"
    "Lnoinit_6_%=:\n"
    "s_lshl_b32 s59, s66, 1\n"
    "s_add_u32 s59, s59, 1\n"
    "s_and_b32 s59, s59, 7\n"
    "s_mul_i32 s59, s59, 0x30000\n"
    "s_add_u32 s59, s59, s53\n"
    "s_add_u32 m0, s51, 0x14000\n"
    "v_mov_b32 v72, 0x7ffffff0\n"
    "buffer_load_dwordx4 v24, s[12:15], s59 offen offset:0 lds\n"
    "buffer_load_dwordx4 v25, s[12:15], s59 offen offset:1024 lds\n"
    "buffer_load_dwordx4 v26, s[12:15], s59 offen offset:2048 lds\n"
    "v_cmp_gt_u32 vcc, 16, v0\n"
    "s_nop 1\n"
    "v_cndmask_b32 v72, v72, v67, vcc\n"
    "buffer_load_dwordx4 v27, s[12:15], s59 offen offset:3072 lds\n"
    "s_add_u32 s59, s66, 0\n"
    "s_and_b32 s59, s59, 3\n"
    "s_mul_i32 s59, s59, 0x60000\n"
    "s_add_u32 s59, s59, s54\n"
    "s_add_u32 m0, s51, 0x18000\n"
    "s_add_u32 s67, s66, 1\n"
    "s_and_b32 s67, s67, 3\n"
    "s_lshl_b32 s67, s67, 8\n"
    "s_add_u32 s67, s67, s52\n"
    "buffer_load_dwordx4 v24, s[12:15], s59 offen offset:0 lds\n"
    "buffer_load_dwordx4 v25, s[12:15], s59 offen offset:1024 lds\n"
    "buffer_load_dwordx4 v26, s[12:15], s59 offen offset:2048 lds\n"
    "buffer_load_dwordx4 v27, s[12:15], s59 offen offset:3072 lds\n"
    "s_add_u32 m0, s51, 0x4000\n"
    "s_lshl_b32 s55, s6, 8\n"
    "s_add_u32 s55, s55, 0x30000\n"
    "buffer_load_dwordx4 v20, s[8:11], s67 offen offset:0 lds\n"
    "buffer_load_dwordx4 v21, s[8:11], s67 offen offset:1024 lds\n"
    "buffer_load_dwordx4 v22, s[8:11], s67 offen offset:2048 lds\n"
    "buffer_load_dwordx4 v23, s[8:11], s67 offen offset:3072 lds\n"
    "buffer_store_dwordx4 v[68:71], v72, s[20:23], s55 offen sc1\n"
    "s_nop 1\n"
    "s_cmp_eq_u32 s7, 0\n"
    "s_cbranch_scc1 Ltp0_%=\n"
    "s_mov_b64 exec, 0\n"
    "s_cmp_eq_u32 s7, 2\n"
    "s_cbranch_scc1 Ltw0_2_%=\n"
    "s_cmp_eq_u32 s7, 3\n"
    "s_cbranch_scc1 Ltw0_3_%=\n"
    "s_branch Lpg1_%=\n"
    "Ltb0_1_0_%=:\n"
    "s_nop 0\n"
    "Ltb0_1_1_%=:\n"
    "s_branch Ltf0_%=\n"
    "Ltw0_2_%=:\n"
    "s_branch Lpg2_%=\n"
    "Ltb0_2_0_%=:\n"
    "s_nop 0\n"
    "Ltb0_2_1_%=:\n"
    "s_branch Ltf0_%=\n"
    "Ltw0_3_%=:\n"
    "s_nop 0\n"
    "Ltb0_3_0_%=:\n"
    "s_nop 0\n"
    "Ltb0_3_1_%=:\n"
    "Ltf0_%=:\n"
    "s_mov_b64 exec, -1\n"
    "Ltp0_%=:\n"
    "s_waitcnt vmcnt(14)\n"
    "s_barrier\n"
    "s_lshl_b32 s59, s66, 1\n"
    "s_add_u32 s59, s59, 2\n"
    "s_and_b32 s59, s59, 7\n"
    "s_mul_i32 s59, s59, 0x30000\n"
    "s_add_u32 s59, s59, s53\n"
    "s_add_u32 m0, s51, 0x1c000\n"
    "ds_read_b64_tr_b16 a[0:1], v38 offset:0\n"
    "buffer_load_dwordx4 v24, s[12:15], s59 offen offset:0 lds\n"
    "buffer_load_dwordx4 v25, s[12:15], s59 offen offset:1024 lds\n"
    "buffer_load_dwordx4 v26, s[12:15], s59 offen offset:2048 lds\n"
    "ds_read_b64_tr_b16 a[2:3], v38 offset:512\n"
    "ds_read_b64_tr_b16 a[64:65], v38 offset:8192\n"
    "ds_read_b64_tr_b16 a[66:67], v38 offset:8704\n"
    "ds_read_b128 v[192:195], v28 offset:0\n"
    "ds_read_b128 v[196:199], v29 offset:0\n"
    "v_mov_b32 v125, 0x7ffffff0\n"
    "v_cmp_eq_u32 vcc, 0, v0\n"
    "ds_read_b128 v[200:203], v30 offset:0\n"
    "v_and_b32 v127, 7, v8\n"
    "v_cndmask_b32 v125, v125, 0, vcc\n"
    "ds_read_b128 v[204:207], v31 offset:0\n"
    "v_lshlrev_b32 v127, 2, v127\n"
    "v_add_u32 v127, 0x400, v127\n"
    "v_lshrrev_b32 v157, 7, v0\n"
    "buffer_load_dwordx4 v27, s[12:15], s59 offen offset:3072 lds\n"
    "s_lshl_b32 s59, s66, 1\n"
    "s_add_u32 s59, s59, 3\n"
    "s_and_b32 s59, s59, 7\n"
    "s_mul_i32 s59, s59, 0x30000\n"
    "s_add_u32 s59, s59, s53\n"
    "s_add_u32 m0, s51, 0x20000\n"
    "ds_read_b64_tr_b16 a[4:5], v38 offset:4096\n"
    "ds_read_b64_tr_b16 a[6:7], v38 offset:4608\n"
    "ds_read_b64_tr_b16 a[68:69], v38 offset:12288\n"
    "s_waitcnt lgkmcnt(9)\n"
    "v_mfma_f32_16x16x32_bf16 v[108:111], v[12:15], a[0:3], 0\n"
    "ds_read_b64_tr_b16 a[70:71], v38 offset:12800\n"
    "ds_read_b128 v[208:211], v32 offset:0\n"
    "s_waitcnt lgkmcnt(9)\n"
    "v_mfma_f32_16x16x32_bf16 v[112:115], v[12:15], a[64:67], 0\n"
    "ds_read_b128 v[212:215], v33 offset:0\n"
    "ds_read_b128 v[216:219], v34 offset:0\n"
    "buffer_load_dwordx4 v24, s[12:15], s59 offen offset:0 lds\n"
    "s_waitcnt lgkmcnt(10)\n"
    "v_mfma_f32_16x16x32_bf16 v[116:119], v[192:195], v[12:15], 0\n"
    "ds_read_b128 v[220:223], v35 offset:0\n"
    "v_mov_b32 v158, 0\n"
    "v_mfma_f32_16x16x32_bf16 v[120:123], v[192:195], v[192:195], 0\n"
    "ds_read_b128 v[224:227], v28 offset:2048\n"
    "v_mov_b32 v159, 0\n"
    "v_mfma_f32_16x16x32_bf16 v[76:79], v[192:195], a[0:3], 0\n"
    "ds_read_b128 v[228:231], v29 offset:2048\n"
    "v_mov_b32 v160, 0\n"
    "v_mfma_f32_16x16x32_bf16 v[92:95], v[192:195], a[64:67], 0\n"
    "ds_read_b128 v[232:235], v30 offset:2048\n"
    "ds_read_b128 v[236:239], v31 offset:2048\n"
    "s_waitcnt lgkmcnt(14)\n"
    "v_mfma_f32_16x16x32_bf16 v[80:83], v[196:199], a[0:3], 0\n"
    "buffer_load_dwordx4 v25, s[12:15], s59 offen offset:1024 lds\n"
    "ds_read_b128 v[240:243], v32 offset:2048\n"
    "v_mfma_f32_16x16x32_bf16 v[96:99], v[196:199], a[64:67], 0\n"
    "v_mov_b32 v161, 0\n"
    "ds_read_b128 v[244:247], v33 offset:2048\n"
    "s_waitcnt lgkmcnt(15)\n"
    "v_mfma_f32_16x16x32_bf16 v[84:87], v[200:203], a[0:3], 0\n"
    "ds_read_b128 v[248:251], v34 offset:2048\n"
    "v_mov_b32 v162, 0\n"
    "v_mfma_f32_16x16x32_bf16 v[100:103], v[200:203], a[64:67], 0\n"
    "ds_read_b128 v[252:255], v35 offset:2048\n"
    "v_mov_b32 v163, 0\n"
    "s_waitcnt lgkmcnt(15)\n"
    "v_mfma_f32_16x16x32_bf16 v[88:91], v[204:207], a[0:3], 0\n"
    "v_mov_b32 v164, 0\n"
    "v_mov_b32 v165, 0\n"
    "buffer_load_dwordx4 v26, s[12:15], s59 offen offset:2048 lds\n"
    "v_mfma_f32_16x16x32_bf16 v[104:107], v[204:207], a[64:67], 0\n"
    "s_waitcnt vmcnt(12) lgkmcnt(12)\n"
    "s_barrier\n"
    "v_mfma_f32_16x16x32_bf16 v[108:111], v[12:15], a[4:7], v[108:111]\n"
    "ds_read_b64_tr_b16 a[8:9], v38 offset:16384\n"
    "ds_read_b64_tr_b16 a[10:11], v38 offset:16896\n"
    "v_mfma_f32_16x16x32_bf16 v[112:115], v[12:15], a[68:71], v[112:115]\n"
    "ds_read_b64_tr_b16 a[72:73], v38 offset:24576\n"
    "buffer_load_dwordx4 v27, s[12:15], s59 offen offset:3072 lds\n"
    "s_waitcnt lgkmcnt(14)\n"
    "v_mfma_f32_16x16x32_bf16 v[116:119], v[208:211], v[12:15], v[116:119]\n"
    "s_add_u32 s59, s66, 1\n"
    "s_and_b32 s59, s59, 3\n"
    "v_mfma_f32_16x16x32_bf16 v[120:123], v[208:211], v[208:211], v[120:123]\n"
    "s_mul_i32 s59, s59, 0x60000\n"
    "s_add_u32 s59, s59, s54\n"
    "v_mfma_f32_16x16x32_bf16 v[76:79], v[208:211], a[4:7], v[76:79]\n"
    "s_add_u32 m0, s51, 0x10000\n"
    "s_add_u32 s67, s66, 2\n"
    "v_mfma_f32_16x16x32_bf16 v[92:95], v[208:211], a[68:71], v[92:95]\n"
    "s_and_b32 s67, s67, 3\n"
    "s_lshl_b32 s67, s67, 8\n"
    "s_waitcnt lgkmcnt(13)\n"
    "v_mfma_f32_16x16x32_bf16 v[80:83], v[212:215], a[4:7], v[80:83]\n"
    "s_add_u32 s67, s67, s52\n"
    "buffer_load_dwordx4 v24, s[12:15], s59 offen offset:0 lds\n"
    "v_mfma_f32_16x16x32_bf16 v[96:99], v[212:215], a[68:71], v[96:99]\n"
    "ds_read_b64_tr_b16 a[74:75], v38 offset:25088\n"
    "ds_read_b64_tr_b16 a[12:13], v38 offset:20480\n"
    "s_waitcnt lgkmcnt(14)\n"
    "v_mfma_f32_16x16x32_bf16 v[84:87], v[216:219], a[4:7], v[84:87]\n"
    "ds_read_b64_tr_b16 a[14:15], v38 offset:20992\n"
    "ds_read_b64_tr_b16 a[76:77], v38 offset:28672\n"
    "v_mfma_f32_16x16x32_bf16 v[100:103], v[216:219], a[68:71], v[100:103]\n"
    "ds_read_b64_tr_b16 a[78:79], v38 offset:29184\n"
    "ds_read_b64_tr_b16 a[128:129], v38 offset:32768\n"
    "s_waitcnt lgkmcnt(15)\n"
    "v_mfma_f32_16x16x32_bf16 v[88:91], v[220:223], a[4:7], v[88:91]\n"
    "ds_read_b64_tr_b16 a[130:131], v38 offset:33280\n"
    "ds_read_b64_tr_b16 a[132:133], v38 offset:36864\n"
    "v_mfma_f32_16x16x32_bf16 v[104:107], v[220:223], a[68:71], v[104:107]\n"
    "buffer_load_dwordx4 v25, s[12:15], s59 offen offset:1024 lds\n"
    "ds_read_b64_tr_b16 a[134:135], v38 offset:37376\n"
    "s_waitcnt lgkmcnt(10)\n"
    "v_mfma_f32_16x16x32_bf16 v[108:111], v[12:15], a[8:11], v[108:111]\n"
    "ds_read_b64_tr_b16 a[136:137], v38 offset:40960\n"
    "ds_read_b64_tr_b16 a[138:139], v38 offset:41472\n"
    "v_mfma_f32_16x16x32_bf16 v[116:119], v[224:227], v[12:15], v[116:119]\n"
    "ds_read_b64_tr_b16 a[140:141], v38 offset:45056\n"
    "ds_read_b64_tr_b16 a[142:143], v38 offset:45568\n"
    "s_waitcnt lgkmcnt(12)\n"
    "v_mfma_f32_16x16x32_bf16 v[112:115], v[12:15], a[72:75], v[112:115]\n"
    "v_readfirstlane_b32 s44, v73\n"
    "v_mfma_f32_16x16x32_bf16 v[120:123], v[224:227], v[224:227], v[120:123]\n"
    "buffer_load_dwordx4 v26, s[12:15], s59 offen offset:2048 lds\n"
    "v_mfma_f32_16x16x32_bf16 v[76:79], v[224:227], a[8:11], v[76:79]\n"
    "s_waitcnt lgkmcnt(6)\n"
    "v_mfma_f32_16x16x32_bf16 v[152:155], a[128:131], v[12:15], 0\n"
    "v_mfma_f32_16x16x32_bf16 v[136:139], a[128:131], v[192:195], 0\n"
    "v_mfma_f32_16x16x32_bf16 v[140:143], a[128:131], v[196:199], 0\n"
    "v_mfma_f32_16x16x32_bf16 v[144:147], a[128:131], v[200:203], 0\n"
    "buffer_load_dwordx4 v27, s[12:15], s59 offen offset:3072 lds\n"
    "s_add_u32 m0, s51, 0x8000\n"
    "v_mfma_f32_16x16x32_bf16 v[148:151], a[128:131], v[204:207], 0\n"
    "s_add_u32 s55, s44, 1\n"
    "s_lshl_b32 s56, s49, 2\n"
    "s_waitcnt lgkmcnt(4)\n"
    "v_mfma_f32_16x16x32_bf16 v[152:155], a[132:135], v[12:15], v[152:155]\n"
    "s_add_u32 s56, s56, 0x400\n"
    "s_cmp_eq_u32 s48, 0\n"
    "v_mfma_f32_16x16x32_bf16 v[136:139], a[132:135], v[208:211], v[136:139]\n"
    "s_cselect_b32 s56, s56, 0x40000000\n"
    "s_lshl_b32 s59, s66, 1\n"
    "v_mfma_f32_16x16x32_bf16 v[140:143], a[132:135], v[212:215], v[140:143]\n"
    "s_add_u32 s59, s59, 4\n"
    "s_and_b32 s59, s59, 7\n"
    "v_mfma_f32_16x16x32_bf16 v[144:147], a[132:135], v[216:219], v[144:147]\n"
    "buffer_load_dwordx4 v20, s[8:11], s67 offen offset:0 lds\n"
    "s_mul_i32 s59, s59, 0x30000\n"
    "v_mfma_f32_16x16x32_bf16 v[148:151], a[132:135], v[220:223], v[148:151]\n"
    "s_add_u32 s59, s59, s53\n"
    "v_mov_b32 v124, s55\n"
    "v_mfma_f32_16x16x32_bf16 v[92:95], v[224:227], a[72:75], v[92:95]\n"
    "v_mfma_f32_16x16x32_bf16 v[80:83], v[228:231], a[8:11], v[80:83]\n"
    "v_mfma_f32_16x16x32_bf16 v[96:99], v[228:231], a[72:75], v[96:99]\n"
    "v_mfma_f32_16x16x32_bf16 v[84:87], v[232:235], a[8:11], v[84:87]\n"
    "buffer_load_dwordx4 v21, s[8:11], s67 offen offset:1024 lds\n"
    "v_mfma_f32_16x16x32_bf16 v[100:103], v[232:235], a[72:75], v[100:103]\n"
    "v_mfma_f32_16x16x32_bf16 v[88:91], v[236:239], a[8:11], v[88:91]\n"
    "v_mfma_f32_16x16x32_bf16 v[104:107], v[236:239], a[72:75], v[104:107]\n"
    "s_waitcnt vmcnt(10) lgkmcnt(0)\n"
    "s_barrier\n"
    "buffer_load_dwordx4 v22, s[8:11], s67 offen offset:2048 lds\n"
    "v_mfma_f32_16x16x32_bf16 v[152:155], a[136:139], v[12:15], v[152:155]\n"
    "ds_read_b64_tr_b16 a[16:17], v38 offset:49152\n"
    "ds_read_b64_tr_b16 a[18:19], v38 offset:49664\n"
    "v_mfma_f32_16x16x32_bf16 v[136:139], a[136:139], v[224:227], v[136:139]\n"
    "ds_read_b64_tr_b16 a[80:81], v38 offset:57344\n"
    "ds_read_b64_tr_b16 a[82:83], v38 offset:57856\n"
    "v_mfma_f32_16x16x32_bf16 v[140:143], a[136:139], v[228:231], v[140:143]\n"
    "ds_read_b128 v[192:195], v28 offset:16384\n"
    "ds_read_b128 v[196:199], v29 offset:16384\n"
    "v_mfma_f32_16x16x32_bf16 v[144:147], a[136:139], v[232:235], v[144:147]\n"
    "ds_read_b128 v[200:203], v30 offset:16384\n"
    "buffer_load_dwordx4 v23, s[8:11], s67 offen offset:3072 lds\n"
    "v_mfma_f32_16x16x32_bf16 v[148:151], a[136:139], v[236:239], v[148:151]\n"
    "s_add_u32 m0, s51, 0x14000\n"
    "ds_read_b128 v[204:207], v31 offset:16384\n"
    "v_mfma_f32_16x16x32_bf16 v[108:111], v[12:15], a[12:15], v[108:111]\n"
    "ds_read_b64_tr_b16 a[20:21], v38 offset:53248\n"
    "ds_read_b64_tr_b16 a[22:23], v38 offset:53760\n"
    "v_mfma_f32_16x16x32_bf16 v[112:115], v[12:15], a[76:79], v[112:115]\n"
    "ds_read_b64_tr_b16 a[84:85], v38 offset:61440\n"
    "ds_read_b64_tr_b16 a[86:87], v38 offset:61952\n"
    "v_mfma_f32_16x16x32_bf16 v[116:119], v[240:243], v[12:15], v[116:119]\n"
    "ds_read_b128 v[208:211], v32 offset:16384\n"
    "ds_read_b128 v[212:215], v33 offset:16384\n"
    "v_mfma_f32_16x16x32_bf16 v[120:123], v[240:243], v[240:243], v[120:123]\n"
    "buffer_store_dword v124, v125, s[20:23], s56 offen sc1\n"
    "ds_read_b128 v[216:219], v34 offset:16384\n"
    "v_mfma_f32_16x16x32_bf16 v[76:79], v[240:243], a[12:15], v[76:79]\n"
    "ds_read_b128 v[220:223], v35 offset:16384\n"
    "v_and_b32 v166, 1, v9\n"
    "v_mfma_f32_16x16x32_bf16 v[92:95], v[240:243], a[76:79], v[92:95]\n"
    "buffer_load_dwordx4 v24, s[12:15], s59 offen offset:0 lds\n"
    "ds_read_b128 v[224:227], v28 offset:18432\n"
    "v_mfma_f32_16x16x32_bf16 v[80:83], v[244:247], a[12:15], v[80:83]\n"
    "ds_read_b128 v[228:231], v29 offset:18432\n"
    "v_cmp_ne_u32 s[60:61], 0, v166\n"
    "v_mfma_f32_16x16x32_bf16 v[96:99], v[244:247], a[76:79], v[96:99]\n"
    "ds_read_b128 v[232:235], v30 offset:18432\n"
    "ds_read_b128 v[236:239], v31 offset:18432\n"
    "v_mfma_f32_16x16x32_bf16 v[84:87], v[248:251], a[12:15], v[84:87]\n"
    "v_and_b32 v166, 2, v9\n"
    "v_cmp_ne_u32 s[62:63], 0, v166\n"
    "v_mfma_f32_16x16x32_bf16 v[100:103], v[248:251], a[76:79], v[100:103]\n"
    "v_lshrrev_b32 v166, 2, v9\n"
    "v_cmp_eq_u32 s[64:65], v166, v10\n"
    "v_mfma_f32_16x16x32_bf16 v[88:91], v[252:255], a[12:15], v[88:91]\n"
    "buffer_load_dwordx4 v25, s[12:15], s59 offen offset:1024 lds\n"
    "v_mov_b32 v166, 0x27e00\n"
    "v_mfma_f32_16x16x32_bf16 v[104:107], v[252:255], a[76:79], v[104:107]\n"
    "v_mfma_f32_16x16x32_bf16 v[152:155], a[140:143], v[12:15], v[152:155]\n"
    "v_mfma_f32_16x16x32_bf16 v[136:139], a[140:143], v[240:243], v[136:139]\n"
    "ds_read_b128 v[240:243], v32 offset:18432\n"
    "v_mfma_f32_16x16x32_bf16 v[140:143], a[140:143], v[244:247], v[140:143]\n"
    "ds_read_b128 v[244:247], v33 offset:18432\n"
    "buffer_load_dwordx4 v26, s[12:15], s59 offen offset:2048 lds\n"
    "v_mfma_f32_16x16x32_bf16 v[144:147], a[140:143], v[248:251], v[144:147]\n"
    "ds_read_b128 v[248:251], v34 offset:18432\n"
    "v_mfma_f32_16x16x32_bf16 v[148:151], a[140:143], v[252:255], v[148:151]\n"
    "ds_read_b128 v[252:255], v35 offset:18432\n"
    "s_waitcnt lgkmcnt(15)\n"
    "v_mfma_f32_16x16x32_bf16 v[108:111], v[12:15], a[16:19], v[108:111]\n"
    "s_waitcnt lgkmcnt(15)\n"
    "v_mfma_f32_16x16x32_bf16 v[112:115], v[12:15], a[80:83], v[112:115]\n"
    "s_waitcnt lgkmcnt(15)\n"
    "v_mfma_f32_16x16x32_bf16 v[116:119], v[192:195], v[12:15], v[116:119]\n"
    "buffer_load_dwordx4 v27, s[12:15], s59 offen offset:3072 lds\n"
    "v_mfma_f32_16x16x32_bf16 v[120:123], v[192:195], v[192:195], v[120:123]\n"
    "s_lshl_b32 s59, s66, 1\n"
    "s_add_u32 s59, s59, 5\n"
    "v_mfma_f32_16x16x32_bf16 v[76:79], v[192:195], a[16:19], v[76:79]\n"
    "s_and_b32 s59, s59, 7\n"
    "s_mul_i32 s59, s59, 0x30000\n"
    "v_mfma_f32_16x16x32_bf16 v[92:95], v[192:195], a[80:83], v[92:95]\n"
    "s_add_u32 s59, s59, s53\n"
    "s_add_u32 m0, s51, 0x18000\n"
    "s_waitcnt lgkmcnt(15)\n"
    "v_mfma_f32_16x16x32_bf16 v[80:83], v[196:199], a[16:19], v[80:83]\n"
    "v_mfma_f32_16x16x32_bf16 v[96:99], v[196:199], a[80:83], v[96:99]\n"
    "buffer_load_dwordx4 v24, s[12:15], s59 offen offset:0 lds\n"
    "s_waitcnt lgkmcnt(15)\n"
    "v_mfma_f32_16x16x32_bf16 v[84:87], v[200:203], a[16:19], v[84:87]\n"
    "v_mfma_f32_16x16x32_bf16 v[100:103], v[200:203], a[80:83], v[100:103]\n"
    "s_waitcnt lgkmcnt(15)\n"
    "v_mfma_f32_16x16x32_bf16 v[88:91], v[204:207], a[16:19], v[88:91]\n"
    "v_mfma_f32_16x16x32_bf16 v[104:107], v[204:207], a[80:83], v[104:107]\n"
    "s_waitcnt vmcnt(10) lgkmcnt(12)\n"
    "s_barrier\n"
    "buffer_load_dwordx4 v25, s[12:15], s59 offen offset:1024 lds\n"
    "v_mfma_f32_16x16x32_bf16 v[108:111], v[12:15], a[20:23], v[108:111]\n"
    "ds_read_b64_tr_b16 a[24:25], v39 offset:0\n"
    "ds_read_b64_tr_b16 a[26:27], v39 offset:512\n"
    "v_mfma_f32_16x16x32_bf16 v[112:115], v[12:15], a[84:87], v[112:115]\n"
    "ds_read_b64_tr_b16 a[88:89], v39 offset:8192\n"
    "ds_read_b64_tr_b16 a[90:91], v39 offset:8704\n"
    "s_waitcnt lgkmcnt(15)\n"
    "v_mfma_f32_16x16x32_bf16 v[116:119], v[208:211], v[12:15], v[116:119]\n"
    "ds_read_b64_tr_b16 a[28:29], v39 offset:4096\n"
    "ds_read_b64_tr_b16 a[30:31], v39 offset:4608\n"
    "v_mfma_f32_16x16x32_bf16 v[120:123], v[208:211], v[208:211], v[120:123]\n"
    "buffer_load_dwordx4 v26, s[12:15], s59 offen offset:2048 lds\n"
    "ds_read_b64_tr_b16 a[92:93], v39 offset:12288\n"
    "v_mfma_f32_16x16x32_bf16 v[76:79], v[208:211], a[20:23], v[76:79]\n"
    "ds_read_b64_tr_b16 a[94:95], v39 offset:12800\n"
    "ds_read_b64_tr_b16 a[144:145], v38 offset:0\n"
    "v_mfma_f32_16x16x32_bf16 v[92:95], v[208:211], a[84:87], v[92:95]\n"
    "ds_read_b64_tr_b16 a[146:147], v38 offset:512\n"
    "ds_read_b64_tr_b16 a[148:149], v38 offset:4096\n"
    "s_waitcnt lgkmcnt(15)\n"
    "v_mfma_f32_16x16x32_bf16 v[80:83], v[212:215], a[20:23], v[80:83]\n"
    "ds_read_b64_tr_b16 a[150:151], v38 offset:4608\n"
    "ds_read_b64_tr_b16 a[152:153], v38 offset:8192\n"
    "v_mfma_f32_16x16x32_bf16 v[96:99], v[212:215], a[84:87], v[96:99]\n"
    "ds_read_b64_tr_b16 a[154:155], v38 offset:8704\n"
    "buffer_load_dwordx4 v27, s[12:15], s59 offen offset:3072 lds\n"
    "s_waitcnt lgkmcnt(15)\n"
    "v_mfma_f32_16x16x32_bf16 v[84:87], v[216:219], a[20:23], v[84:87]\n"
    "s_add_u32 s59, s66, 2\n"
    "s_and_b32 s59, s59, 3\n"
    "v_mfma_f32_16x16x32_bf16 v[100:103], v[216:219], a[84:87], v[100:103]\n"
    "s_mul_i32 s59, s59, 0x60000\n"
    "s_add_u32 s59, s59, s54\n"
    "s_waitcnt lgkmcnt(15)\n"
    "v_mfma_f32_16x16x32_bf16 v[88:91], v[220:223], a[20:23], v[88:91]\n"
    "s_add_u32 m0, s51, 0x1c000\n"
    "s_add_u32 s67, s66, 3\n"
    "v_mfma_f32_16x16x32_bf16 v[104:107], v[220:223], a[84:87], v[104:107]\n"
    "s_and_b32 s67, s67, 3\n"
    "s_lshl_b32 s67, s67, 8\n"
    "s_waitcnt lgkmcnt(4)\n"
    "v_mfma_f32_16x16x32_bf16 v[152:155], a[144:147], v[12:15], v[152:155]\n"
    "buffer_load_dwordx4 v24, s[12:15], s59 offen offset:0 lds\n"
    "s_add_u32 s67, s67, s52\n"
    "v_mfma_f32_16x16x32_bf16 v[136:139], a[144:147], v[192:195], v[136:139]\n"
    "ds_read_b64_tr_b16 a[156:157], v38 offset:12288\n"
    "ds_read_b64_tr_b16 a[158:159], v38 offset:12800\n"
    "v_mfma_f32_16x16x32_bf16 v[140:143], a[144:147], v[196:199], v[140:143]\n"
    "v_lshlrev_b32 v172, 6, v0\n"
    "v_mov_b32 v174, -1\n"
    "v_mfma_f32_16x16x32_bf16 v[144:147], a[144:147], v[200:203], v[144:147]\n"
    "v_mov_b32 v175, -1\n"
    "v_mov_b32 v176, -1\n"
    "v_mfma_f32_16x16x32_bf16 v[148:151], a[144:147], v[204:207], v[148:151]\n"
    "v_mov_b32 v177, -1\n"
    "v_lshlrev_b32 v173, 4, v0\n"
    "s_waitcnt lgkmcnt(4)\n"
    "v_mfma_f32_16x16x32_bf16 v[152:155], a[148:151], v[12:15], v[152:155]\n"
    "buffer_load_dwordx4 v25, s[12:15], s59 offen offset:1024 lds\n"
    "v_mfma_f32_16x16x32_bf16 v[136:139], a[148:151], v[208:211], v[136:139]\n"
    "v_mfma_f32_16x16x32_bf16 v[140:143], a[148:151], v[212:215], v[140:143]\n"
    "v_mfma_f32_16x16x32_bf16 v[144:147], a[148:151], v[216:219], v[144:147]\n"
    "v_mfma_f32_16x16x32_bf16 v[148:151], a[148:151], v[220:223], v[148:151]\n"
    "v_mfma_f32_16x16x32_bf16 v[108:111], v[12:15], a[24:27], v[108:111]\n"
    "buffer_load_dwordx4 v26, s[12:15], s59 offen offset:2048 lds\n"
    "v_mfma_f32_16x16x32_bf16 v[112:115], v[12:15], a[88:91], v[112:115]\n"
    "Lpg1_%=:\n"
    "s_cbranch_execz Ltb0_1_0_%=\n"
    "v_mfma_f32_16x16x32_bf16 v[116:119], v[224:227], v[12:15], v[116:119]\n"
    "v_mfma_f32_16x16x32_bf16 v[120:123], v[224:227], v[224:227], v[120:123]\n"
    "v_mfma_f32_16x16x32_bf16 v[76:79], v[224:227], a[24:27], v[76:79]\n"
    "v_mfma_f32_16x16x32_bf16 v[92:95], v[224:227], a[88:91], v[92:95]\n"
    "buffer_load_dwordx4 v27, s[12:15], s59 offen offset:3072 lds\n"
    "s_add_u32 m0, s51, 0xc000\n"
    "v_mfma_f32_16x16x32_bf16 v[80:83], v[228:231], a[24:27], v[80:83]\n"
    "s_lshl_b32 s59, s66, 1\n"
    "s_add_u32 s59, s59, 6\n"
    "v_mfma_f32_16x16x32_bf16 v[96:99], v[228:231], a[88:91], v[96:99]\n"
    "s_and_b32 s59, s59, 7\n"
    "s_mul_i32 s59, s59, 0x30000\n"
    "v_mfma_f32_16x16x32_bf16 v[84:87], v[232:235], a[24:27], v[84:87]\n"
    "s_add_u32 s59, s59, s53\n"
    "v_mfma_f32_16x16x32_bf16 v[100:103], v[232:235], a[88:91], v[100:103]\n"
    "v_mfma_f32_16x16x32_bf16 v[88:91], v[236:239], a[24:27], v[88:91]\n"
    "buffer_load_dwordx4 v20, s[8:11], s67 offen offset:0 lds\n"
    "v_mfma_f32_16x16x32_bf16 v[104:107], v[236:239], a[88:91], v[104:107]\n"
    "s_waitcnt vmcnt(9) lgkmcnt(0)\n"
    "s_barrier\n"
    "v_mfma_f32_16x16x32_bf16 v[152:155], a[152:155], v[12:15], v[152:155]\n"
    "ds_read_b64_tr_b16 a[32:33], v38 offset:16384\n"
    "ds_read_b64_tr_b16 a[34:35], v38 offset:16896\n"
    "v_mfma_f32_16x16x32_bf16 v[136:139], a[152:155], v[224:227], v[136:139]\n"
    "buffer_load_dwordx4 v21, s[8:11], s67 offen offset:1024 lds\n"
    "ds_read_b64_tr_b16 a[96:97], v38 offset:24576\n"
    "v_mfma_f32_16x16x32_bf16 v[140:143], a[152:155], v[228:231], v[140:143]\n"
    "ds_read_b64_tr_b16 a[98:99], v38 offset:25088\n"
    "ds_read_b128 v[192:195], v28 offset:32768\n"
    "v_mfma_f32_16x16x32_bf16 v[144:147], a[152:155], v[232:235], v[144:147]\n"
    "ds_read_b128 v[196:199], v29 offset:32768\n"
    "ds_read_b128 v[200:203], v30 offset:32768\n"
    "v_mfma_f32_16x16x32_bf16 v[148:151], a[152:155], v[236:239], v[148:151]\n"
    "ds_read_b128 v[204:207], v31 offset:32768\n"
    "v_mfma_f32_16x16x32_bf16 v[108:111], v[12:15], a[28:31], v[108:111]\n"
    "ds_read_b64_tr_b16 a[36:37], v38 offset:20480\n"
    "ds_read_b64_tr_b16 a[38:39], v38 offset:20992\n"
    "buffer_load_dwordx4 v22, s[8:11], s67 offen offset:2048 lds\n"
    "v_mfma_f32_16x16x32_bf16 v[112:115], v[12:15], a[92:95], v[112:115]\n"
    "ds_read_b64_tr_b16 a[100:101], v38 offset:28672\n"
    "ds_read_b64_tr_b16 a[102:103], v38 offset:29184\n"
    "v_mfma_f32_16x16x32_bf16 v[116:119], v[240:243], v[12:15], v[116:119]\n"
    "ds_read_b128 v[208:211], v32 offset:32768\n"
    "ds_read_b128 v[212:215], v33 offset:32768\n"
    "v_mfma_f32_16x16x32_bf16 v[120:123], v[240:243], v[240:243], v[120:123]\n"
    "ds_read_b128 v[216:219], v34 offset:32768\n"
    "v_mfma_f32_16x16x32_bf16 v[76:79], v[240:243], a[28:31], v[76:79]\n"
    "ds_read_b128 v[220:223], v35 offset:32768\n"
    "v_mfma_f32_16x16x32_bf16 v[92:95], v[240:243], a[92:95], v[92:95]\n"
    "buffer_load_dwordx4 v23, s[8:11], s67 offen offset:3072 lds\n"
    "s_add_u32 m0, s51, 0x20000\n"
    "v_mfma_f32_16x16x32_bf16 v[80:83], v[244:247], a[28:31], v[80:83]\n"
    "ds_read_b128 v[224:227], v28 offset:34816\n"
    "ds_read_b128 v[228:231], v29 offset:34816\n"
    "v_mfma_f32_16x16x32_bf16 v[96:99], v[244:247], a[92:95], v[96:99]\n"
    "ds_read_b128 v[232:235], v30 offset:34816\n"
    "v_mfma_f32_16x16x32_bf16 v[84:87], v[248:251], a[28:31], v[84:87]\n"
    "ds_read_b128 v[236:239], v31 offset:34816\n"
    "v_mfma_f32_16x16x32_bf16 v[100:103], v[248:251], a[92:95], v[100:103]\n"
    "v_mfma_f32_16x16x32_bf16 v[88:91], v[252:255], a[28:31], v[88:91]\n"
    "buffer_load_dwordx4 v24, s[12:15], s59 offen offset:0 lds\n"
    "v_mfma_f32_16x16x32_bf16 v[104:107], v[252:255], a[92:95], v[104:107]\n"
    "v_mfma_f32_16x16x32_bf16 v[152:155], a[156:159], v[12:15], v[152:155]\n"
    "v_mfma_f32_16x16x32_bf16 v[136:139], a[156:159], v[240:243], v[136:139]\n"
    "ds_read_b128 v[240:243], v32 offset:34816\n"
    "v_mfma_f32_16x16x32_bf16 v[140:143], a[156:159], v[244:247], v[140:143]\n"
    "ds_read_b128 v[244:247], v33 offset:34816\n"
    "v_mfma_f32_16x16x32_bf16 v[144:147], a[156:159], v[248:251], v[144:147]\n"
    "buffer_load_dwordx4 v25, s[12:15], s59 offen offset:1024 lds\n"
    "ds_read_b128 v[248:251], v34 offset:34816\n"
    "v_mfma_f32_16x16x32_bf16 v[148:151], a[156:159], v[252:255], v[148:151]\n"
    "ds_read_b128 v[252:255], v35 offset:34816\n"
    "s_waitcnt lgkmcnt(15)\n"
    "v_mfma_f32_16x16x32_bf16 v[108:111], v[12:15], a[32:35], v[108:111]\n"
    "s_waitcnt lgkmcnt(15)\n"
    "v_mfma_f32_16x16x32_bf16 v[112:115], v[12:15], a[96:99], v[112:115]\n"
    "s_waitcnt lgkmcnt(15)\n"
    "v_mfma_f32_16x16x32_bf16 v[116:119], v[192:195], v[12:15], v[116:119]\n"
    "buffer_load_dwordx4 v26, s[12:15], s59 offen offset:2048 lds\n"
    "v_mfma_f32_16x16x32_bf16 v[120:123], v[192:195], v[192:195], v[120:123]\n"
    "v_mfma_f32_16x16x32_bf16 v[76:79], v[192:195], a[32:35], v[76:79]\n"
    "v_mfma_f32_16x16x32_bf16 v[92:95], v[192:195], a[96:99], v[92:95]\n"
    "s_waitcnt lgkmcnt(15)\n"
    "v_mfma_f32_16x16x32_bf16 v[80:83], v[196:199], a[32:35], v[80:83]\n"
    "v_mfma_f32_16x16x32_bf16 v[96:99], v[196:199], a[96:99], v[96:99]\n"
    "buffer_load_dwordx4 v27, s[12:15], s59 offen offset:3072 lds\n"
    "s_waitcnt lgkmcnt(15)\n"
    "v_mfma_f32_16x16x32_bf16 v[84:87], v[200:203], a[32:35], v[84:87]\n"
    "s_lshl_b32 s59, s66, 1\n"
    "s_add_u32 s59, s59, 7\n"
    "v_mfma_f32_16x16x32_bf16 v[100:103], v[200:203], a[96:99], v[100:103]\n"
    "s_and_b32 s59, s59, 7\n"
    "s_mul_i32 s59, s59, 0x30000\n"
    "s_waitcnt lgkmcnt(15)\n"
    "v_mfma_f32_16x16x32_bf16 v[88:91], v[204:207], a[32:35], v[88:91]\n"
    "s_add_u32 s59, s59, s53\n"
    "s_add_u32 m0, s51, 0x10000\n"
    "v_mfma_f32_16x16x32_bf16 v[104:107], v[204:207], a[96:99], v[104:107]\n"
    "s_waitcnt vmcnt(8) lgkmcnt(12)\n"
    "s_barrier\n"
    "buffer_load_dwordx4 v24, s[12:15], s59 offen offset:0 lds\n"
    "v_mfma_f32_16x16x32_bf16 v[108:111], v[12:15], a[36:39], v[108:111]\n"
    "ds_read_b64_tr_b16 a[40:41], v38 offset:32768\n"
    "ds_read_b64_tr_b16 a[42:43], v38 offset:33280\n"
    "v_mfma_f32_16x16x32_bf16 v[112:115], v[12:15], a[100:103], v[112:115]\n"
    "ds_read_b64_tr_b16 a[104:105], v38 offset:40960\n"
    "ds_read_b64_tr_b16 a[106:107], v38 offset:41472\n"
    "s_waitcnt lgkmcnt(15)\n"
    "v_mfma_f32_16x16x32_bf16 v[116:119], v[208:211], v[12:15], v[116:119]\n"
    "ds_read_b64_tr_b16 a[44:45], v38 offset:36864\n"
    "ds_read_b64_tr_b16 a[46:47], v38 offset:37376\n"
    "v_mfma_f32_16x16x32_bf16 v[120:123], v[208:211], v[208:211], v[120:123]\n"
    "buffer_load_dwordx4 v25, s[12:15], s59 offen offset:1024 lds\n"
    "ds_read_b64_tr_b16 a[108:109], v38 offset:45056\n"
    "v_mfma_f32_16x16x32_bf16 v[76:79], v[208:211], a[36:39], v[76:79]\n"
    "ds_read_b64_tr_b16 a[110:111], v38 offset:45568\n"
    "ds_read_b64_tr_b16 a[160:161], v38 offset:49152\n"
    "v_mfma_f32_16x16x32_bf16 v[92:95], v[208:211], a[100:103], v[92:95]\n"
    "ds_read_b64_tr_b16 a[162:163], v38 offset:49664\n"
    "ds_read_b64_tr_b16 a[164:165], v38 offset:53248\n"
    "s_waitcnt lgkmcnt(15)\n"
    "v_mfma_f32_16x16x32_bf16 v[80:83], v[212:215], a[36:39], v[80:83]\n"
    "ds_read_b64_tr_b16 a[166:167], v38 offset:53760\n"
    "ds_read_b64_tr_b16 a[168:169], v38 offset:57344\n"
    "v_mfma_f32_16x16x32_bf16 v[96:99], v[212:215], a[100:103], v[96:99]\n"
    "ds_read_b64_tr_b16 a[170:171], v38 offset:57856\n"
    "buffer_load_dwordx4 v26, s[12:15], s59 offen offset:2048 lds\n"
    "s_waitcnt lgkmcnt(15)\n"
    "v_mfma_f32_16x16x32_bf16 v[84:87], v[216:219], a[36:39], v[84:87]\n"
    "ds_read_b64_tr_b16 a[172:173], v38 offset:61440\n"
    "ds_read_b64_tr_b16 a[174:175], v38 offset:61952\n"
    "v_mfma_f32_16x16x32_bf16 v[100:103], v[216:219], a[100:103], v[100:103]\n"
    "s_waitcnt lgkmcnt(15)\n"
    "v_mfma_f32_16x16x32_bf16 v[88:91], v[220:223], a[36:39], v[88:91]\n"
    "v_mfma_f32_16x16x32_bf16 v[104:107], v[220:223], a[100:103], v[104:107]\n"
    "s_waitcnt lgkmcnt(6)\n"
    "v_mfma_f32_16x16x32_bf16 v[152:155], a[160:163], v[12:15], v[152:155]\n"
    "buffer_load_dwordx4 v27, s[12:15], s59 offen offset:3072 lds\n"
    "s_add_u32 s59, s66, 3\n"
    "v_mfma_f32_16x16x32_bf16 v[136:139], a[160:163], v[192:195], v[136:139]\n"
    "s_and_b32 s59, s59, 3\n"
    "s_mul_i32 s59, s59, 0x60000\n"
    "v_mfma_f32_16x16x32_bf16 v[140:143], a[160:163], v[196:199], v[140:143]\n"
    "s_add_u32 s59, s59, s54\n"
    "v_mfma_f32_16x16x32_bf16 v[144:147], a[160:163], v[200:203], v[144:147]\n"
    "v_mfma_f32_16x16x32_bf16 v[148:151], a[160:163], v[204:207], v[148:151]\n"
    "s_waitcnt lgkmcnt(4)\n"
    "v_mfma_f32_16x16x32_bf16 v[152:155], a[164:167], v[12:15], v[152:155]\n"
    "buffer_load_dword v126, v127, s[20:23], 0 offen sc1\n"
    "s_add_u32 m0, s51, 0x14000\n"
    "v_mfma_f32_16x16x32_bf16 v[136:139], a[164:167], v[208:211], v[136:139]\n"
    "s_and_b32 s55, s6, 7\n"
    "buffer_load_dwordx4 v24, s[12:15], s59 offen offset:0 lds\n"
    "v_mfma_f32_16x16x32_bf16 v[140:143], a[164:167], v[212:215], v[140:143]\n"
    "s_lshl_b32 s55, s55, 5\n"
    "s_add_u32 s100, s55, s47\n"
    "v_mfma_f32_16x16x32_bf16 v[144:147], a[164:167], v[216:219], v[144:147]\n"
    "s_lshl_b32 s55, s100, 9\n"
    "v_lshl_add_u32 v156, v0, 1, s55\n"
    "v_mfma_f32_16x16x32_bf16 v[148:151], a[164:167], v[220:223], v[148:151]\n"
    "s_lshl_b32 s55, s100, 2\n"
    "v_lshl_add_u32 v157, v157, 1, s55\n"
    "v_mfma_f32_16x16x32_bf16 v[108:111], v[12:15], a[40:43], v[108:111]\n"
    "s_mov_b32 s55, 0x3b000000\n"
    "v_mfma_f32_16x16x32_bf16 v[112:115], v[12:15], a[104:107], v[112:115]\n"
    "buffer_load_dwordx4 v25, s[12:15], s59 offen offset:1024 lds\n"
    "v_mfma_f32_16x16x32_bf16 v[116:119], v[224:227], v[12:15], v[116:119]\n"
    "v_mfma_f32_16x16x32_bf16 v[120:123], v[224:227], v[224:227], v[120:123]\n"
    "v_mfma_f32_16x16x32_bf16 v[76:79], v[224:227], a[40:43], v[76:79]\n"
    "v_mfma_f32_16x16x32_bf16 v[92:95], v[224:227], a[104:107], v[92:95]\n"
    "v_mfma_f32_16x16x32_bf16 v[80:83], v[228:231], a[40:43], v[80:83]\n"
    "buffer_load_dwordx4 v26, s[12:15], s59 offen offset:2048 lds\n"
    "v_mfma_f32_16x16x32_bf16 v[96:99], v[228:231], a[104:107], v[96:99]\n"
    "v_mfma_f32_16x16x32_bf16 v[84:87], v[232:235], a[40:43], v[84:87]\n"
    "v_mfma_f32_16x16x32_bf16 v[100:103], v[232:235], a[104:107], v[100:103]\n"
    "v_mfma_f32_16x16x32_bf16 v[88:91], v[236:239], a[40:43], v[88:91]\n"
    "v_mfma_f32_16x16x32_bf16 v[104:107], v[236:239], a[104:107], v[104:107]\n"
    "s_waitcnt vmcnt(8) lgkmcnt(0)\n"
    "s_barrier\n"
    "buffer_load_dwordx4 v27, s[12:15], s59 offen offset:3072 lds\n"
    "v_mfma_f32_16x16x32_bf16 v[152:155], a[168:171], v[12:15], v[152:155]\n"
    "s_lshl_b32 s59, s66, 1\n"
    "s_add_u32 s59, s59, 0\n"
    "v_mfma_f32_16x16x32_bf16 v[136:139], a[168:171], v[224:227], v[136:139]\n"
    "s_and_b32 s59, s59, 7\n"
    "s_lshl_b32 s59, s59, 16\n"
    "v_mfma_f32_16x16x32_bf16 v[140:143], a[168:171], v[228:231], v[140:143]\n"
    "s_add_u32 s59, s59, s101\n"
    "global_load_short_d16_hi v158, v156, s[28:29]\n"
    "v_mfma_f32_16x16x32_bf16 v[144:147], a[168:171], v[232:235], v[144:147]\n"
    "s_add_u32 m0, s51, 0x18000\n"
    "ds_read_b128 v[192:195], v28 offset:49152\n"
    "v_mfma_f32_16x16x32_bf16 v[148:151], a[168:171], v[236:239], v[148:151]\n"
    "global_load_short_d16_hi v159, v156, s[30:31]\n"
    "ds_read_b128 v[208:211], v32 offset:49152\n"
    "v_mfma_f32_16x16x32_bf16 v[108:111], v[12:15], a[44:47], v[108:111]\n"
    "global_load_short_d16_hi v160, v156, s[32:33]\n"
    "ds_read_b128 v[128:131], v28 offset:51200\n"
    "v_mfma_f32_16x16x32_bf16 v[112:115], v[12:15], a[108:111], v[112:115]\n"
    "ds_read_b128 v[132:135], v32 offset:51200\n"
    "global_load_short_d16_hi v161, v156, s[34:35]\n"
    "v_mfma_f32_16x16x32_bf16 v[116:119], v[240:243], v[12:15], v[116:119]\n"
    "ds_read_b64_tr_b16 a[48:49], v39 offset:0\n"
    "ds_read_b64_tr_b16 a[50:51], v39 offset:512\n"
    "v_mfma_f32_16x16x32_bf16 v[120:123], v[240:243], v[240:243], v[120:123]\n"
    "global_load_short_d16_hi v162, v156, s[36:37]\n"
    "ds_read_b64_tr_b16 a[112:113], v39 offset:8192\n"
    "v_mfma_f32_16x16x32_bf16 v[76:79], v[240:243], a[44:47], v[76:79]\n"
    "global_load_short_d16_hi v163, v156, s[38:39]\n"
    "ds_read_b64_tr_b16 a[114:115], v39 offset:8704\n"
    "v_mfma_f32_16x16x32_bf16 v[92:95], v[240:243], a[108:111], v[92:95]\n"
    "ds_read_b128 v[196:199], v29 offset:49152\n"
    "global_load_short_d16_hi v164, v157, s[40:41]\n"
    "v_mfma_f32_16x16x32_bf16 v[80:83], v[244:247], a[44:47], v[80:83]\n"
    "ds_read_b128 v[200:203], v30 offset:49152\n"
    "ds_read_b128 v[204:207], v31 offset:49152\n"
    "v_mfma_f32_16x16x32_bf16 v[96:99], v[244:247], a[108:111], v[96:99]\n"
    "global_load_short_d16_hi v165, v157, s[42:43]\n"
    "ds_read_b64_tr_b16 a[52:53], v39 offset:4096\n"
    "v_mfma_f32_16x16x32_bf16 v[84:87], v[248:251], a[44:47], v[84:87]\n"
    "buffer_load_dwordx4 v42, s[16:19], s59 offen offset:0 lds\n"
    "ds_read_b64_tr_b16 a[54:55], v39 offset:4608\n"
    "v_mfma_f32_16x16x32_bf16 v[100:103], v[248:251], a[108:111], v[100:103]\n"
    "ds_read_b64_tr_b16 a[116:117], v39 offset:12288\n"
    "ds_read_b64_tr_b16 a[118:119], v39 offset:12800\n"
    "v_mfma_f32_16x16x32_bf16 v[88:91], v[252:255], a[44:47], v[88:91]\n"
    "ds_read_b128 v[212:215], v33 offset:49152\n"
    "ds_read_b128 v[216:219], v34 offset:49152\n"
    "v_mfma_f32_16x16x32_bf16 v[104:107], v[252:255], a[108:111], v[104:107]\n"
    "ds_read_b128 v[220:223], v35 offset:49152\n"
    "ds_read_b128 v[228:231], v29 offset:51200\n"
    "v_mfma_f32_16x16x32_bf16 v[152:155], a[172:175], v[12:15], v[152:155]\n"
    "ds_read_b128 v[232:235], v30 offset:51200\n"
    "v_mfma_f32_16x16x32_bf16 v[136:139], a[172:175], v[240:243], v[136:139]\n"
    "buffer_load_dwordx4 v43, s[16:19], s59 offen offset:1024 lds\n"
    "ds_read_b128 v[236:239], v31 offset:51200\n"
    "v_mfma_f32_16x16x32_bf16 v[140:143], a[172:175], v[244:247], v[140:143]\n"
    "ds_read_b128 v[244:247], v33 offset:51200\n"
    "v_mfma_f32_16x16x32_bf16 v[144:147], a[172:175], v[248:251], v[144:147]\n"
    "ds_read_b128 v[248:251], v34 offset:51200\n"
    "v_mfma_f32_16x16x32_bf16 v[148:151], a[172:175], v[252:255], v[148:151]\n"
    "ds_read_b128 v[252:255], v35 offset:51200\n"
    "s_waitcnt lgkmcnt(15)\n"
    "v_mfma_f32_16x16x32_bf16 v[116:119], v[192:195], v[12:15], v[116:119]\n"
    "buffer_load_dwordx4 v44, s[16:19], s59 offen offset:2048 lds\n"
    "v_mfma_f32_16x16x32_bf16 v[120:123], v[192:195], v[192:195], v[120:123]\n"
    "s_waitcnt lgkmcnt(15)\n"
    "v_mfma_f32_16x16x32_bf16 v[116:119], v[208:211], v[12:15], v[116:119]\n"
    "v_mfma_f32_16x16x32_bf16 v[120:123], v[208:211], v[208:211], v[120:123]\n"
    "s_waitcnt lgkmcnt(15)\n"
    "v_mfma_f32_16x16x32_bf16 v[116:119], v[128:131], v[12:15], v[116:119]\n"
    "v_mfma_f32_16x16x32_bf16 v[120:123], v[128:131], v[128:131], v[120:123]\n"
    "buffer_load_dwordx4 v45, s[16:19], s59 offen offset:3072 lds\n"
    "s_lshl_b32 s59, s66, 1\n"
    "s_waitcnt lgkmcnt(15)\n"
    "v_mfma_f32_16x16x32_bf16 v[116:119], v[132:135], v[12:15], v[116:119]\n"
    "s_add_u32 s59, s59, 2\n"
    "s_and_b32 s59, s59, 7\n"
    "v_mfma_f32_16x16x32_bf16 v[120:123], v[132:135], v[132:135], v[120:123]\n"
    "s_lshl_b32 s59, s59, 16\n"
    "s_add_u32 s59, s59, s101\n"
    "s_waitcnt lgkmcnt(15)\n"
    "v_mfma_f32_16x16x32_bf16 v[108:111], v[12:15], a[48:51], v[108:111]\n"
    "s_add_u32 m0, s51, 0x1c000\n"
    "s_lshl_b32 s56, s7, 6\n"
    "s_waitcnt lgkmcnt(15)\n"
    "v_mfma_f32_16x16x32_bf16 v[112:115], v[12:15], a[112:115], v[112:115]\n"
    "s_add_u32 s56, s56, 0x27800\n"
    "v_cndmask_b32 v167, v116, v117, s[60:61]\n"
    "v_mfma_f32_16x16x32_bf16 v[76:79], v[192:195], a[48:51], v[76:79]\n"
    "buffer_load_dwordx4 v42, s[16:19], s59 offen offset:0 lds\n"
    "v_cndmask_b32 v168, v118, v119, s[60:61]\n"
    "v_mfma_f32_16x16x32_bf16 v[92:95], v[192:195], a[112:115], v[92:95]\n"
    "v_cndmask_b32 v169, v167, v168, s[62:63]\n"
    "v_cndmask_b32 v167, v120, v121, s[60:61]\n"
    "s_waitcnt lgkmcnt(15)\n"
    "v_mfma_f32_16x16x32_bf16 v[80:83], v[196:199], a[48:51], v[80:83]\n"
    "v_cndmask_b32 v168, v122, v123, s[60:61]\n"
    "v_cndmask_b32 v170, v167, v168, s[62:63]\n"
    "v_mfma_f32_16x16x32_bf16 v[96:99], v[196:199], a[112:115], v[96:99]\n"
    "v_mul_f32 v169, 0x3b000000, v169\n"
    "v_mul_f32 v167, v169, v169\n"
    "s_waitcnt lgkmcnt(14)\n"
    "v_mfma_f32_16x16x32_bf16 v[84:87], v[200:203], a[48:51], v[84:87]\n"
    "v_fma_f32 v170, v170, s55, -v167\n"
    "buffer_load_dwordx4 v43, s[16:19], s59 offen offset:1024 lds\n"
    "v_mfma_f32_16x16x32_bf16 v[100:103], v[200:203], a[112:115], v[100:103]\n"
    "v_max_f32 v170, 0, v170\n"
    "v_cvt_pk_bf16_f32 v169, 0, v169\n"
    "s_waitcnt lgkmcnt(13)\n"
    "v_mfma_f32_16x16x32_bf16 v[88:91], v[204:207], a[48:51], v[88:91]\n"
    "v_cvt_pk_bf16_f32 v170, 0, v170\n"
    "v_add_f32 v170, 0x3727c5ac, v170\n"
    "v_mfma_f32_16x16x32_bf16 v[104:107], v[204:207], a[112:115], v[104:107]\n"
    "s_waitcnt vmcnt(14) lgkmcnt(9)\n"
    "s_barrier\n"
    "buffer_load_dwordx4 v44, s[16:19], s59 offen offset:2048 lds\n"
    "v_mfma_f32_16x16x32_bf16 v[108:111], v[12:15], a[52:55], v[108:111]\n"
    "ds_read_b64_tr_b16 a[56:57], v38 offset:0\n"
    "ds_read_b64_tr_b16 a[58:59], v38 offset:512\n"
    "v_mfma_f32_16x16x32_bf16 v[112:115], v[12:15], a[116:119], v[112:115]\n"
    "ds_read_b64_tr_b16 a[120:121], v38 offset:8192\n"
    "ds_read_b64_tr_b16 a[122:123], v38 offset:8704\n"
    "v_mfma_f32_16x16x32_bf16 v[76:79], v[208:211], a[52:55], v[76:79]\n"
    "ds_read_b64_tr_b16 a[60:61], v38 offset:4096\n"
    "ds_read_b64_tr_b16 a[62:63], v38 offset:4608\n"
    "v_mfma_f32_16x16x32_bf16 v[92:95], v[208:211], a[116:119], v[92:95]\n"
    "ds_read_b64_tr_b16 a[124:125], v38 offset:12288\n"
    "ds_read_b64_tr_b16 a[126:127], v38 offset:12800\n"
    "s_waitcnt lgkmcnt(15)\n"
    "v_mfma_f32_16x16x32_bf16 v[80:83], v[212:215], a[52:55], v[80:83]\n"
    "buffer_load_dwordx4 v45, s[16:19], s59 offen offset:3072 lds\n"
    "s_lshl_b32 s59, s66, 1\n"
    "v_mfma_f32_16x16x32_bf16 v[96:99], v[212:215], a[116:119], v[96:99]\n"
    "s_add_u32 s59, s59, 4\n"
    "s_and_b32 s59, s59, 7\n"
    "s_waitcnt lgkmcnt(15)\n"
    "v_mfma_f32_16x16x32_bf16 v[84:87], v[216:219], a[52:55], v[84:87]\n"
    "s_lshl_b32 s59, s59, 16\n"
    "s_add_u32 s59, s59, s101\n"
    "v_mfma_f32_16x16x32_bf16 v[100:103], v[216:219], a[116:119], v[100:103]\n"
    "s_add_u32 m0, s51, 0x20000\n"
    "s_lshl_b32 s55, s47, 14\n"
    "s_waitcnt lgkmcnt(14)\n"
    "v_mfma_f32_16x16x32_bf16 v[88:91], v[220:223], a[52:55], v[88:91]\n"
    "s_lshl_b32 s78, s6, 13\n"
    "s_add_u32 s78, s78, 0x500000\n"
    "v_mfma_f32_16x16x32_bf16 v[104:107], v[220:223], a[116:119], v[104:107]\n"
    "buffer_load_dwordx4 v42, s[16:19], s59 offen offset:0 lds\n"
    "s_add_u32 s79, s78, 0x1000\n"
    "s_waitcnt lgkmcnt(6)\n"
    "v_mfma_f32_16x16x32_bf16 v[108:111], v[12:15], a[56:59], v[108:111]\n"
    "ds_read_b64_tr_b16 a[176:177], v38 offset:16384\n"
    "ds_read_b64_tr_b16 a[178:179], v38 offset:16896\n"
    "s_waitcnt lgkmcnt(6)\n"
    "v_mfma_f32_16x16x32_bf16 v[112:115], v[12:15], a[120:123], v[112:115]\n"
    "ds_read_b64_tr_b16 a[180:181], v38 offset:20480\n"
    "ds_read_b64_tr_b16 a[182:183], v38 offset:20992\n"
    "v_mfma_f32_16x16x32_bf16 v[76:79], v[128:131], a[56:59], v[76:79]\n"
    "ds_read_b64_tr_b16 a[184:185], v38 offset:24576\n"
    "ds_read_b64_tr_b16 a[186:187], v38 offset:25088\n"
    "v_mfma_f32_16x16x32_bf16 v[92:95], v[128:131], a[120:123], v[92:95]\n"
    "buffer_load_dwordx4 v43, s[16:19], s59 offen offset:1024 lds\n"
    "ds_read_b64_tr_b16 a[188:189], v38 offset:28672\n"
    "v_mfma_f32_16x16x32_bf16 v[80:83], v[228:231], a[56:59], v[80:83]\n"
    "ds_read_b64_tr_b16 a[190:191], v38 offset:29184\n"
    "v_cvt_pk_bf16_f32 v170, 0, v170\n"
    "v_mfma_f32_16x16x32_bf16 v[96:99], v[228:231], a[120:123], v[96:99]\n"
    "v_sqrt_f32 v170, v170\n"
    "v_mfma_f32_16x16x32_bf16 v[84:87], v[232:235], a[56:59], v[84:87]\n"
    "v_cvt_pk_bf16_f32 v170, 0, v170\n"
    "v_rcp_f32 v170, v170\n"
    "s_waitcnt lgkmcnt(6)\n"
    "v_mfma_f32_16x16x32_bf16 v[152:155], a[176:179], v[12:15], v[152:155]\n"
    "v_lshl_add_u32 v171, v9, 2, s56\n"
    "buffer_load_dwordx4 v44, s[16:19], s59 offen offset:2048 lds\n"
    "v_mfma_f32_16x16x32_bf16 v[136:139], a[176:179], v[192:195], v[136:139]\n"
    "s_add_u32 s56, s44, 1\n"
    "v_cndmask_b32 v171, v166, v171, s[64:65]\n"
    "v_mfma_f32_16x16x32_bf16 v[140:143], a[176:179], v[196:199], v[140:143]\n"
    "ds_write_b32 v171, v170\n"
    "ds_write_b32 v171, v169 offset:256\n"
    "v_mfma_f32_16x16x32_bf16 v[144:147], a[176:179], v[200:203], v[144:147]\n"
    "v_mfma_f32_16x16x32_bf16 v[148:151], a[176:179], v[204:207], v[148:151]\n"
    "s_waitcnt lgkmcnt(6)\n"
    "v_mfma_f32_16x16x32_bf16 v[152:155], a[180:183], v[12:15], v[152:155]\n"
    "buffer_load_dwordx4 v45, s[16:19], s59 offen offset:3072 lds\n"
    "v_mfma_f32_16x16x32_bf16 v[136:139], a[180:183], v[208:211], v[136:139]\n"
    "v_mfma_f32_16x16x32_bf16 v[140:143], a[180:183], v[212:215], v[140:143]\n"
    "v_mfma_f32_16x16x32_bf16 v[144:147], a[180:183], v[216:219], v[144:147]\n"
    "v_mfma_f32_16x16x32_bf16 v[148:151], a[180:183], v[220:223], v[148:151]\n"
    "v_mfma_f32_16x16x32_bf16 v[100:103], v[232:235], a[120:123], v[100:103]\n"
    "buffer_load_dword v189, v172, s[16:19], s55 offen\n"
    "v_mfma_f32_16x16x32_bf16 v[88:91], v[236:239], a[56:59], v[88:91]\n"
    "v_mfma_f32_16x16x32_bf16 v[104:107], v[236:239], a[120:123], v[104:107]\n"
    "s_waitcnt lgkmcnt(0)\n"
    "s_barrier\n"
    "v_mfma_f32_16x16x32_bf16 v[152:155], a[184:187], v[12:15], v[152:155]\n"
    "buffer_store_dwordx4 v[174:177], v173, s[20:23], s78 offen sc1\n"
    "v_cmp_eq_u32 vcc, s56, v126\n"
    "v_mfma_f32_16x16x32_bf16 v[136:139], a[184:187], v[128:131], v[136:139]\n"
    "s_cmp_eq_u64 vcc, exec\n"
    "v_mfma_f32_16x16x32_bf16 v[140:143], a[184:187], v[228:231], v[140:143]\n"
    "buffer_store_dwordx4 v[174:177], v173, s[20:23], s79 offen sc1\n"
    "v_mfma_f32_16x16x32_bf16 v[144:147], a[184:187], v[232:235], v[144:147]\n"
    "v_mfma_f32_16x16x32_bf16 v[148:151], a[184:187], v[236:239], v[148:151]\n"
    "s_cbranch_scc1 Lflagok_7_%=\n"
    "s_mov_b32 s76, 0\n"
    "Lflagpoll_8_%=:\n"
    "buffer_load_dword v126, v127, s[20:23], 0 offen sc1\n"
    "s_waitcnt vmcnt(0) lgkmcnt(0)\n"
    "v_cmp_eq_u32 vcc, s56, v126\n"
    "s_nop 0\n"
    "s_cmp_eq_u64 vcc, exec\n"
    "s_cbranch_scc1 Lflagok_7_%=\n"
    "s_add_u32 s76, s76, 1\n"
    "s_cmp_gt_u32 s76, 0x40000\n"
    "s_cbranch_scc1 Lflagok_7_%=\n"
    "s_sleep 1\n"
    "s_branch Lflagpoll_8_%=\n"
    "Lflagok_7_%=:\n"
    "v_mfma_f32_16x16x32_bf16 v[108:111], v[12:15], a[60:63], v[108:111]\n"
    "s_lshl_b32 s74, s50, 10\n"
    "s_add_u32 s74, s74, 0x10000\n"
    "v_mfma_f32_16x16x32_bf16 v[112:115], v[12:15], a[124:127], v[112:115]\n"
    "s_lshl_b32 s54, s50, 7\n"
    "s_add_u32 s54, s54, 0x18000\n"
    "v_mfma_f32_16x16x32_bf16 v[76:79], v[132:135], a[60:63], v[76:79]\n"
    "ds_read_b128 v[72:75], v60\n"
    "ds_read_b128 v[166:169], v60 offset:256\n"
    "v_mfma_f32_16x16x32_bf16 v[92:95], v[132:135], a[124:127], v[92:95]\n"
    "ds_read_b128 v[124:127], v61\n"
    "s_lshl_b32 s75, s48, 16\n"
    "v_mfma_f32_16x16x32_bf16 v[80:83], v[244:247], a[60:63], v[80:83]\n"
    "ds_read_b128 v[20:23], v61 offset:256\n"
    "ds_read2_b32 v[40:41], v50 offset0:0 offset1:64\n"
    "v_mfma_f32_16x16x32_bf16 v[96:99], v[244:247], a[124:127], v[96:99]\n"
    "v_mov_b32 v2, v108\n"
    "ds_read2_b32 v[54:55], v51 offset0:0 offset1:64\n"
    "v_mfma_f32_16x16x32_bf16 v[84:87], v[248:251], a[60:63], v[84:87]\n"
    "v_mov_b32 v3, v112\n"
    "ds_read2_b32 v[156:157], v52 offset0:0 offset1:64\n"
    "v_mfma_f32_16x16x32_bf16 v[100:103], v[248:251], a[124:127], v[100:103]\n"
    "v_mfma_f32_16x16x32_bf16 v[88:91], v[252:255], a[60:63], v[88:91]\n"
    "v_mfma_f32_16x16x32_bf16 v[104:107], v[252:255], a[124:127], v[104:107]\n"
    "v_mfma_f32_16x16x32_bf16 v[152:155], a[188:191], v[12:15], v[152:155]\n"
    "v_mfma_f32_16x16x32_bf16 v[136:139], a[188:191], v[132:135], v[136:139]\n"
    "v_mfma_f32_16x16x32_bf16 v[140:143], a[188:191], v[244:247], v[140:143]\n"
    "s_nop 5\n"
    "v_mov_b32 v4, v152\n"
    "v_mov_b32 v5, v153\n"
    "v_mfma_f32_16x16x32_bf16 v[144:147], a[188:191], v[248:251], v[144:147]\n"
    "v_mov_b32 v6, v154\n"
    "v_mov_b32 v7, v155\n"
    "v_mfma_f32_16x16x32_bf16 v[148:151], a[188:191], v[252:255], v[148:151]\n"
    "s_waitcnt lgkmcnt(5)\n"
    "v_pk_fma_f32 v[116:117], v[166:167], v[2:3], v[76:77] op_sel:[0,0,0] op_sel_hi:[1,0,1] neg_lo:[1,0,0] neg_hi:[1,0,0]\n"
    "v_pk_fma_f32 v[120:121], v[116:117], v[72:73], s[68:69]\n"
    "v_pk_fma_f32 v[116:117], v[120:121], s[70:71], s[70:71] op_sel:[0,0,1] op_sel_hi:[1,0,1]\n"
    "v_pk_fma_f32 v[118:119], v[168:169], v[2:3], v[78:79] op_sel:[0,0,0] op_sel_hi:[1,0,1] neg_lo:[1,0,0] neg_hi:[1,0,0]\n"
    "ds_read_b128 v[76:79], v63\n"
    "v_pk_fma_f32 v[122:123], v[118:119], v[74:75], s[68:69]\n"
    "v_pk_fma_f32 v[118:119], v[122:123], s[70:71], s[70:71] op_sel:[0,0,1] op_sel_hi:[1,0,1]\n"
    "v_exp_f32 v174, v116 clamp\n"
    "v_exp_f32 v175, v117 clamp\n"
    "v_exp_f32 v176, v118 clamp\n"
    "v_exp_f32 v177, v119 clamp\n"
    "v_pk_add_f32 v[174:175], v[174:175], s[72:73]\n"
    "v_pk_add_f32 v[176:177], v[176:177], s[72:73]\n"
    "v_max_f32 v174, v120, v174\n"
    "v_max_f32 v175, v121, v175\n"
    "v_max_f32 v176, v122, v176\n"
    "v_max_f32 v177, v123, v177\n"
    "ds_read_b128 v[120:123], v62 offset:256\n"
    "v_cvt_pk_bf16_f32 v16, v174, v175\n"
    "v_cvt_pk_bf16_f32 v17, v176, v177\n"
    "v_pk_fma_f32 v[170:171], v[166:167], v[2:3], v[92:93] op_sel:[0,1,0] op_sel_hi:[1,1,1] neg_lo:[1,0,0] neg_hi:[1,0,0]\n"
    "v_pk_mul_f32 v[170:171], v[170:171], v[72:73]\n"
    "v_cvt_pk_bf16_f32 v68, v170, v171\n"
    "v_pk_fma_f32 v[172:173], v[168:169], v[2:3], v[94:95] op_sel:[0,1,0] op_sel_hi:[1,1,1] neg_lo:[1,0,0] neg_hi:[1,0,0]\n"
    "ds_read_b128 v[92:95], v63 offset:256\n"
    "v_pk_mul_f32 v[172:173], v[172:173], v[74:75]\n"
    "v_cvt_pk_bf16_f32 v69, v172, v173\n"
    "s_waitcnt lgkmcnt(6)\n"
    "v_pk_fma_f32 v[128:129], v[20:21], v[2:3], v[80:81] op_sel:[0,0,0] op_sel_hi:[1,0,1] neg_lo:[1,0,0] neg_hi:[1,0,0]\n"
    "v_pk_fma_f32 v[132:133], v[128:129], v[124:125], s[68:69]\n"
    "v_pk_fma_f32 v[128:129], v[132:133], s[70:71], s[70:71] op_sel:[0,0,1] op_sel_hi:[1,0,1]\n"
    "v_pk_fma_f32 v[130:131], v[22:23], v[2:3], v[82:83] op_sel:[0,0,0] op_sel_hi:[1,0,1] neg_lo:[1,0,0] neg_hi:[1,0,0]\n"
    "v_pk_fma_f32 v[134:135], v[130:131], v[126:127], s[68:69]\n"
    "v_pk_fma_f32 v[130:131], v[134:135], s[70:71], s[70:71] op_sel:[0,0,1] op_sel_hi:[1,0,1]\n"
    "v_exp_f32 v108, v128 clamp\n"
    "v_exp_f32 v109, v129 clamp\n"
    "v_exp_f32 v110, v130 clamp\n"
    "v_exp_f32 v111, v131 clamp\n"
    "v_pk_add_f32 v[108:109], v[108:109], s[72:73]\n"
    "v_pk_add_f32 v[110:111], v[110:111], s[72:73]\n"
    "v_max_f32 v108, v132, v108\n"
    "v_max_f32 v109, v133, v109\n"
    "v_max_f32 v110, v134, v110\n"
    "v_max_f32 v111, v135, v111\n"
    "v_cvt_pk_bf16_f32 v18, v108, v109\n"
    "v_cvt_pk_bf16_f32 v19, v110, v111\n"
    "v_pk_fma_f32 v[24:25], v[20:21], v[2:3], v[96:97] op_sel:[0,1,0] op_sel_hi:[1,1,1] neg_lo:[1,0,0] neg_hi:[1,0,0]\n"
    "v_pk_mul_f32 v[24:25], v[24:25], v[124:125]\n"
    "v_mfma_f32_16x16x32_bf16 v[184:187], v[12:15], v[16:19], 0\n"
    "v_cvt_pk_bf16_f32 v70, v24, v25\n"
    "v_pk_fma_f32 v[26:27], v[22:23], v[2:3], v[98:99] op_sel:[0,1,0] op_sel_hi:[1,1,1] neg_lo:[1,0,0] neg_hi:[1,0,0]\n"
    "v_pk_mul_f32 v[26:27], v[26:27], v[126:127]\n"
    "v_cvt_pk_bf16_f32 v71, v26, v27\n"
    "s_waitcnt lgkmcnt(1)\n"
    "v_pk_fma_f32 v[170:171], v[120:121], v[2:3], v[84:85] op_sel:[0,0,0] op_sel_hi:[1,0,1] neg_lo:[1,0,0] neg_hi:[1,0,0]\n"
    "v_pk_fma_f32 v[172:173], v[122:123], v[2:3], v[86:87] op_sel:[0,0,0] op_sel_hi:[1,0,1] neg_lo:[1,0,0] neg_hi:[1,0,0]\n"
    "v_mfma_f32_16x16x32_bf16 v[180:183], v[68:71], v[16:19], 0\n"
    "ds_read_b128 v[66:69], v62\n"
    "v_pk_fma_f32 v[174:175], v[120:121], v[2:3], v[100:101] op_sel:[0,1,0] op_sel_hi:[1,1,1] neg_lo:[1,0,0] neg_hi:[1,0,0]\n"
    "v_pk_fma_f32 v[176:177], v[122:123], v[2:3], v[102:103] op_sel:[0,1,0] op_sel_hi:[1,1,1] neg_lo:[1,0,0] neg_hi:[1,0,0]\n"
    "s_waitcnt lgkmcnt(1)\n"
    "v_pk_fma_f32 v[128:129], v[92:93], v[2:3], v[88:89] op_sel:[0,0,0] op_sel_hi:[1,0,1] neg_lo:[1,0,0] neg_hi:[1,0,0]\n"
    "v_pk_fma_f32 v[132:133], v[128:129], v[76:77], s[68:69]\n"
    "v_pk_fma_f32 v[128:129], v[132:133], s[70:71], s[70:71] op_sel:[0,0,1] op_sel_hi:[1,0,1]\n"
    "v_pk_fma_f32 v[130:131], v[94:95], v[2:3], v[90:91] op_sel:[0,0,0] op_sel_hi:[1,0,1] neg_lo:[1,0,0] neg_hi:[1,0,0]\n"
    "v_pk_fma_f32 v[134:135], v[130:131], v[78:79], s[68:69]\n"
    "v_pk_fma_f32 v[130:131], v[134:135], s[70:71], s[70:71] op_sel:[0,0,1] op_sel_hi:[1,0,1]\n"
    "v_exp_f32 v108, v128 clamp\n"
    "v_exp_f32 v109, v129 clamp\n"
    "v_exp_f32 v110, v130 clamp\n"
    "v_exp_f32 v111, v131 clamp\n"
    "v_pk_add_f32 v[108:109], v[108:109], s[72:73]\n"
    "v_pk_add_f32 v[110:111], v[110:111], s[72:73]\n"
    "v_max_f32 v108, v132, v108\n"
    "v_max_f32 v109, v133, v109\n"
    "v_max_f32 v110, v134, v110\n"
    "v_max_f32 v111, v135, v111\n"
    "v_cvt_pk_bf16_f32 v114, v108, v109\n"
    "s_waitcnt lgkmcnt(0)\n"
    "v_pk_fma_f32 v[72:73], v[170:171], v[66:67], s[68:69]\n"
    "v_pk_fma_f32 v[170:171], v[72:73], s[70:71], s[70:71] op_sel:[0,0,1] op_sel_hi:[1,0,1]\n"
    "v_pk_fma_f32 v[74:75], v[172:173], v[68:69], s[68:69]\n"
    "v_pk_fma_f32 v[172:173], v[74:75], s[70:71], s[70:71] op_sel:[0,0,1] op_sel_hi:[1,0,1]\n"
    "v_exp_f32 v166, v170 clamp\n"
    "v_exp_f32 v167, v171 clamp\n"
    "v_exp_f32 v168, v172 clamp\n"
    "v_exp_f32 v169, v173 clamp\n"
    "v_pk_add_f32 v[166:167], v[166:167], s[72:73]\n"
    "v_pk_add_f32 v[168:169], v[168:169], s[72:73]\n"
    "v_max_f32 v166, v72, v166\n"
    "v_max_f32 v167, v73, v167\n"
    "v_max_f32 v168, v74, v168\n"
    "v_max_f32 v169, v75, v169\n"
    "v_cvt_pk_bf16_f32 v112, v166, v167\n"
    "ds_read2_b32 v[166:167], v53 offset0:0 offset1:64\n"
    "v_cvt_pk_bf16_f32 v113, v168, v169\n"
    "v_pk_mul_f32 v[174:175], v[174:175], v[66:67]\n"
    "v_cvt_pk_bf16_f32 v116, v174, v175\n"
    "v_pk_mul_f32 v[176:177], v[176:177], v[68:69]\n"
    "v_cvt_pk_bf16_f32 v117, v176, v177\n"
    "v_cvt_pk_bf16_f32 v115, v110, v111\n"
    "v_pk_fma_f32 v[60:61], v[92:93], v[2:3], v[104:105] op_sel:[0,1,0] op_sel_hi:[1,1,1] neg_lo:[1,0,0] neg_hi:[1,0,0]\n"
    "v_pk_mul_f32 v[60:61], v[60:61], v[76:77]\n"
    "v_mfma_f32_16x16x32_bf16 v[184:187], v[12:15], v[112:115], v[184:187]\n"
    "v_cvt_pk_bf16_f32 v118, v60, v61\n"
    "v_pk_fma_f32 v[62:63], v[94:95], v[2:3], v[106:107] op_sel:[0,1,0] op_sel_hi:[1,1,1] neg_lo:[1,0,0] neg_hi:[1,0,0]\n"
    "v_pk_mul_f32 v[62:63], v[62:63], v[78:79]\n"
    "v_cvt_pk_bf16_f32 v119, v62, v63\n"
    "v_pk_fma_f32 v[24:25], v[4:5], v[40:41], v[136:137] op_sel:[0,1,0] op_sel_hi:[1,1,1] neg_lo:[1,0,0] neg_hi:[1,0,0]\n"
    "s_nop 2\n"
    "buffer_atomic_add_f32 v184, v65, s[20:23], s54 offen\n"
    "v_mfma_f32_16x16x32_bf16 v[180:183], v[116:119], v[112:115], v[180:183]\n"
    "v_pk_fma_f32 v[124:125], v[24:25], v[40:41], s[68:69] op_sel:[0,0,0] op_sel_hi:[1,0,1]\n"
    "v_pk_fma_f32 v[24:25], v[124:125], s[70:71], s[70:71] op_sel:[0,0,1] op_sel_hi:[1,0,1]\n"
    "v_pk_fma_f32 v[26:27], v[6:7], v[40:41], v[138:139] op_sel:[0,1,0] op_sel_hi:[1,1,1] neg_lo:[1,0,0] neg_hi:[1,0,0]\n"
    "v_pk_fma_f32 v[126:127], v[26:27], v[40:41], s[68:69] op_sel:[0,0,0] op_sel_hi:[1,0,1]\n"
    "s_nop 3\n"
    "v_mov_b32 v185, v180\n"
    "v_mov_b32_dpp v180, v182 row_shr:8 row_mask:0xf bank_mask:0xc\n"
    "v_mov_b32 v186, v181\n"
    "v_mov_b32_dpp v182, v185 row_shl:8 row_mask:0xf bank_mask:0x3\n"
    "v_mov_b32_dpp v181, v183 row_shr:8 row_mask:0xf bank_mask:0xc\n"
    "v_mov_b32_dpp v183, v186 row_shl:8 row_mask:0xf bank_mask:0x3\n"
    "v_mov_b32 v185, v180\n"
    "v_mov_b32_dpp v180, v181 row_shr:4 row_mask:0xf bank_mask:0xa\n"
    "buffer_atomic_add_f32 v180, v64, s[20:23], s74 offen offset:0\n"
    "v_mov_b32_dpp v181, v185 row_shl:4 row_mask:0xf bank_mask:0x5\n"
    "buffer_atomic_add_f32 v181, v64, s[20:23], s74 offen offset:256\n"
    "v_mov_b32 v186, v182\n"
    "v_mov_b32_dpp v182, v183 row_shr:4 row_mask:0xf bank_mask:0xa\n"
    "buffer_atomic_add_f32 v182, v64, s[20:23], s74 offen offset:512\n"
    "v_mov_b32_dpp v183, v186 row_shl:4 row_mask:0xf bank_mask:0x5\n"
    "buffer_atomic_add_f32 v183, v64, s[20:23], s74 offen offset:768\n"
    "s_lshl_b32 s74, s50, 10\n"
    "s_add_u32 s75, s75, s74\n"
    "s_add_u32 s75, s75, 0x100000\n"
    "v_pk_fma_f32 v[26:27], v[126:127], s[70:71], s[70:71] op_sel:[0,0,1] op_sel_hi:[1,0,1]\n"
    "v_exp_f32 v20, v24 clamp\n"
    "v_exp_f32 v21, v25 clamp\n"
    "v_exp_f32 v22, v26 clamp\n"
    "v_exp_f32 v23, v27 clamp\n"
    "v_pk_add_f32 v[20:21], v[20:21], s[72:73]\n"
    "Lpg2_%=:\n"
    "s_cbranch_execz Ltb0_2_0_%=\n"
    "v_pk_add_f32 v[22:23], v[22:23], s[72:73]\n"
    "v_max_f32 v20, v124, v20\n"
    "v_max_f32 v21, v125, v21\n"
    "v_max_f32 v22, v126, v22\n"
    "v_max_f32 v23, v127, v23\n"
    "v_cvt_pk_bf16_f32 v178, v20, v21\n"
    "v_cvt_pk_bf16_f32 v179, v22, v23\n"
    "v_pk_fma_f32 v[80:81], v[4:5], v[54:55], v[140:141] op_sel:[0,1,0] op_sel_hi:[1,1,1] neg_lo:[1,0,0] neg_hi:[1,0,0]\n"
    "v_pk_fma_f32 v[96:97], v[80:81], v[54:55], s[68:69] op_sel:[0,0,0] op_sel_hi:[1,0,1]\n"
    "v_pk_fma_f32 v[80:81], v[96:97], s[70:71], s[70:71] op_sel:[0,0,1] op_sel_hi:[1,0,1]\n"
    "v_pk_fma_f32 v[82:83], v[6:7], v[54:55], v[142:143] op_sel:[0,1,0] op_sel_hi:[1,1,1] neg_lo:[1,0,0] neg_hi:[1,0,0]\n"
    "v_pk_fma_f32 v[98:99], v[82:83], v[54:55], s[68:69] op_sel:[0,0,0] op_sel_hi:[1,0,1]\n"
    "v_pk_fma_f32 v[82:83], v[98:99], s[70:71], s[70:71] op_sel:[0,0,1] op_sel_hi:[1,0,1]\n"
    "v_exp_f32 v16, v80 clamp\n"
    "v_exp_f32 v17, v81 clamp\n"
    "v_exp_f32 v18, v82 clamp\n"
    "v_exp_f32 v19, v83 clamp\n"
    "v_pk_add_f32 v[16:17], v[16:17], s[72:73]\n"
    "v_pk_add_f32 v[18:19], v[18:19], s[72:73]\n"
    "v_max_f32 v16, v96, v16\n"
    "v_max_f32 v17, v97, v17\n"
    "v_max_f32 v18, v98, v18\n"
    "v_max_f32 v19, v99, v19\n"
    "v_cvt_pk_bf16_f32 v180, v16, v17\n"
    "v_cvt_pk_bf16_f32 v181, v18, v19\n"
    "v_pk_fma_f32 v[70:71], v[4:5], v[156:157], v[144:145] op_sel:[0,1,0] op_sel_hi:[1,1,1] neg_lo:[1,0,0] neg_hi:[1,0,0]\n"
    "v_permlane16_swap_b32 v178, v180\n"
    "v_permlane16_swap_b32 v179, v181\n"
    "buffer_store_dwordx4 v[178:181], v57, s[20:23], s75 offen sc1\n"
    "v_pk_fma_f32 v[170:171], v[70:71], v[156:157], s[68:69] op_sel:[0,0,0] op_sel_hi:[1,0,1]\n"
    "v_pk_fma_f32 v[70:71], v[170:171], s[70:71], s[70:71] op_sel:[0,0,1] op_sel_hi:[1,0,1]\n"
    "v_pk_fma_f32 v[72:73], v[6:7], v[156:157], v[146:147] op_sel:[0,1,0] op_sel_hi:[1,1,1] neg_lo:[1,0,0] neg_hi:[1,0,0]\n"
    "v_pk_fma_f32 v[172:173], v[72:73], v[156:157], s[68:69] op_sel:[0,0,0] op_sel_hi:[1,0,1]\n"
    "v_pk_fma_f32 v[72:73], v[172:173], s[70:71], s[70:71] op_sel:[0,0,1] op_sel_hi:[1,0,1]\n"
    "v_exp_f32 v74, v70 clamp\n"
    "v_exp_f32 v75, v71 clamp\n"
    "v_exp_f32 v76, v72 clamp\n"
    "v_exp_f32 v77, v73 clamp\n"
    "v_pk_add_f32 v[74:75], v[74:75], s[72:73]\n"
    "v_pk_add_f32 v[76:77], v[76:77], s[72:73]\n"
    "v_max_f32 v74, v170, v74\n"
    "v_max_f32 v75, v171, v75\n"
    "v_max_f32 v76, v172, v76\n"
    "v_max_f32 v77, v173, v77\n"
    "v_cvt_pk_bf16_f32 v36, v74, v75\n"
    "v_cvt_pk_bf16_f32 v37, v76, v77\n"
    "s_waitcnt lgkmcnt(0)\n"
    "v_pk_fma_f32 v[168:169], v[4:5], v[166:167], v[148:149] op_sel:[0,1,0] op_sel_hi:[1,1,1] neg_lo:[1,0,0] neg_hi:[1,0,0]\n"
    "v_pk_fma_f32 v[174:175], v[168:169], v[166:167], s[68:69] op_sel:[0,0,0] op_sel_hi:[1,0,1]\n"
    "v_pk_fma_f32 v[168:169], v[174:175], s[70:71], s[70:71] op_sel:[0,0,1] op_sel_hi:[1,0,1]\n"
    "v_pk_fma_f32 v[170:171], v[6:7], v[166:167], v[150:151] op_sel:[0,1,0] op_sel_hi:[1,1,1] neg_lo:[1,0,0] neg_hi:[1,0,0]\n"
    "v_pk_fma_f32 v[176:177], v[170:171], v[166:167], s[68:69] op_sel:[0,0,0] op_sel_hi:[1,0,1]\n"
    "v_pk_fma_f32 v[170:171], v[176:177], s[70:71], s[70:71] op_sel:[0,0,1] op_sel_hi:[1,0,1]\n"
    "v_exp_f32 v66, v168 clamp\n"
    "v_exp_f32 v67, v169 clamp\n"
    "v_exp_f32 v68, v170 clamp\n"
    "v_exp_f32 v69, v171 clamp\n"
    "v_pk_add_f32 v[66:67], v[66:67], s[72:73]\n"
    "v_pk_add_f32 v[68:69], v[68:69], s[72:73]\n"
    "v_max_f32 v66, v174, v66\n"
    "v_max_f32 v67, v175, v67\n"
    "v_max_f32 v68, v176, v68\n"
    "v_max_f32 v69, v177, v69\n"
    "v_cvt_pk_bf16_f32 v38, v66, v67\n"
    "v_cvt_pk_bf16_f32 v39, v68, v69\n"
    "s_nop 0\n"
    "v_permlane16_swap_b32 v36, v38\n"
    "v_permlane16_swap_b32 v37, v39\n"
    "buffer_store_dwordx4 v[36:39], v59, s[20:23], s75 offen sc1\n"
    "s_barrier\n"
    "s_lshl_b32 s59, s66, 1\n"
    "s_add_u32 s59, s59, 6\n"
    "s_and_b32 s59, s59, 7\n"
    "s_lshl_b32 s59, s59, 16\n"
    "s_add_u32 s59, s59, s101\n"
    "s_add_u32 m0, s51, 0x10000\n"
    "s_lshr_b32 s55, s24, 1\n"
    "buffer_load_dwordx4 v42, s[16:19], s59 offen offset:0 lds\n"
    "buffer_load_dwordx4 v43, s[16:19], s59 offen offset:1024 lds\n"
    "buffer_load_dwordx4 v44, s[16:19], s59 offen offset:2048 lds\n"
    "s_lshl_b32 s55, s55, 16\n"
    "s_lshl_b32 s56, s45, 14\n"
    "s_add_u32 s55, s55, s56\n"
    "s_lshl_b32 s56, s7, 12\n"
    "s_add_u32 s55, s55, s56\n"
    "s_and_b32 s56, s24, 1\n"
    "s_lshl_b32 s56, s56, 6\n"
    "s_add_u32 s52, s55, s56\n"
    "s_lshl_b32 s55, s24, 15\n"
    "s_lshl_b32 s56, s45, 8\n"
    "s_add_u32 s55, s55, s56\n"
    "s_lshl_b32 s56, s7, 6\n"
    "s_add_u32 s55, s55, s56\n"
    "s_add_u32 s55, s55, 0xc00\n"
    "s_add_u32 s56, s55, 0x4000\n"
    "s_mov_b32 s36, 0\n"
    "buffer_load_dwordx4 v45, s[16:19], s59 offen offset:3072 lds\n"
    "s_mov_b32 s37, 0x41500000\n"
    "s_mov_b32 s38, 0\n"
    "s_mov_b32 s39, 0xc1900000\n"
    "s_mov_b32 s40, 0x48000000\n"
    "s_mov_b32 s41, 0x4a000000\n"
    "v_and_b32 v16, 1, v9\n"
    "v_lshlrev_b32 v16, 4, v16\n"
    "v_mov_b32 v26, 0x3f80\n"
    "v_lshlrev_b32 v26, v16, v26\n"
    "v_lshrrev_b32 v16, 3, v9\n"
    "v_lshrrev_b32 v17, 1, v10\n"
    "v_cmp_eq_u32 s[60:61], v16, v17\n"
    "v_bfe_u32 v27, v9, 1, 2\n"
    "v_and_b32 v16, 1, v10\n"
    "v_cndmask_b32 v26, 0, v26, s[60:61]\n"
    "v_lshl_add_u32 v27, v16, 2, v27\n"
    "v_cmp_eq_u32 vcc, 0, v27\n"
    "v_lshlrev_b32 v28, 7, v9\n"
    "v_and_b32 v16, 1, v10\n"
    "v_cndmask_b32 v18, 0, v26, vcc\n"
    "v_cmp_eq_u32 vcc, 1, v27\n"
    "v_lshl_add_u32 v28, v16, 5, v28\n"
    "v_lshrrev_b32 v16, 1, v10\n"
    "v_cndmask_b32 v19, 0, v26, vcc\n"
    "v_cmp_eq_u32 vcc, 2, v27\n"
    "v_lshl_add_u32 v28, v16, 4, v28\n"
    "v_lshlrev_b32 v17, 10, v9\n"
    "v_cndmask_b32 v20, 0, v26, vcc\n"
    "v_cmp_eq_u32 vcc, 3, v27\n"
    "v_and_b32 v16, 1, v10\n"
    "v_lshl_add_u32 v17, v16, 5, v17\n"
    "v_cndmask_b32 v21, 0, v26, vcc\n"
    "v_cmp_eq_u32 vcc, 4, v27\n"
    "v_lshrrev_b32 v16, 1, v10\n"
    "v_lshl_add_u32 v17, v16, 4, v17\n"
    "buffer_load_dwordx4 v[30:33], v17, s[8:11], s55 offen\n"
    "buffer_load_dwordx4 v[34:37], v17, s[8:11], s56 offen\n"
    "s_lshl_b32 s55, s7, 10\n"
    "s_add_u32 s55, s55, 0x8000\n"
    "v_cndmask_b32 v22, 0, v26, vcc\n"
    "v_cmp_eq_u32 vcc, 5, v27\n"
    "v_and_b32 v39, 31, v8\n"
    "v_lshrrev_b32 v40, 5, v8\n"
    "v_cndmask_b32 v23, 0, v26, vcc\n"
    "v_cmp_eq_u32 vcc, 6, v27\n"
    "v_lshlrev_b32 v29, 4, v8\n"
    "v_lshl_add_u32 v41, v9, 7, s55\n"
    "s_sub_u32 s55, s7, s66\n"
    "s_and_b32 s55, s55, 3\n"
    "s_lshl_b32 s55, s55, 8\n"
    "v_cndmask_b32 v24, 0, v26, vcc\n"
    "v_cmp_eq_u32 vcc, 7, v27\n"
    "v_lshl_add_u32 v41, v10, 4, v41\n"
    "v_mov_b32 v16, 0x7ffffff0\n"
    "v_cndmask_b32 v25, 0, v26, vcc\n"
    "v_cmp_gt_u32 vcc, 8, v9\n"
    "v_mov_b32 v38, v29\n"
    "s_nop 0\n"
    "v_cndmask_b32 v41, v16, v41, vcc\n"
    "v_xor_b32 v16, v40, v9\n"
    "v_lshlrev_b32 v50, 4, v16\n"
    "v_lshl_add_u32 v50, v39, 10, v50\n"
    "v_add_u32 v50, s55, v50\n"
    "v_add_u32 v50, 0x18000, v50\n"
    "v_xor_b32 v16, v10, v9\n"
    "v_lshlrev_b32 v16, 4, v16\n"
    "v_lshl_add_u32 v51, v9, 10, v16\n"
    "v_add_u32 v51, 0x18000, v51\n"
    "v_xor_b32 v52, 64, v51\n"
    "v_xor_b32 v53, 0x80, v51\n"
    "v_xor_b32 v54, 0xc0, v51\n"
    "v_lshrrev_b32 v16, 1, v10\n"
    "v_lshlrev_b32 v55, 14, v16\n"
    "v_bfe_u32 v16, v9, 1, 2\n"
    "v_lshl_add_u32 v55, v16, 10, v55\n"
    "v_and_b32 v16, 1, v10\n"
    "v_lshl_add_u32 v55, v16, 5, v55\n"
    "v_lshrrev_b32 v16, 3, v9\n"
    "v_lshl_add_u32 v55, v16, 4, v55\n"
    "v_and_b32 v16, 1, v9\n"
    "v_lshl_add_u32 v55, v16, 3, v55\n"
    "v_mov_b32 v16, v16\n"
    "s_waitcnt vmcnt(6)\n"
    "s_barrier\n"
    "s_cmp_eq_u32 s7, 0\n"
    "s_cbranch_scc0 Larr_9_%=\n"
    "s_and_b32 s55, s6, 7\n"
    "s_lshl_b32 s55, s55, 8\n"
    "s_add_u32 s55, s55, 0x1000\n"
    "v_mov_b32 v188, 1\n"
    "v_mov_b32 v16, 0x7ffffff0\n"
    "v_cmp_eq_u32 vcc, 0, v8\n"
    "s_nop 1\n"
    "v_cndmask_b32 v16, v16, 0, vcc\n"
    "buffer_atomic_add v188, v16, s[20:23], s55 offen sc0\n"
    "Larr_9_%=:\n"
    "ds_read_b64_tr_b16 a[192:193], v46 offset:32768\n"
    "ds_read_b64_tr_b16 a[194:195], v46 offset:33280\n"
    "ds_read_b64_tr_b16 v[192:193], v49 offset:32768\n"
    "ds_read_b64_tr_b16 v[194:195], v49 offset:33280\n"
    "ds_read_b64_tr_b16 a[196:197], v46 offset:34816\n"
    "ds_read_b64_tr_b16 a[198:199], v46 offset:35328\n"
    "ds_read_b64_tr_b16 v[196:197], v49 offset:34816\n"
    "ds_read_b64_tr_b16 v[198:199], v49 offset:35328\n"
    "ds_read_b64_tr_b16 a[208:209], v46 offset:49152\n"
    "s_lshl_b32 s59, s66, 1\n"
    "ds_read_b64_tr_b16 a[210:211], v46 offset:49664\n"
    "s_add_u32 s59, s59, 1\n"
    "ds_read_b64_tr_b16 v[208:209], v49 offset:49152\n"
    "s_and_b32 s59, s59, 7\n"
    "ds_read_b64_tr_b16 v[210:211], v49 offset:49664\n"
    "ds_read_b64_tr_b16 a[212:213], v46 offset:51200\n"
    "s_lshl_b32 s59, s59, 16\n"
    "ds_read_b64_tr_b16 a[214:215], v46 offset:51712\n"
    "s_add_u32 s59, s59, s101\n"
    "ds_read_b64_tr_b16 v[212:213], v49 offset:51200\n"
    "s_add_u32 m0, s51, 0x14000\n"
    "ds_read_b64_tr_b16 v[214:215], v49 offset:51712\n"
    "buffer_load_dwordx4 v42, s[16:19], s59 offen offset:0 lds\n"
    "buffer_load_dwordx4 v43, s[16:19], s59 offen offset:1024 lds\n"
    "buffer_load_dwordx4 v44, s[16:19], s59 offen offset:2048 lds\n"
    "v_and_b32 v112, 0x7f, v0\n"
    "v_bfe_u32 v111, v112, 1, 2\n"
    "v_bfe_u32 v110, v112, 3, 1\n"
    "v_lshlrev_b32 v110, 1, v110\n"
    "v_xor_b32 v111, v111, v110\n"
    "v_lshlrev_b32 v111, 5, v111\n"
    "v_lshrrev_b32 v110, 7, v0\n"
    "v_lshl_add_u32 v110, v110, 8, v112\n"
    "v_lshl_add_u32 v110, v110, 7, v111\n"
    "v_xor_b32 v113, 32, v110\n"
    "v_xor_b32 v114, 64, v110\n"
    "v_xor_b32 v115, 0x60, v110\n"
    "v_mul_f32 v116, 0x3fb8aa3b, v164\n"
    "v_exp_f32 v116, v116\n"
    "buffer_load_dwordx4 v45, s[16:19], s59 offen offset:3072 lds\n"
    "v_cvt_pk_bf16_f32 v116, 0, v116\n"
    "v_mul_f32 v118, v116, v158\n"
    "v_mul_f32 v119, v116, v159\n"
    "v_mul_f32 v122, v116, v160\n"
    "v_mul_f32 v123, v116, v161\n"
    "v_mul_f32 v120, v163, v123\n"
    "v_mul_f32 v121, v163, v122\n"
    "v_fma_f32 v120, v162, v122, -v120\n"
    "v_fmac_f32 v121, v162, v123\n"
    "v_mul_f32 v118, 0x3fb8aa3b, v118\n"
    "v_mul_f32 v119, 0x3e22f983, v119\n"
    "v_exp_f32 v117, v118\n"
    "v_fract_f32 v126, v119\n"
    "v_cos_f32 v124, v126\n"
    "v_sin_f32 v125, v126\n"
    "v_mul_f32 v124, v117, v124\n"
    "v_mul_f32 v125, v117, v125\n"
    "v_mul_f32 v127, 0x41000000, v118\n"
    "v_exp_f32 v127, v127\n"
    "v_mul_f32 v130, 0x41000000, v119\n"
    "v_fract_f32 v130, v130\n"
    "v_cos_f32 v128, v130\n"
    "v_sin_f32 v129, v130\n"
    "v_mul_f32 v128, v127, v128\n"
    "v_mul_f32 v129, v127, v129\n"
    "v_mul_f32 v131, 0x43000000, v118\n"
    "v_exp_f32 v131, v131\n"
    "v_mul_f32 v134, 0x43000000, v119\n"
    "v_fract_f32 v134, v134\n"
    "v_cos_f32 v132, v134\n"
    "v_sin_f32 v133, v134\n"
    "v_mul_f32 v132, v131, v132\n"
    "v_mul_f32 v133, v131, v133\n"
    "v_pk_mul_f32 v[116:117], v[120:121], v[124:125] op_sel:[0,0] op_sel_hi:[0,1]\n"
    "v_pk_fma_f32 v[116:117], v[120:121], v[124:125], v[116:117] op_sel:[1,1,0] op_sel_hi:[1,0,1] neg_lo:[1,0,0]\n"
    "v_pk_mul_f32 v[118:119], v[116:117], v[124:125] op_sel:[0,0] op_sel_hi:[0,1]\n"
    "v_pk_fma_f32 v[118:119], v[116:117], v[124:125], v[118:119] op_sel:[1,1,0] op_sel_hi:[1,0,1] neg_lo:[1,0,0]\n"
    "v_pk_mul_f32 v[122:123], v[118:119], v[124:125] op_sel:[0,0] op_sel_hi:[0,1]\n"
    "v_pk_fma_f32 v[122:123], v[118:119], v[124:125], v[122:123] op_sel:[1,1,0] op_sel_hi:[1,0,1] neg_lo:[1,0,0]\n"
    "v_pk_mul_f32 v[126:127], v[122:123], v[124:125] op_sel:[0,0] op_sel_hi:[0,1]\n"
    "v_pk_fma_f32 v[126:127], v[122:123], v[124:125], v[126:127] op_sel:[1,1,0] op_sel_hi:[1,0,1] neg_lo:[1,0,0]\n"
    "v_pk_mul_f32 v[130:131], v[126:127], v[124:125] op_sel:[0,0] op_sel_hi:[0,1]\n"
    "v_pk_fma_f32 v[130:131], v[126:127], v[124:125], v[130:131] op_sel:[1,1,0] op_sel_hi:[1,0,1] neg_lo:[1,0,0]\n"
    "v_pk_mul_f32 v[134:135], v[130:131], v[124:125] op_sel:[0,0] op_sel_hi:[0,1]\n"
    "v_pk_fma_f32 v[134:135], v[130:131], v[124:125], v[134:135] op_sel:[1,1,0] op_sel_hi:[1,0,1] neg_lo:[1,0,0]\n"
    "v_pk_mul_f32 v[136:137], v[134:135], v[124:125] op_sel:[0,0] op_sel_hi:[0,1]\n"
    "v_pk_fma_f32 v[136:137], v[134:135], v[124:125], v[136:137] op_sel:[1,1,0] op_sel_hi:[1,0,1] neg_lo:[1,0,0]\n"
    "v_cvt_pk_bf16_f32 v102, v120, v116\n"
    "v_cvt_pk_bf16_f32 v103, v118, v122\n"
    "v_cvt_pk_bf16_f32 v104, v126, v130\n"
    "v_cvt_pk_bf16_f32 v105, v134, v136\n"
    "ds_write_b128 v110, v[102:105] offset:0\n"
    "v_cvt_pk_bf16_f32 v106, -v121, -v117\n"
    "v_cvt_pk_bf16_f32 v107, -v119, -v123\n"
    "v_cvt_pk_bf16_f32 v108, -v127, -v131\n"
    "v_cvt_pk_bf16_f32 v109, -v135, -v137\n"
    "v_pk_mul_f32 v[138:139], v[132:133], v[132:133] op_sel:[0,0] op_sel_hi:[0,1]\n"
    "ds_write_b128 v110, v[106:109] offset:16384\n"
    "v_pk_fma_f32 v[138:139], v[132:133], v[132:133], v[138:139] op_sel:[1,1,0] op_sel_hi:[1,0,1] neg_lo:[1,0,0]\n"
    "v_pk_mul_f32 v[140:141], v[138:139], v[132:133] op_sel:[0,0] op_sel_hi:[0,1]\n"
    "v_pk_fma_f32 v[140:141], v[138:139], v[132:133], v[140:141] op_sel:[1,1,0] op_sel_hi:[1,0,1] neg_lo:[1,0,0]\n"
    "v_pk_mul_f32 v[142:143], v[138:139], v[138:139] op_sel:[0,0] op_sel_hi:[0,1]\n"
    "v_pk_fma_f32 v[142:143], v[138:139], v[138:139], v[142:143] op_sel:[1,1,0] op_sel_hi:[1,0,1] neg_lo:[1,0,0]\n"
    "s_cmp_eq_u32 s7, 0\n"
    "s_cbranch_scc0 Lrel_10_%=\n"
    "s_waitcnt vmcnt(4)\n"
    "v_readfirstlane_b32 s55, v188\n"
    "s_nop 0\n"
    "s_cmp_eq_u32 s55, 31\n"
    "s_cbranch_scc0 Lrel_10_%=\n"
    "s_and_b32 s55, s6, 7\n"
    "s_lshl_b32 s55, s55, 8\n"
    "s_add_u32 s55, s55, 0x1800\n"
    "v_lshlrev_b32 v17, 11, v8\n"
    "v_mov_b32 v26, 0x7ffffff0\n"
    "v_cmp_gt_u32 vcc, 8, v8\n"
    "s_nop 1\n"
    "v_cndmask_b32 v17, v26, v17, vcc\n"
    "v_mov_b32 v26, 1\n"
    "buffer_store_dword v26, v17, s[20:23], s55 offen sc1\n"
    "Lrel_10_%=:\n"
    "ds_read_b64_tr_b16 a[224:225], v47 offset:0\n"
    "ds_read_b64_tr_b16 a[226:227], v47 offset:512\n"
    "ds_read_b64_tr_b16 v[224:225], v48 offset:0\n"
    "ds_read_b64_tr_b16 v[226:227], v48 offset:512\n"
    "ds_read_b64_tr_b16 a[228:229], v47 offset:2048\n"
    "ds_read_b64_tr_b16 a[230:231], v47 offset:2560\n"
    "ds_read_b64_tr_b16 v[228:229], v48 offset:2048\n"
    "ds_read_b64_tr_b16 v[230:231], v48 offset:2560\n"
    "s_waitcnt vmcnt(6)\n"
    "ds_read_b64_tr_b16 a[240:241], v46 offset:0\n"
    "s_lshl_b32 s59, s66, 1\n"
    "ds_read_b64_tr_b16 a[242:243], v46 offset:512\n"
    "s_add_u32 s59, s59, 3\n"
    "ds_read_b64_tr_b16 v[240:241], v49 offset:0\n"
    "s_and_b32 s59, s59, 7\n"
    "ds_read_b64_tr_b16 v[242:243], v49 offset:512\n"
    "ds_read_b64_tr_b16 a[244:245], v46 offset:2048\n"
    "s_lshl_b32 s59, s59, 16\n"
    "ds_read_b64_tr_b16 a[246:247], v46 offset:2560\n"
    "s_add_u32 s59, s59, s101\n"
    "ds_read_b64_tr_b16 v[244:245], v49 offset:2048\n"
    "v_pk_mul_f32 v[124:125], v[120:121], v[132:133] op_sel:[0,0] op_sel_hi:[0,1]\n"
    "ds_read_b64_tr_b16 v[246:247], v49 offset:2560\n"
    "v_pk_fma_f32 v[124:125], v[120:121], v[132:133], v[124:125] op_sel:[1,1,0] op_sel_hi:[1,0,1] neg_lo:[1,0,0]\n"
    "v_pk_mul_f32 v[144:145], v[116:117], v[132:133] op_sel:[0,0] op_sel_hi:[0,1]\n"
    "v_pk_fma_f32 v[144:145], v[116:117], v[132:133], v[144:145] op_sel:[1,1,0] op_sel_hi:[1,0,1] neg_lo:[1,0,0]\n"
    "v_pk_mul_f32 v[146:147], v[118:119], v[132:133] op_sel:[0,0] op_sel_hi:[0,1]\n"
    "v_pk_fma_f32 v[146:147], v[118:119], v[132:133], v[146:147] op_sel:[1,1,0] op_sel_hi:[1,0,1] neg_lo:[1,0,0]\n"
    "v_pk_mul_f32 v[148:149], v[122:123], v[132:133] op_sel:[0,0] op_sel_hi:[0,1]\n"
    "v_pk_fma_f32 v[148:149], v[122:123], v[132:133], v[148:149] op_sel:[1,1,0] op_sel_hi:[1,0,1] neg_lo:[1,0,0]\n"
    "v_pk_mul_f32 v[150:151], v[126:127], v[132:133] op_sel:[0,0] op_sel_hi:[0,1]\n"
    "v_pk_fma_f32 v[150:151], v[126:127], v[132:133], v[150:151] op_sel:[1,1,0] op_sel_hi:[1,0,1] neg_lo:[1,0,0]\n"
    "v_pk_mul_f32 v[152:153], v[130:131], v[132:133] op_sel:[0,0] op_sel_hi:[0,1]\n"
    "v_pk_fma_f32 v[152:153], v[130:131], v[132:133], v[152:153] op_sel:[1,1,0] op_sel_hi:[1,0,1] neg_lo:[1,0,0]\n"
    "v_pk_mul_f32 v[154:155], v[134:135], v[132:133] op_sel:[0,0] op_sel_hi:[0,1]\n"
    "v_pk_fma_f32 v[154:155], v[134:135], v[132:133], v[154:155] op_sel:[1,1,0] op_sel_hi:[1,0,1] neg_lo:[1,0,0]\n"
    "s_waitcnt lgkmcnt(8)\n"
    "s_add_u32 m0, s51, 0x20000\n"
    "v_pk_mul_f32 v[156:157], v[136:137], v[132:133] op_sel:[0,0] op_sel_hi:[0,1]\n"
    "buffer_load_dwordx4 v42, s[16:19], s59 offen offset:0 lds\n"
    "buffer_load_dwordx4 v43, s[16:19], s59 offen offset:1024 lds\n"
    "buffer_load_dwordx4 v44, s[16:19], s59 offen offset:2048 lds\n"
    "v_pk_fma_f32 v[156:157], v[136:137], v[132:133], v[156:157] op_sel:[1,1,0] op_sel_hi:[1,0,1] neg_lo:[1,0,0]\n"
    "v_cvt_pk_bf16_f32 v102, v124, v144\n"
    "v_cvt_pk_bf16_f32 v103, v146, v148\n"
    "v_cvt_pk_bf16_f32 v104, v150, v152\n"
    "v_cvt_pk_bf16_f32 v105, v154, v156\n"
    "ds_write_b128 v110, v[102:105] offset:16\n"
    "v_cvt_pk_bf16_f32 v106, -v125, -v145\n"
    "v_cvt_pk_bf16_f32 v107, -v147, -v149\n"
    "v_cvt_pk_bf16_f32 v108, -v151, -v153\n"
    "v_cvt_pk_bf16_f32 v109, -v155, -v157\n"
    "v_pk_mul_f32 v[124:125], v[120:121], v[138:139] op_sel:[0,0] op_sel_hi:[0,1]\n"
    "ds_write_b128 v110, v[106:109] offset:16400\n"
    "v_pk_fma_f32 v[124:125], v[120:121], v[138:139], v[124:125] op_sel:[1,1,0] op_sel_hi:[1,0,1] neg_lo:[1,0,0]\n"
    "buffer_load_dwordx4 v45, s[16:19], s59 offen offset:3072 lds\n"
    "s_lshl_b32 s59, s66, 1\n"
    "s_add_u32 s59, s59, 5\n"
    "s_and_b32 s59, s59, 7\n"
    "s_lshl_b32 s59, s59, 16\n"
    "s_add_u32 s59, s59, s101\n"
    "s_waitcnt lgkmcnt(2)\n"
    "s_add_u32 m0, s51, 0x10000\n"
    "v_pk_mul_f32 v[144:145], v[118:119], v[138:139] op_sel:[0,0] op_sel_hi:[0,1]\n"
    "v_pk_fma_f32 v[144:145], v[118:119], v[138:139], v[144:145] op_sel:[1,1,0] op_sel_hi:[1,0,1] neg_lo:[1,0,0]\n"
    "v_pk_mul_f32 v[146:147], v[126:127], v[138:139] op_sel:[0,0] op_sel_hi:[0,1]\n"
    "v_pk_fma_f32 v[146:147], v[126:127], v[138:139], v[146:147] op_sel:[1,1,0] op_sel_hi:[1,0,1] neg_lo:[1,0,0]\n"
    "v_pk_mul_f32 v[148:149], v[134:135], v[138:139] op_sel:[0,0] op_sel_hi:[0,1]\n"
    "v_pk_fma_f32 v[148:149], v[134:135], v[138:139], v[148:149] op_sel:[1,1,0] op_sel_hi:[1,0,1] neg_lo:[1,0,0]\n"
    "v_pk_mul_f32 v[150:151], v[120:121], v[140:141] op_sel:[0,0] op_sel_hi:[0,1]\n"
    "v_pk_fma_f32 v[150:151], v[120:121], v[140:141], v[150:151] op_sel:[1,1,0] op_sel_hi:[1,0,1] neg_lo:[1,0,0]\n"
    "v_pk_mul_f32 v[152:153], v[118:119], v[140:141] op_sel:[0,0] op_sel_hi:[0,1]\n"
    "v_pk_fma_f32 v[152:153], v[118:119], v[140:141], v[152:153] op_sel:[1,1,0] op_sel_hi:[1,0,1] neg_lo:[1,0,0]\n"
    "buffer_load_dwordx4 v42, s[16:19], s59 offen offset:0 lds\n"
    "v_pk_mul_f32 v[154:155], v[126:127], v[140:141] op_sel:[0,0] op_sel_hi:[0,1]\n"
    "v_pk_fma_f32 v[154:155], v[126:127], v[140:141], v[154:155] op_sel:[1,1,0] op_sel_hi:[1,0,1] neg_lo:[1,0,0]\n"
    "v_pk_mul_f32 v[156:157], v[134:135], v[140:141] op_sel:[0,0] op_sel_hi:[0,1]\n"
    "v_pk_fma_f32 v[156:157], v[134:135], v[140:141], v[156:157] op_sel:[1,1,0] op_sel_hi:[1,0,1] neg_lo:[1,0,0]\n"
    "v_cvt_pk_bf16_f32 v102, v124, v144\n"
    "v_cvt_pk_bf16_f32 v103, v146, v148\n"
    "v_cvt_pk_bf16_f32 v104, v150, v152\n"
    "v_cvt_pk_bf16_f32 v105, v154, v156\n"
    "ds_write_b128 v113, v[102:105] offset:0\n"
    "v_cvt_pk_bf16_f32 v106, -v125, -v145\n"
    "v_cvt_pk_bf16_f32 v107, -v147, -v149\n"
    "v_cvt_pk_bf16_f32 v108, -v151, -v153\n"
    "v_cvt_pk_bf16_f32 v109, -v155, -v157\n"
    "ds_write_b128 v113, v[106:109] offset:16384\n"
    "buffer_load_dwordx4 v43, s[16:19], s59 offen offset:1024 lds\n"
    "buffer_load_dwordx4 v44, s[16:19], s59 offen offset:2048 lds\n"
    "buffer_load_dwordx4 v45, s[16:19], s59 offen offset:3072 lds\n"
    "v_pk_mul_f32 v[116:117], v[142:143], v[132:133] op_sel:[0,0] op_sel_hi:[0,1]\n"
    "v_pk_fma_f32 v[116:117], v[142:143], v[132:133], v[116:117] op_sel:[1,1,0] op_sel_hi:[1,0,1] neg_lo:[1,0,0]\n"
    "v_pk_mul_f32 v[118:119], v[142:143], v[138:139] op_sel:[0,0] op_sel_hi:[0,1]\n"
    "v_pk_fma_f32 v[118:119], v[142:143], v[138:139], v[118:119] op_sel:[1,1,0] op_sel_hi:[1,0,1] neg_lo:[1,0,0]\n"
    "v_pk_mul_f32 v[122:123], v[142:143], v[140:141] op_sel:[0,0] op_sel_hi:[0,1]\n"
    "v_pk_fma_f32 v[122:123], v[142:143], v[140:141], v[122:123] op_sel:[1,1,0] op_sel_hi:[1,0,1] neg_lo:[1,0,0]\n"
    "v_pk_mul_f32 v[124:125], v[142:143], v[142:143] op_sel:[0,0] op_sel_hi:[0,1]\n"
    "v_pk_fma_f32 v[124:125], v[142:143], v[142:143], v[124:125] op_sel:[1,1,0] op_sel_hi:[1,0,1] neg_lo:[1,0,0]\n"
    "v_pk_mul_f32 v[130:131], v[120:121], v[142:143] op_sel:[0,0] op_sel_hi:[0,1]\n"
    "v_pk_fma_f32 v[130:131], v[120:121], v[142:143], v[130:131] op_sel:[1,1,0] op_sel_hi:[1,0,1] neg_lo:[1,0,0]\n"
    "v_pk_mul_f32 v[134:135], v[126:127], v[142:143] op_sel:[0,0] op_sel_hi:[0,1]\n"
    "v_pk_fma_f32 v[134:135], v[126:127], v[142:143], v[134:135] op_sel:[1,1,0] op_sel_hi:[1,0,1] neg_lo:[1,0,0]\n"
    "v_pk_mul_f32 v[136:137], v[120:121], v[116:117] op_sel:[0,0] op_sel_hi:[0,1]\n"
    "v_pk_fma_f32 v[136:137], v[120:121], v[116:117], v[136:137] op_sel:[1,1,0] op_sel_hi:[1,0,1] neg_lo:[1,0,0]\n"
    "v_pk_mul_f32 v[144:145], v[126:127], v[116:117] op_sel:[0,0] op_sel_hi:[0,1]\n"
    "v_pk_fma_f32 v[144:145], v[126:127], v[116:117], v[144:145] op_sel:[1,1,0] op_sel_hi:[1,0,1] neg_lo:[1,0,0]\n"
    "v_pk_mul_f32 v[146:147], v[120:121], v[118:119] op_sel:[0,0] op_sel_hi:[0,1]\n"
    "v_pk_fma_f32 v[146:147], v[120:121], v[118:119], v[146:147] op_sel:[1,1,0] op_sel_hi:[1,0,1] neg_lo:[1,0,0]\n"
    "v_pk_mul_f32 v[148:149], v[126:127], v[118:119] op_sel:[0,0] op_sel_hi:[0,1]\n"
    "v_pk_fma_f32 v[148:149], v[126:127], v[118:119], v[148:149] op_sel:[1,1,0] op_sel_hi:[1,0,1] neg_lo:[1,0,0]\n"
    "v_pk_mul_f32 v[150:151], v[120:121], v[122:123] op_sel:[0,0] op_sel_hi:[0,1]\n"
    "v_pk_fma_f32 v[150:151], v[120:121], v[122:123], v[150:151] op_sel:[1,1,0] op_sel_hi:[1,0,1] neg_lo:[1,0,0]\n"
    "v_pk_mul_f32 v[152:153], v[126:127], v[122:123] op_sel:[0,0] op_sel_hi:[0,1]\n"
    "v_pk_fma_f32 v[152:153], v[126:127], v[122:123], v[152:153] op_sel:[1,1,0] op_sel_hi:[1,0,1] neg_lo:[1,0,0]\n"
    "v_cvt_pk_bf16_f32 v102, v130, v134\n"
    "v_cvt_pk_bf16_f32 v103, v136, v144\n"
    "v_cvt_pk_bf16_f32 v104, v146, v148\n"
    "v_cvt_pk_bf16_f32 v105, v150, v152\n"
    "ds_write_b128 v113, v[102:105] offset:16\n"
    "v_cvt_pk_bf16_f32 v106, -v131, -v135\n"
    "v_cvt_pk_bf16_f32 v107, -v137, -v145\n"
    "v_cvt_pk_bf16_f32 v108, -v147, -v149\n"
    "v_cvt_pk_bf16_f32 v109, -v151, -v153\n"
    "ds_write_b128 v113, v[106:109] offset:16400\n"
    "s_cmp_eq_u32 s7, 0\n"
    "s_cbranch_scc0 Lbwe_14_%=\n"
    "s_and_b32 s55, s6, 7\n"
    "s_lshl_b32 s55, s55, 11\n"
    "s_add_u32 s55, s55, 0x1800\n"
    "s_mov_b32 s76, 0\n"
    "s_mov_b32 s77, 1\n"
    "s_mov_b64 s[60:61], exec\n"
    "v_and_b32 v27, 7, v8\n"
    "v_lshlrev_b32 v27, 8, v27\n"
    "s_mov_b64 exec, 0xff\n"
    "s_nop 1\n"
    "buffer_load_dword v188, v27, s[20:23], s55 offen sc1\n"
    "s_sleep 1\n"
    "buffer_load_dword v189, v27, s[20:23], s55 offen sc1\n"
    "s_sleep 1\n"
    "buffer_load_dword v190, v27, s[20:23], s55 offen sc1\n"
    "s_sleep 1\n"
    "Lbwp_12_%=:\n"
    "buffer_load_dword v191, v27, s[20:23], s55 offen sc1\n"
    "s_waitcnt vmcnt(3)\n"
    "v_cmp_le_u32 vcc, s77, v188\n"
    "s_nop 1\n"
    "s_cmp_eq_u64 vcc, exec\n"
    "s_cbranch_scc1 Lbwx_13_%=\n"
    "buffer_load_dword v188, v27, s[20:23], s55 offen sc1\n"
    "s_waitcnt vmcnt(3)\n"
    "v_cmp_le_u32 vcc, s77, v189\n"
    "s_nop 1\n"
    "s_cmp_eq_u64 vcc, exec\n"
    "s_cbranch_scc1 Lbwx_13_%=\n"
    "buffer_load_dword v189, v27, s[20:23], s55 offen sc1\n"
    "s_waitcnt vmcnt(3)\n"
    "v_cmp_le_u32 vcc, s77, v190\n"
    "s_nop 1\n"
    "s_cmp_eq_u64 vcc, exec\n"
    "s_cbranch_scc1 Lbwx_13_%=\n"
    "buffer_load_dword v190, v27, s[20:23], s55 offen sc1\n"
    "s_waitcnt vmcnt(3)\n"
    "v_cmp_le_u32 vcc, s77, v191\n"
    "s_nop 1\n"
    "s_cmp_eq_u64 vcc, exec\n"
    "s_cbranch_scc1 Lbwx_13_%=\n"
    "s_add_u32 s76, s76, 1\n"
    "s_cmp_lt_u32 s76, 0x10000\n"
    "s_cbranch_scc1 Lbwp_12_%=\n"
    "Lbwx_13_%=:\n"
    "s_mov_b64 exec, s[60:61]\n"
    "s_nop 0\n"
    "s_branch Lbw_11_%=\n"
    "Lbwe_14_%=:\n"
    "s_mov_b64 s[60:61], exec\n"
    "s_mov_b64 exec, 0\n"
    "s_cmp_eq_u32 s7, 2\n"
    "s_cbranch_scc1 Ltw1_2_%=\n"
    "s_cmp_eq_u32 s7, 3\n"
    "s_cbranch_scc1 Ltw1_3_%=\n"
    "s_branch Lpg3_%=\n"
    "Ltb1_1_0_%=:\n"
    "s_branch Lpg6_%=\n"
    "Ltb1_1_1_%=:\n"
    "s_branch Ltf1_%=\n"
    "Ltw1_2_%=:\n"
    "s_branch Lpg4_%=\n"
    "Ltb1_2_0_%=:\n"
    "s_branch Lpg7_%=\n"
    "Ltb1_2_1_%=:\n"
    "s_branch Ltf1_%=\n"
    "Ltw1_3_%=:\n"
    "s_branch Lpg5_%=\n"
    "Ltb1_3_0_%=:\n"
    "s_nop 0\n"
    "Ltb1_3_1_%=:\n"
    "Ltf1_%=:\n"
    "s_mov_b64 exec, s[60:61]\n"
    "Lbw_11_%=:\n"
    "s_waitcnt lgkmcnt(0)\n"
    "s_barrier\n"
    "s_lshl_b32 s57, s7, 13\n"
    "s_add_u32 s57, s57, 0x10000\n"
    "s_mov_b32 s56, 0x10000\n"
    "buffer_load_dwordx4 v[56:59], v41, s[20:23], s56 offen sc1\n"
    "buffer_load_dwordx4 v[60:63], v29, s[20:23], s57 offen offset:0 sc1\n"
    "s_lshl_b32 s58, s24, 15\n"
    "s_lshl_b32 s55, s7, 13\n"
    "s_add_u32 s58, s58, s55\n"
    "s_add_u32 s58, s58, 0x100000\n"
    "buffer_load_dwordx4 v[64:67], v38, s[20:23], s58 offen offset:0 sc1\n"
    "buffer_load_dwordx4 v[68:71], v29, s[20:23], s57 offen offset:1024 sc1\n"
    "buffer_load_dwordx4 v[72:75], v38, s[20:23], s58 offen offset:1024 sc1\n"
    "buffer_load_dwordx4 v[76:79], v29, s[20:23], s57 offen offset:2048 sc1\n"
    "s_waitcnt vmcnt(14)\n"
    "ds_read_b64_tr_b16 a[200:201], v46 offset:16384\n"
    "ds_read_b64_tr_b16 a[202:203], v46 offset:16896\n"
    "ds_read_b64_tr_b16 v[200:201], v49 offset:16384\n"
    "ds_read_b64_tr_b16 v[202:203], v49 offset:16896\n"
    "ds_read_b64_tr_b16 a[204:205], v46 offset:18432\n"
    "ds_read_b64_tr_b16 a[206:207], v46 offset:18944\n"
    "ds_read_b64_tr_b16 v[204:205], v49 offset:18432\n"
    "ds_read_b64_tr_b16 v[206:207], v49 offset:18944\n"
    "s_waitcnt vmcnt(10)\n"
    "ds_read_b64_tr_b16 a[216:217], v47 offset:0\n"
    "v_mov_b32 v40, 0x7ffffff0\n"
    "ds_read_b64_tr_b16 a[218:219], v47 offset:512\n"
    "v_cmp_eq_u32 vcc, 0, v0\n"
    "ds_read_b64_tr_b16 v[216:217], v48 offset:0\n"
    "ds_read_b64_tr_b16 v[218:219], v48 offset:512\n"
    "buffer_load_dwordx4 v[80:83], v38, s[20:23], s58 offen offset:2048 sc1\n"
    "ds_read_b64_tr_b16 a[220:221], v47 offset:2048\n"
    "v_cndmask_b32 v40, v40, 0, vcc\n"
    "ds_read_b64_tr_b16 a[222:223], v47 offset:2560\n"
    "ds_read_b64_tr_b16 v[220:221], v48 offset:2048\n"
    "v_pk_mul_f32 v[116:117], v[120:121], v[124:125] op_sel:[0,0] op_sel_hi:[0,1]\n"
    "ds_read_b64_tr_b16 v[222:223], v48 offset:2560\n"
    "s_waitcnt vmcnt(7)\n"
    "ds_read_b64_tr_b16 a[232:233], v46 offset:0\n"
    "v_pk_fma_f32 v[116:117], v[120:121], v[124:125], v[116:117] op_sel:[1,1,0] op_sel_hi:[1,0,1] neg_lo:[1,0,0]\n"
    "ds_read_b64_tr_b16 a[234:235], v46 offset:512\n"
    "v_pk_mul_f32 v[118:119], v[116:117], v[132:133] op_sel:[0,0] op_sel_hi:[0,1]\n"
    "ds_read_b64_tr_b16 v[232:233], v49 offset:0\n"
    "ds_read_b64_tr_b16 v[234:235], v49 offset:512\n"
    "v_pk_fma_f32 v[118:119], v[116:117], v[132:133], v[118:119] op_sel:[1,1,0] op_sel_hi:[1,0,1] neg_lo:[1,0,0]\n"
    "ds_read_b64_tr_b16 a[236:237], v46 offset:2048\n"
    "ds_read_b64_tr_b16 a[238:239], v46 offset:2560\n"
    "buffer_load_dwordx4 v[84:87], v29, s[20:23], s57 offen offset:3072 sc1\n"
    "s_add_u32 s57, s57, 0x1000\n"
    "ds_read_b64_tr_b16 v[236:237], v49 offset:2048\n"
    "ds_read_b64_tr_b16 v[238:239], v49 offset:2560\n"
    "v_pk_mul_f32 v[122:123], v[118:119], v[132:133] op_sel:[0,0] op_sel_hi:[0,1]\n"
    "v_pk_fma_f32 v[122:123], v[118:119], v[132:133], v[122:123] op_sel:[1,1,0] op_sel_hi:[1,0,1] neg_lo:[1,0,0]\n"
    "v_pk_mul_f32 v[126:127], v[122:123], v[132:133] op_sel:[0,0] op_sel_hi:[0,1]\n"
    "v_pk_fma_f32 v[126:127], v[122:123], v[132:133], v[126:127] op_sel:[1,1,0] op_sel_hi:[1,0,1] neg_lo:[1,0,0]\n"
    "v_pk_mul_f32 v[130:131], v[126:127], v[132:133] op_sel:[0,0] op_sel_hi:[0,1]\n"
    "v_pk_fma_f32 v[130:131], v[126:127], v[132:133], v[130:131] op_sel:[1,1,0] op_sel_hi:[1,0,1] neg_lo:[1,0,0]\n"
    "v_pk_mul_f32 v[134:135], v[130:131], v[132:133] op_sel:[0,0] op_sel_hi:[0,1]\n"
    "v_pk_fma_f32 v[134:135], v[130:131], v[132:133], v[134:135] op_sel:[1,1,0] op_sel_hi:[1,0,1] neg_lo:[1,0,0]\n"
    "v_pk_mul_f32 v[136:137], v[134:135], v[132:133] op_sel:[0,0] op_sel_hi:[0,1]\n"
    "v_pk_fma_f32 v[136:137], v[134:135], v[132:133], v[136:137] op_sel:[1,1,0] op_sel_hi:[1,0,1] neg_lo:[1,0,0]\n"
    "v_pk_mul_f32 v[138:139], v[136:137], v[132:133] op_sel:[0,0] op_sel_hi:[0,1]\n"
    "v_pk_fma_f32 v[138:139], v[136:137], v[132:133], v[138:139] op_sel:[1,1,0] op_sel_hi:[1,0,1] neg_lo:[1,0,0]\n"
    "buffer_load_dwordx4 v[88:91], v38, s[20:23], s58 offen offset:3072 sc1\n"
    "s_add_u32 s58, s58, 0x1000\n"
    "s_add_u32 s55, s44, 8\n"
    "s_cmp_eq_u32 s6, 0\n"
    "s_cselect_b32 s56, 0, 0x40000000\n"
    "s_lshl_b32 s59, s66, 1\n"
    "s_add_u32 s59, s59, 7\n"
    "s_and_b32 s59, s59, 7\n"
    "s_lshl_b32 s59, s59, 16\n"
    "s_add_u32 s59, s59, s101\n"
    "s_waitcnt lgkmcnt(15)\n"
    "s_add_u32 m0, s51, 0x14000\n"
    "s_lshl_b32 s78, s6, 13\n"
    "buffer_load_dwordx4 v42, s[16:19], s59 offen offset:0 lds\n"
    "buffer_load_dwordx4 v43, s[16:19], s59 offen offset:1024 lds\n"
    "buffer_load_dwordx4 v44, s[16:19], s59 offen offset:2048 lds\n"
    "buffer_load_dwordx4 v45, s[16:19], s59 offen offset:3072 lds\n"
    "v_mfma_f32_16x16x32_bf16 v[42:45], v[30:33], v[18:21], 0\n"
    "s_add_u32 s78, s78, 0x900000\n"
    "s_add_u32 s79, s78, 0x1000\n"
    "v_mov_b32 v39, s55\n"
    "v_pk_mul_f32 v[140:141], v[138:139], v[132:133] op_sel:[0,0] op_sel_hi:[0,1]\n"
    "v_pk_fma_f32 v[140:141], v[138:139], v[132:133], v[140:141] op_sel:[1,1,0] op_sel_hi:[1,0,1] neg_lo:[1,0,0]\n"
    "v_cvt_pk_bf16_f32 v102, v116, v118\n"
    "v_cvt_pk_bf16_f32 v103, v122, v126\n"
    "v_cvt_pk_bf16_f32 v104, v130, v134\n"
    "v_cvt_pk_bf16_f32 v105, v136, v138\n"
    "ds_write_b128 v114, v[102:105] offset:0\n"
    "v_cvt_pk_bf16_f32 v106, -v117, -v119\n"
    "v_cvt_pk_bf16_f32 v107, -v123, -v127\n"
    "v_cvt_pk_bf16_f32 v108, -v131, -v135\n"
    "v_cvt_pk_bf16_f32 v109, -v137, -v139\n"
    "v_cvt_pk_bf16_f32 v102, v140, 0\n"
    "ds_write_b128 v114, v[106:109] offset:16384\n"
    "v_mov_b32 v103, 0\n"
    "v_mov_b32 v104, 0\n"
    "v_mov_b32 v105, 0\n"
    "v_cvt_pk_bf16_f32 v106, -v141, 0\n"
    "v_mfma_f32_16x16x32_bf16 v[138:141], v[34:37], v[22:25], 0\n"
    "v_mov_b32 v107, 0\n"
    "v_mov_b32 v108, 0\n"
    "v_mov_b32 v109, 0\n"
    "v_pk_mul_f32 v[116:117], v[128:129], v[128:129] op_sel:[0,0] op_sel_hi:[0,1]\n"
    "v_pk_fma_f32 v[116:117], v[128:129], v[128:129], v[116:117] op_sel:[1,1,0] op_sel_hi:[1,0,1] neg_lo:[1,0,0]\n"
    "ds_write_b128 v114, v[102:105] offset:16\n"
    "v_pk_mul_f32 v[118:119], v[116:117], v[128:129] op_sel:[0,0] op_sel_hi:[0,1]\n"
    "v_pk_fma_f32 v[118:119], v[116:117], v[128:129], v[118:119] op_sel:[1,1,0] op_sel_hi:[1,0,1] neg_lo:[1,0,0]\n"
    "v_pk_mul_f32 v[120:121], v[118:119], v[128:129] op_sel:[0,0] op_sel_hi:[0,1]\n"
    "v_pk_fma_f32 v[120:121], v[118:119], v[128:129], v[120:121] op_sel:[1,1,0] op_sel_hi:[1,0,1] neg_lo:[1,0,0]\n"
    "v_pk_mul_f32 v[122:123], v[120:121], v[128:129] op_sel:[0,0] op_sel_hi:[0,1]\n"
    "v_pk_fma_f32 v[122:123], v[120:121], v[128:129], v[122:123] op_sel:[1,1,0] op_sel_hi:[1,0,1] neg_lo:[1,0,0]\n"
    "v_pk_mul_f32 v[124:125], v[122:123], v[128:129] op_sel:[0,0] op_sel_hi:[0,1]\n"
    "v_pk_fma_f32 v[124:125], v[122:123], v[128:129], v[124:125] op_sel:[1,1,0] op_sel_hi:[1,0,1] neg_lo:[1,0,0]\n"
    "v_pk_mul_f32 v[126:127], v[124:125], v[128:129] op_sel:[0,0] op_sel_hi:[0,1]\n"
    "ds_write_b128 v114, v[106:109] offset:16400\n"
    "v_pk_fma_f32 v[126:127], v[124:125], v[128:129], v[126:127] op_sel:[1,1,0] op_sel_hi:[1,0,1] neg_lo:[1,0,0]\n"
    "v_cvt_pk_bf16_f32 v102, 1.0, v128\n"
    "v_cvt_pk_bf16_f32 v103, v116, v118\n"
    "v_cvt_pk_bf16_f32 v104, v120, v122\n"
    "v_cvt_pk_bf16_f32 v105, v124, v126\n"
    "v_cvt_pk_bf16_f32 v106, 0, v129\n"
    "v_cvt_pk_bf16_f32 v107, v117, v119\n"
    "v_cvt_pk_bf16_f32 v108, v121, v123\n"
    "v_cvt_pk_bf16_f32 v109, v125, v127\n"
    "v_pk_mul_f32 v[116:117], v[126:127], v[128:129] op_sel:[0,0] op_sel_hi:[0,1]\n"
    "ds_write_b128 v115, v[102:105] offset:0\n"
    "v_pk_fma_f32 v[116:117], v[126:127], v[128:129], v[116:117] op_sel:[1,1,0] op_sel_hi:[1,0,1] neg_lo:[1,0,0]\n"
    "v_pk_mul_f32 v[118:119], v[116:117], v[128:129] op_sel:[0,0] op_sel_hi:[0,1]\n"
    "v_pk_fma_f32 v[118:119], v[116:117], v[128:129], v[118:119] op_sel:[1,1,0] op_sel_hi:[1,0,1] neg_lo:[1,0,0]\n"
    "v_pk_mul_f32 v[120:121], v[118:119], v[128:129] op_sel:[0,0] op_sel_hi:[0,1]\n"
    "v_pk_fma_f32 v[120:121], v[118:119], v[128:129], v[120:121] op_sel:[1,1,0] op_sel_hi:[1,0,1] neg_lo:[1,0,0]\n"
    "v_pk_mul_f32 v[122:123], v[120:121], v[128:129] op_sel:[0,0] op_sel_hi:[0,1]\n"
    "Lpg3_%=:\n"
    "s_cbranch_execz Ltb1_1_0_%=\n"
    "v_pk_fma_f32 v[122:123], v[120:121], v[128:129], v[122:123] op_sel:[1,1,0] op_sel_hi:[1,0,1] neg_lo:[1,0,0]\n"
    "v_pk_mul_f32 v[124:125], v[122:123], v[128:129] op_sel:[0,0] op_sel_hi:[0,1]\n"
    "v_pk_fma_f32 v[124:125], v[122:123], v[128:129], v[124:125] op_sel:[1,1,0] op_sel_hi:[1,0,1] neg_lo:[1,0,0]\n"
    "ds_write_b128 v115, v[106:109] offset:16384\n"
    "v_pk_mul_f32 v[130:131], v[124:125], v[128:129] op_sel:[0,0] op_sel_hi:[0,1]\n"
    "v_pk_fma_f32 v[130:131], v[124:125], v[128:129], v[130:131] op_sel:[1,1,0] op_sel_hi:[1,0,1] neg_lo:[1,0,0]\n"
    "v_pk_mul_f32 v[132:133], v[130:131], v[128:129] op_sel:[0,0] op_sel_hi:[0,1]\n"
    "v_pk_fma_f32 v[132:133], v[130:131], v[128:129], v[132:133] op_sel:[1,1,0] op_sel_hi:[1,0,1] neg_lo:[1,0,0]\n"
    "v_pk_mul_f32 v[134:135], v[132:133], v[128:129] op_sel:[0,0] op_sel_hi:[0,1]\n"
    "v_pk_fma_f32 v[134:135], v[132:133], v[128:129], v[134:135] op_sel:[1,1,0] op_sel_hi:[1,0,1] neg_lo:[1,0,0]\n"
    "buffer_load_dwordx4 v[92:95], v29, s[20:23], s57 offen offset:0 sc1\n"
    "v_cvt_pk_bf16_f32 v102, v116, v118\n"
    "v_cvt_pk_bf16_f32 v103, v120, v122\n"
    "v_cvt_pk_bf16_f32 v104, v124, v130\n"
    "v_cvt_pk_bf16_f32 v105, v132, v134\n"
    "ds_write_b128 v115, v[102:105] offset:16\n"
    "v_cvt_pk_bf16_f32 v106, v117, v119\n"
    "v_cvt_pk_bf16_f32 v107, v121, v123\n"
    "v_cvt_pk_bf16_f32 v108, v125, v131\n"
    "v_cvt_pk_bf16_f32 v109, v133, v135\n"
    "v_mfma_f32_16x16x32_bf16 v[134:137], v[34:37], v[18:21], 0\n"
    "s_waitcnt vmcnt(13)\n"
    "v_cvt_pk_bf16_f32 v100, v56, v57\n"
    "v_cvt_pk_bf16_f32 v101, v58, v59\n"
    "ds_write_b128 v115, v[106:109] offset:16400\n"
    "v_mov_b32 v41, -1\n"
    "v_lshlrev_b32 v16, 4, v0\n"
    "buffer_load_dwordx4 v[96:99], v38, s[20:23], s58 offen offset:0 sc1\n"
    "s_waitcnt vmcnt(13)\n"
    "v_cvt_pk_bf16_f32 v142, v60, v61\n"
    "v_cvt_pk_bf16_f32 v143, v62, v63\n"
    "v_mov_b32_dpp v144, v100 row_newbcast:0 row_mask:0xf bank_mask:0xf\n"
    "v_mov_b32_dpp v145, v101 row_newbcast:0 row_mask:0xf bank_mask:0xf\n"
    "v_xor_b32 v26, 0, v50\n"
    "v_permlane16_swap_b32 v142, v144\n"
    "v_permlane16_swap_b32 v143, v145\n"
    "s_waitcnt vmcnt(11)\n"
    "v_cvt_pk_bf16_f32 v150, v68, v69\n"
    "v_mfma_f32_32x32x16_bf16 v[102:117], v[142:145], v[64:67], 0\n"
    "v_cvt_pk_bf16_f32 v151, v70, v71\n"
    "v_mov_b32_dpp v152, v100 row_newbcast:1 row_mask:0xf bank_mask:0xf\n"
    "v_mov_b32_dpp v153, v101 row_newbcast:1 row_mask:0xf bank_mask:0xf\n"
    "v_xor_b32 v154, 32, v50\n"
    "v_permlane16_swap_b32 v150, v152\n"
    "buffer_load_dwordx4 v[166:169], v29, s[20:23], s57 offen offset:1024 sc1\n"
    "v_permlane16_swap_b32 v151, v153\n"
    "v_xor_b32 v156, 64, v50\n"
    "s_waitcnt vmcnt(11)\n"
    "v_mfma_f32_32x32x16_bf16 v[118:133], v[150:153], v[72:75], 0\n"
    "s_nop 1\n"
    "v_add_f32 v17, s72, v110\n"
    "v_rcp_f32 v17, v17\n"
    "s_nop 0\n"
    "v_mul_f32 v102, v17, v102\n"
    "v_mul_f32 v103, v17, v103\n"
    "v_mul_f32 v104, v17, v104\n"
    "v_mul_f32 v105, v17, v105\n"
    "v_mul_f32 v106, v17, v106\n"
    "v_mul_f32 v107, v17, v107\n"
    "v_mul_f32 v108, v17, v108\n"
    "v_mul_f32 v109, v17, v109\n"
    "v_cvt_pk_bf16_f32 v146, v102, v103\n"
    "buffer_load_dwordx4 v[170:173], v38, s[20:23], s58 offen offset:1024 sc1\n"
    "v_cvt_pk_bf16_f32 v147, v104, v105\n"
    "v_cvt_pk_bf16_f32 v148, v106, v107\n"
    "v_cvt_pk_bf16_f32 v149, v108, v109\n"
    "v_add_f32 v27, s72, v126\n"
    "v_rcp_f32 v27, v27\n"
    "s_nop 0\n"
    "v_mul_f32 v118, v27, v118\n"
    "v_mul_f32 v119, v27, v119\n"
    "v_mul_f32 v120, v27, v120\n"
    "v_mul_f32 v121, v27, v121\n"
    "v_mul_f32 v122, v27, v122\n"
    "v_mul_f32 v123, v27, v123\n"
    "v_mul_f32 v124, v27, v124\n"
    "v_mul_f32 v125, v27, v125\n"
    "v_cvt_pk_bf16_f32 v56, v118, v119\n"
    "v_cvt_pk_bf16_f32 v57, v120, v121\n"
    "v_cvt_pk_bf16_f32 v58, v122, v123\n"
    "v_cvt_pk_bf16_f32 v59, v124, v125\n"
    "buffer_load_dwordx4 v[174:177], v29, s[20:23], s57 offen offset:2048 sc1\n"
    "ds_write_b128 v154, v[56:59]\n"
    "v_mov_b32_dpp v36, v100 row_newbcast:3 row_mask:0xf bank_mask:0xf\n"
    "v_mov_b32_dpp v37, v101 row_newbcast:3 row_mask:0xf bank_mask:0xf\n"
    "v_xor_b32 v162, 0x60, v50\n"
    "v_xor_b32 v164, 0x80, v50\n"
    "v_xor_b32 v187, 0xa0, v50\n"
    "v_xor_b32 v17, 0xc0, v50\n"
    "v_xor_b32 v27, 0xe0, v50\n"
    "s_waitcnt vmcnt(10)\n"
    "v_cvt_pk_bf16_f32 v34, v84, v85\n"
    "v_cvt_pk_bf16_f32 v35, v86, v87\n"
    "s_nop 0\n"
    "v_permlane16_swap_b32 v34, v36\n"
    "buffer_load_dwordx4 v[178:181], v38, s[20:23], s58 offen offset:2048 sc1\n"
    "v_permlane16_swap_b32 v35, v37\n"
    "s_waitcnt vmcnt(10)\n"
    "s_nop 0\n"
    "v_mfma_f32_32x32x16_bf16 v[118:133], v[34:37], v[88:91], 0\n"
    "buffer_load_dwordx4 v[182:185], v29, s[20:23], s57 offen offset:3072 sc1\n"
    "s_nop 10\n"
    "v_add_f32 v157, s72, v126\n"
    "v_rcp_f32 v157, v157\n"
    "s_nop 0\n"
    "v_mul_f32 v118, v157, v118\n"
    "v_mul_f32 v119, v157, v119\n"
    "v_mul_f32 v120, v157, v120\n"
    "v_mul_f32 v121, v157, v121\n"
    "v_mul_f32 v122, v157, v122\n"
    "v_mul_f32 v123, v157, v123\n"
    "v_mul_f32 v124, v157, v124\n"
    "v_mul_f32 v125, v157, v125\n"
    "v_cvt_pk_bf16_f32 v60, v118, v119\n"
    "buffer_load_dwordx4 v[158:161], v38, s[20:23], s58 offen offset:3072 sc1\n"
    "v_mov_b32 v38, -1\n"
    "v_cvt_pk_bf16_f32 v61, v120, v121\n"
    "v_cvt_pk_bf16_f32 v62, v122, v123\n"
    "v_cvt_pk_bf16_f32 v63, v124, v125\n"
    "ds_write_b128 v162, v[60:63]\n"
    "buffer_store_dword v39, v40, s[20:23], s56 offen sc1\n"
    "s_and_b32 s29, s6, 7\n"
    "s_lshl_b32 s29, s29, 8\n"
    "s_add_u32 s29, s29, 0x1000\n"
    "v_mov_b32 v39, -1\n"
    "v_mov_b32 v40, -1\n"
    "v_mov_b32 v162, 0x7ffffff0\n"
    "v_cmp_eq_u32 vcc, 0, v0\n"
    "buffer_store_dwordx4 v[38:41], v16, s[20:23], s78 offen sc1\n"
    "s_nop 0\n"
    "v_cndmask_b32 v162, v162, 0, vcc\n"
    "buffer_store_dwordx4 v[38:41], v16, s[20:23], s79 offen sc1\n"
    "ds_write_b128 v26, v[146:149]\n"
    "s_nop 0\n"
    "v_cvt_pk_bf16_f32 v38, v76, v77\n"
    "v_cvt_pk_bf16_f32 v39, v78, v79\n"
    "v_mov_b32_dpp v40, v100 row_newbcast:2 row_mask:0xf bank_mask:0xf\n"
    "v_mov_b32_dpp v41, v101 row_newbcast:2 row_mask:0xf bank_mask:0xf\n"
    "v_mov_b32_dpp v78, v100 row_newbcast:4 row_mask:0xf bank_mask:0xf\n"
    "v_permlane16_swap_b32 v38, v40\n"
    "v_permlane16_swap_b32 v39, v41\n"
    "v_mov_b32_dpp v79, v101 row_newbcast:4 row_mask:0xf bank_mask:0xf\n"
    "s_nop 0\n"
    "v_mfma_f32_32x32x16_bf16 v[102:117], v[38:41], v[80:83], 0\n"
    "v_mov_b32_dpp v82, v100 row_newbcast:5 row_mask:0xf bank_mask:0xf\n"
    "v_mov_b32_dpp v83, v101 row_newbcast:5 row_mask:0xf bank_mask:0xf\n"
    "s_waitcnt vmcnt(10)\n"
    "v_cvt_pk_bf16_f32 v76, v92, v93\n"
    "v_cvt_pk_bf16_f32 v77, v94, v95\n"
    "s_nop 6\n"
    "v_add_f32 v155, s72, v110\n"
    "v_rcp_f32 v155, v155\n"
    "s_waitcnt lgkmcnt(0)\n"
    "s_barrier\n"
    "ds_read_b128 v[142:145], v51 offset:0\n"
    "ds_read_b128 v[64:67], v51 offset:16384\n"
    "ds_read_b128 v[146:149], v51 offset:256\n"
    "v_mul_f32 v102, v155, v102\n"
    "v_mul_f32 v103, v155, v103\n"
    "v_mul_f32 v104, v155, v104\n"
    "ds_read_b128 v[68:71], v51 offset:16640\n"
    "v_mul_f32 v105, v155, v105\n"
    "v_mul_f32 v106, v155, v106\n"
    "ds_read_b128 v[150:153], v51 offset:512\n"
    "v_mul_f32 v107, v155, v107\n"
    "v_mul_f32 v108, v155, v108\n"
    "v_mul_f32 v109, v155, v109\n"
    "ds_read_b128 v[72:75], v51 offset:16896\n"
    "v_permlane16_swap_b32 v76, v78\n"
    "v_permlane16_swap_b32 v77, v79\n"
    "ds_read_b128 v[56:59], v51 offset:17152\n"
    "v_mov_b32 v188, 1\n"
    "s_waitcnt vmcnt(8)\n"
    "v_cvt_pk_bf16_f32 v80, v166, v167\n"
    "v_cvt_pk_bf16_f32 v81, v168, v169\n"
    "s_waitcnt lgkmcnt(6)\n"
    "v_mfma_f32_16x16x32_bf16 v[42:45], v[142:145], a[192:195], v[42:45]\n"
    "v_permlane16_swap_b32 v80, v82\n"
    "s_waitcnt lgkmcnt(5)\n"
    "v_mfma_f32_16x16x32_bf16 v[134:137], v[64:67], a[192:195], v[134:137]\n"
    "v_permlane16_swap_b32 v81, v83\n"
    "v_mfma_f32_16x16x32_bf16 v[138:141], v[64:67], v[192:195], v[138:141]\n"
    "s_waitcnt lgkmcnt(4)\n"
    "v_mfma_f32_16x16x32_bf16 v[42:45], v[146:149], a[208:211], v[42:45]\n"
    "ds_read_b64_tr_b16 a[248:249], v46 offset:16384\n"
    "ds_read_b64_tr_b16 a[250:251], v46 offset:16896\n"
    "s_waitcnt lgkmcnt(5)\n"
    "v_mfma_f32_16x16x32_bf16 v[134:137], v[68:71], a[208:211], v[134:137]\n"
    "ds_read_b64_tr_b16 v[248:249], v49 offset:16384\n"
    "ds_read_b64_tr_b16 v[250:251], v49 offset:16896\n"
    "v_mfma_f32_16x16x32_bf16 v[138:141], v[68:71], v[208:211], v[138:141]\n"
    "ds_read_b64_tr_b16 a[252:253], v46 offset:18432\n"
    "ds_read_b64_tr_b16 a[254:255], v46 offset:18944\n"
    "s_waitcnt lgkmcnt(8)\n"
    "v_mfma_f32_16x16x32_bf16 v[42:45], v[150:153], a[224:227], v[42:45]\n"
    "ds_read_b64_tr_b16 v[252:253], v49 offset:18432\n"
    "ds_read_b64_tr_b16 v[254:255], v49 offset:18944\n"
    "v_mfma_f32_16x16x32_bf16 v[46:49], v[30:33], v[22:25], 0\n"
    "s_add_u32 s55, s52, 0x500000\n"
    "s_lshr_b32 s56, s24, 4\n"
    "v_mfma_f32_16x16x32_bf16 v[46:49], v[142:145], v[192:195], v[46:49]\n"
    "s_lshl_b32 s56, s56, 12\n"
    "v_cvt_pk_bf16_f32 v30, v102, v103\n"
    "v_mfma_f32_16x16x32_bf16 v[46:49], v[146:149], v[208:211], v[46:49]\n"
    "v_cvt_pk_bf16_f32 v31, v104, v105\n"
    "v_cvt_pk_bf16_f32 v32, v106, v107\n"
    "v_mfma_f32_16x16x32_bf16 v[46:49], v[150:153], v[224:227], v[46:49]\n"
    "v_cvt_pk_bf16_f32 v33, v108, v109\n"
    "ds_write_b128 v156, v[30:33]\n"
    "s_waitcnt lgkmcnt(10)\n"
    "v_mfma_f32_16x16x32_bf16 v[134:137], v[72:75], a[224:227], v[134:137]\n"
    "s_waitcnt vmcnt(6)\n"
    "v_cvt_pk_bf16_f32 v150, v174, v175\n"
    "v_cvt_pk_bf16_f32 v151, v176, v177\n"
    "v_mfma_f32_16x16x32_bf16 v[138:141], v[72:75], v[224:227], v[138:141]\n"
    "ds_read_b128 v[154:157], v51 offset:768\n"
    "v_mov_b32_dpp v152, v100 row_newbcast:6 row_mask:0xf bank_mask:0xf\n"
    "s_waitcnt lgkmcnt(10)\n"
    "v_mfma_f32_16x16x32_bf16 v[134:137], v[56:59], a[240:243], v[134:137]\n"
    "v_mov_b32_dpp v153, v101 row_newbcast:6 row_mask:0xf bank_mask:0xf\n"
    "v_permlane16_swap_b32 v150, v152\n"
    "v_mfma_f32_16x16x32_bf16 v[138:141], v[56:59], v[240:243], v[138:141]\n"
    "v_permlane16_swap_b32 v151, v153\n"
    "v_mfma_f32_32x32x16_bf16 v[102:117], v[76:79], v[96:99], 0\n"
    "v_mfma_f32_32x32x16_bf16 v[118:133], v[80:83], v[170:173], 0\n"
    "s_waitcnt lgkmcnt(1)\n"
    "s_barrier\n"
    "ds_read_b128 v[84:87], v52 offset:0\n"
    "ds_read_b128 v[34:37], v52 offset:16384\n"
    "s_waitcnt lgkmcnt(2)\n"
    "v_mfma_f32_16x16x32_bf16 v[42:45], v[154:157], a[240:243], v[42:45]\n"
    "ds_read_b128 v[88:91], v52 offset:256\n"
    "s_nop 3\n"
    "v_add_f32 v163, s72, v110\n"
    "v_mfma_f32_16x16x32_bf16 v[46:49], v[154:157], v[240:243], v[46:49]\n"
    "ds_read_b128 v[60:63], v52 offset:16640\n"
    "ds_read_b128 v[142:145], v52 offset:512\n"
    "v_rcp_f32 v163, v163\n"
    "s_nop 0\n"
    "v_mul_f32 v102, v163, v102\n"
    "ds_read_b128 v[64:67], v52 offset:16896\n"
    "v_mul_f32 v103, v163, v103\n"
    "v_mul_f32 v104, v163, v104\n"
    "v_mul_f32 v105, v163, v105\n"
    "ds_read_b128 v[146:149], v52 offset:768\n"
    "v_mul_f32 v106, v163, v106\n"
    "v_mul_f32 v107, v163, v107\n"
    "v_mul_f32 v108, v163, v108\n"
    "ds_read_b128 v[68:71], v52 offset:17152\n"
    "v_mul_f32 v109, v163, v109\n"
    "v_cvt_pk_bf16_f32 v38, v102, v103\n"
    "v_cvt_pk_bf16_f32 v39, v104, v105\n"
    "s_waitcnt lgkmcnt(7)\n"
    "v_mfma_f32_16x16x32_bf16 v[42:45], v[84:87], a[196:199], v[42:45]\n"
    "v_cvt_pk_bf16_f32 v40, v106, v107\n"
    "v_cvt_pk_bf16_f32 v41, v108, v109\n"
    "v_mfma_f32_16x16x32_bf16 v[46:49], v[84:87], v[196:199], v[46:49]\n"
    "ds_write_b128 v164, v[38:41]\n"
    "v_add_f32 v186, s72, v126\n"
    "s_waitcnt lgkmcnt(7)\n"
    "v_mfma_f32_16x16x32_bf16 v[134:137], v[34:37], a[196:199], v[134:137]\n"
    "v_rcp_f32 v186, v186\n"
    "v_mfma_f32_16x16x32_bf16 v[138:141], v[34:37], v[196:199], v[138:141]\n"
    "v_mul_f32 v118, v186, v118\n"
    "v_mul_f32 v119, v186, v119\n"
    "s_waitcnt lgkmcnt(6)\n"
    "v_mfma_f32_16x16x32_bf16 v[42:45], v[88:91], a[212:215], v[42:45]\n"
    "v_mul_f32 v120, v186, v120\n"
    "v_mul_f32 v121, v186, v121\n"
    "v_mfma_f32_16x16x32_bf16 v[46:49], v[88:91], v[212:215], v[46:49]\n"
    "v_mul_f32 v122, v186, v122\n"
    "v_mul_f32 v123, v186, v123\n"
    "s_waitcnt lgkmcnt(5)\n"
    "v_mfma_f32_16x16x32_bf16 v[134:137], v[60:63], a[212:215], v[134:137]\n"
    "v_mul_f32 v124, v186, v124\n"
    "v_mul_f32 v125, v186, v125\n"
    "v_mfma_f32_16x16x32_bf16 v[138:141], v[60:63], v[212:215], v[138:141]\n"
    "v_cvt_pk_bf16_f32 v30, v118, v119\n"
    "v_cvt_pk_bf16_f32 v31, v120, v121\n"
    "s_waitcnt lgkmcnt(4)\n"
    "v_mfma_f32_16x16x32_bf16 v[42:45], v[142:145], a[228:231], v[42:45]\n"
    "v_cvt_pk_bf16_f32 v32, v122, v123\n"
    "v_cvt_pk_bf16_f32 v33, v124, v125\n"
    "v_mfma_f32_16x16x32_bf16 v[46:49], v[142:145], v[228:231], v[46:49]\n"
    "ds_write_b128 v187, v[30:33]\n"
    "s_waitcnt vmcnt(4)\n"
    "v_cvt_pk_bf16_f32 v154, v182, v183\n"
    "s_waitcnt lgkmcnt(4)\n"
    "v_mfma_f32_16x16x32_bf16 v[134:137], v[64:67], a[228:231], v[134:137]\n"
    "v_cvt_pk_bf16_f32 v155, v184, v185\n"
    "v_mov_b32_dpp v156, v100 row_newbcast:7 row_mask:0xf bank_mask:0xf\n"
    "v_mfma_f32_16x16x32_bf16 v[138:141], v[64:67], v[228:231], v[138:141]\n"
    "v_mov_b32_dpp v157, v101 row_newbcast:7 row_mask:0xf bank_mask:0xf\n"
    "v_permlane16_swap_b32 v154, v156\n"
    "s_waitcnt lgkmcnt(3)\n"
    "v_mfma_f32_16x16x32_bf16 v[42:45], v[146:149], a[244:247], v[42:45]\n"
    "v_permlane16_swap_b32 v155, v157\n"
    "v_mfma_f32_16x16x32_bf16 v[46:49], v[146:149], v[244:247], v[46:49]\n"
    "s_waitcnt lgkmcnt(2)\n"
    "v_mfma_f32_16x16x32_bf16 v[134:137], v[68:71], a[244:247], v[134:137]\n"
    "v_mfma_f32_16x16x32_bf16 v[138:141], v[68:71], v[244:247], v[138:141]\n"
    "s_waitcnt lgkmcnt(0)\n"
    "s_barrier\n"
    "v_mfma_f32_32x32x16_bf16 v[102:117], v[150:153], v[178:181], 0\n"
    "ds_read_b128 v[92:95], v53 offset:0\n"
    "ds_read_b128 v[76:79], v53 offset:16384\n"
    "ds_read_b128 v[96:99], v53 offset:256\n"
    "s_waitcnt vmcnt(3)\n"
    "v_mfma_f32_32x32x16_bf16 v[118:133], v[154:157], v[158:161], 0\n"
    "ds_read_b128 v[38:41], v53 offset:16640\n"
    "ds_read_b128 v[166:169], v53 offset:512\n"
    "s_nop 4\n"
    "v_add_f32 v16, s72, v110\n"
    "v_rcp_f32 v16, v16\n"
    "ds_read_b128 v[80:83], v53 offset:16896\n"
    "v_mul_f32 v102, v16, v102\n"
    "v_mul_f32 v103, v16, v103\n"
    "v_mul_f32 v104, v16, v104\n"
    "ds_read_b128 v[170:173], v53 offset:768\n"
    "v_mul_f32 v105, v16, v105\n"
    "v_mul_f32 v106, v16, v106\n"
    "v_mul_f32 v107, v16, v107\n"
    "ds_read_b128 v[30:33], v53 offset:17152\n"
    "v_mul_f32 v108, v16, v108\n"
    "v_mul_f32 v109, v16, v109\n"
    "v_cvt_pk_bf16_f32 v72, v102, v103\n"
    "v_cvt_pk_bf16_f32 v73, v104, v105\n"
    "s_waitcnt lgkmcnt(7)\n"
    "v_mfma_f32_16x16x32_bf16 v[42:45], v[92:95], a[200:203], v[42:45]\n"
    "v_cvt_pk_bf16_f32 v74, v106, v107\n"
    "v_cvt_pk_bf16_f32 v75, v108, v109\n"
    "v_mfma_f32_16x16x32_bf16 v[46:49], v[92:95], v[200:203], v[46:49]\n"
    "ds_write_b128 v17, v[72:75]\n"
    "v_add_f32 v26, s72, v126\n"
    "s_waitcnt lgkmcnt(7)\n"
    "v_mfma_f32_16x16x32_bf16 v[134:137], v[76:79], a[200:203], v[134:137]\n"
    "v_rcp_f32 v26, v26\n"
    "v_mfma_f32_16x16x32_bf16 v[138:141], v[76:79], v[200:203], v[138:141]\n"
    "v_mul_f32 v118, v26, v118\n"
    "v_mul_f32 v119, v26, v119\n"
    "s_waitcnt lgkmcnt(6)\n"
    "v_mfma_f32_16x16x32_bf16 v[42:45], v[96:99], a[216:219], v[42:45]\n"
    "v_mul_f32 v120, v26, v120\n"
    "v_mul_f32 v121, v26, v121\n"
    "v_mfma_f32_16x16x32_bf16 v[46:49], v[96:99], v[216:219], v[46:49]\n"
    "v_mul_f32 v122, v26, v122\n"
    "v_mul_f32 v123, v26, v123\n"
    "s_waitcnt lgkmcnt(5)\n"
    "v_mfma_f32_16x16x32_bf16 v[134:137], v[38:41], a[216:219], v[134:137]\n"
    "v_mul_f32 v124, v26, v124\n"
    "v_mul_f32 v125, v26, v125\n"
    "v_mfma_f32_16x16x32_bf16 v[138:141], v[38:41], v[216:219], v[138:141]\n"
    "v_cvt_pk_bf16_f32 v56, v118, v119\n"
    "v_cvt_pk_bf16_f32 v57, v120, v121\n"
    "s_waitcnt lgkmcnt(4)\n"
    "v_mfma_f32_16x16x32_bf16 v[42:45], v[166:169], a[232:235], v[42:45]\n"
    "v_cvt_pk_bf16_f32 v58, v122, v123\n"
    "v_cvt_pk_bf16_f32 v59, v124, v125\n"
    "v_mfma_f32_16x16x32_bf16 v[46:49], v[166:169], v[232:235], v[46:49]\n"
    "ds_write_b128 v27, v[56:59]\n"
    "v_mov_b32 v58, 0x7f800000\n"
    "s_waitcnt lgkmcnt(4)\n"
    "v_mfma_f32_16x16x32_bf16 v[134:137], v[80:83], a[232:235], v[134:137]\n"
    "v_mfma_f32_16x16x32_bf16 v[138:141], v[80:83], v[232:235], v[138:141]\n"
    "s_waitcnt lgkmcnt(3)\n"
    "v_mfma_f32_16x16x32_bf16 v[42:45], v[170:173], a[248:251], v[42:45]\n"
    "v_mfma_f32_16x16x32_bf16 v[46:49], v[170:173], v[248:251], v[46:49]\n"
    "s_waitcnt lgkmcnt(2)\n"
    "v_mfma_f32_16x16x32_bf16 v[134:137], v[30:33], a[248:251], v[134:137]\n"
    "v_mfma_f32_16x16x32_bf16 v[138:141], v[30:33], v[248:251], v[138:141]\n"
    "s_waitcnt vmcnt(0) lgkmcnt(0)\n"
    "s_barrier\n"
    "buffer_atomic_add v188, v162, s[20:23], s29 offen sc0\n"
    "ds_read_b128 v[84:87], v54 offset:0\n"
    "ds_read_b128 v[34:37], v54 offset:16384\n"
    "ds_read_b128 v[88:91], v54 offset:256\n"
    "ds_read_b128 v[60:63], v54 offset:16640\n"
    "ds_read_b128 v[142:145], v54 offset:512\n"
    "ds_read_b128 v[64:67], v54 offset:16896\n"
    "ds_read_b128 v[146:149], v54 offset:768\n"
    "ds_read_b128 v[68:71], v54 offset:17152\n"
    "s_waitcnt lgkmcnt(7)\n"
    "v_mfma_f32_16x16x32_bf16 v[42:45], v[84:87], a[204:207], v[42:45]\n"
    "v_mfma_f32_16x16x32_bf16 v[46:49], v[84:87], v[204:207], v[46:49]\n"
    "s_waitcnt lgkmcnt(6)\n"
    "v_mfma_f32_16x16x32_bf16 v[134:137], v[34:37], a[204:207], v[134:137]\n"
    "v_mfma_f32_16x16x32_bf16 v[138:141], v[34:37], v[204:207], v[138:141]\n"
    "s_waitcnt lgkmcnt(5)\n"
    "v_mfma_f32_16x16x32_bf16 v[42:45], v[88:91], a[220:223], v[42:45]\n"
    "v_mfma_f32_16x16x32_bf16 v[46:49], v[88:91], v[220:223], v[46:49]\n"
    "s_waitcnt lgkmcnt(4)\n"
    "v_mfma_f32_16x16x32_bf16 v[134:137], v[60:63], a[220:223], v[134:137]\n"
    "v_mfma_f32_16x16x32_bf16 v[138:141], v[60:63], v[220:223], v[138:141]\n"
    "s_waitcnt lgkmcnt(3)\n"
    "v_mfma_f32_16x16x32_bf16 v[42:45], v[142:145], a[236:239], v[42:45]\n"
    "v_mfma_f32_16x16x32_bf16 v[46:49], v[142:145], v[236:239], v[46:49]\n"
    "s_waitcnt lgkmcnt(2)\n"
    "v_mfma_f32_16x16x32_bf16 v[134:137], v[64:67], a[236:239], v[134:137]\n"
    "v_mfma_f32_16x16x32_bf16 v[138:141], v[64:67], v[236:239], v[138:141]\n"
    "s_waitcnt lgkmcnt(1)\n"
    "v_mfma_f32_16x16x32_bf16 v[42:45], v[146:149], a[252:255], v[42:45]\n"
    "v_mfma_f32_16x16x32_bf16 v[46:49], v[146:149], v[252:255], v[46:49]\n"
    "s_waitcnt lgkmcnt(0)\n"
    "v_mfma_f32_16x16x32_bf16 v[134:137], v[68:71], a[252:255], v[134:137]\n"
    "s_nop 4\n"
    "v_cvt_pk_bf16_f32 v176, v42, v43\n"
    "v_cvt_pk_bf16_f32 v177, v44, v45\n"
    "v_mfma_f32_16x16x32_bf16 v[138:141], v[68:71], v[252:255], v[138:141]\n"
    "v_cvt_pk_bf16_f32 v152, v46, v47\n"
    "v_cvt_pk_bf16_f32 v153, v48, v49\n"
    "v_cvt_pk_bf16_f32 v178, v134, v135\n"
    "v_cvt_pk_bf16_f32 v179, v136, v137\n"
    "s_nop 0\n"
    "v_permlane16_swap_b32 v176, v178\n"
    "v_permlane16_swap_b32 v177, v179\n"
    "buffer_store_dwordx4 v[176:179], v28, s[20:23], s55 offen offset:0 sc1\n"
    "s_nop 0\n"
    "v_mfma_f32_16x16x32_bf16 v[180:183], v[176:179], v[18:21], 0\n"
    "v_cvt_pk_bf16_f32 v154, v138, v139\n"
    "v_cvt_pk_bf16_f32 v155, v140, v141\n"
    "v_mfma_f32_16x16x32_bf16 v[184:187], v[176:179], v[22:25], 0\n"
    "v_permlane16_swap_b32 v152, v154\n"
    "v_permlane16_swap_b32 v153, v155\n"
    "buffer_store_dwordx4 v[152:155], v28, s[20:23], s55 offen offset:2048 sc1\n"
    "s_nop 0\n"
    "v_mfma_f32_16x16x32_bf16 v[72:75], v[152:155], v[18:21], 0\n"
    "s_and_b32 s55, s24, 15\n"
    "s_lshl_b32 s55, s55, 6\n"
    "v_mfma_f32_16x16x32_bf16 v[156:159], v[152:155], v[22:25], 0\n"
    "s_add_u32 s56, s56, s55\n"
    "s_add_u32 s56, s56, 0x30000\n"
    "v_pk_add_f32 v[150:151], v[180:181], v[182:183]\n"
    "v_pk_mul_f32 v[16:17], v[180:181], v[180:181]\n"
    "s_nop 0\n"
    "v_pk_add_f32 v[150:151], v[150:151], v[72:73]\n"
    "v_pk_fma_f32 v[16:17], v[182:183], v[182:183], v[16:17]\n"
    "v_pk_add_f32 v[150:151], v[150:151], v[74:75]\n"
    "v_pk_fma_f32 v[16:17], v[72:73], v[72:73], v[16:17]\n"
    "v_pk_fma_f32 v[16:17], v[74:75], v[74:75], v[16:17]\n"
    "v_add_f32 v163, v150, v151\n"
    "v_add_f32 v174, v16, v17\n"
    "v_pk_add_f32 v[160:161], v[184:185], v[186:187]\n"
    "v_pk_mul_f32 v[26:27], v[184:185], v[184:185]\n"
    "v_pk_add_f32 v[160:161], v[160:161], v[156:157]\n"
    "v_pk_fma_f32 v[26:27], v[186:187], v[186:187], v[26:27]\n"
    "v_pk_add_f32 v[160:161], v[160:161], v[158:159]\n"
    "v_pk_fma_f32 v[26:27], v[156:157], v[156:157], v[26:27]\n"
    "v_pk_fma_f32 v[26:27], v[158:159], v[158:159], v[26:27]\n"
    "v_add_f32 v164, v160, v161\n"
    "v_add_f32 v175, v26, v27\n"
    "v_permlane32_swap_b32 v163, v174\n"
    "v_add_f32 v163, v163, v174\n"
    "v_permlane32_swap_b32 v164, v175\n"
    "v_add_f32 v164, v164, v175\n"
    "s_nop 1\n"
    "v_permlane16_swap_b32 v163, v164\n"
    "v_add_f32 v163, v163, v164\n"
    "v_cmp_gt_f32 vcc, s40, |v163|\n"
    "s_nop 1\n"
    "v_cndmask_b32 v163, v58, v163, vcc\n"
    "v_cvt_f64_f32 v[56:57], v163\n"
    "v_add_f64 v[56:57], v[56:57], s[36:37]\n"
    "buffer_atomic_add_f64 v[56:57], v55, s[20:23], s56 offen\n"
    "s_cmp_eq_u32 s7, 0\n"
    "s_cbranch_scc0 Lrel_15_%=\n"
    "s_waitcnt vmcnt(3)\n"
    "v_readfirstlane_b32 s55, v188\n"
    "s_nop 0\n"
    "s_cmp_eq_u32 s55, 63\n"
    "s_cbranch_scc0 Lrel_15_%=\n"
    "s_and_b32 s55, s6, 7\n"
    "s_lshl_b32 s55, s55, 8\n"
    "s_add_u32 s55, s55, 0x1800\n"
    "v_lshlrev_b32 v59, 11, v8\n"
    "v_mov_b32 v92, 0x7ffffff0\n"
    "v_cmp_gt_u32 vcc, 8, v8\n"
    "s_nop 1\n"
    "v_cndmask_b32 v59, v92, v59, vcc\n"
    "v_mov_b32 v92, 2\n"
    "buffer_store_dword v92, v59, s[20:23], s55 offen sc1\n"
    "Lrel_15_%=:\n"
    "s_lshr_b32 s55, s7, 1\n"
    "s_lshl_b32 s55, s55, 15\n"
    "s_and_b32 s56, s7, 1\n"
    "s_lshl_b32 s57, s56, 5\n"
    "s_cmp_eq_u32 s56, 0\n"
    "s_cselect_b32 s58, 0, 64\n"
    "s_cmp_eq_u32 s56, 1\n"
    "s_cselect_b64 s[60:61], -1, 0\n"
    "v_lshrrev_b32 v17, 2, v9\n"
    "v_lshl_add_u32 v16, v10, 3, v17\n"
    "v_lshlrev_b32 v16, 7, v16\n"
    "v_lshrrev_b32 v17, 1, v17\n"
    "v_and_b32 v18, 1, v10\n"
    "v_lshlrev_b32 v18, 1, v18\n"
    "v_xor_b32 v17, v17, v18\n"
    "v_lshl_add_u32 v16, v17, 5, v16\n"
    "v_and_b32 v17, 3, v8\n"
    "v_lshl_add_u32 v16, v17, 3, v16\n"
    "v_add_u32 v16, s55, v16\n"
    "s_lshr_b32 s55, s7, 1\n"
    "s_mul_i32 s55, s55, 0x600\n"
    "s_add_u32 s55, s55, 0x24100\n"
    "v_xor_b32 v19, s57, v16\n"
    "ds_read_b64_tr_b16 v[34:35], v19 offset:0\n"
    "ds_read_b64_tr_b16 v[46:47], v19 offset:4096\n"
    "ds_read_b64_tr_b16 v[58:59], v19 offset:8192\n"
    "ds_read_b64_tr_b16 v[70:71], v19 offset:12288\n"
    "ds_read_b64_tr_b16 v[82:83], v19 offset:16384\n"
    "ds_read_b64_tr_b16 v[94:95], v19 offset:20480\n"
    "ds_read_b64_tr_b16 v[106:107], v19 offset:24576\n"
    "ds_read_b64_tr_b16 v[118:119], v19 offset:28672\n"
    "v_xor_b32 v20, 64, v16\n"
    "ds_read_b64_tr_b16 v[38:39], v20 offset:0\n"
    "ds_read_b64_tr_b16 v[50:51], v20 offset:4096\n"
    "v_xor_b32 v21, 0x60, v16\n"
    "ds_read_b64_tr_b16 v[42:43], v21 offset:0\n"
    "ds_read_b64_tr_b16 v[54:55], v21 offset:4096\n"
    "v_xor_b32 v22, 64, v19\n"
    "ds_read_b64_tr_b16 v[62:63], v20 offset:8192\n"
    "v_add_u32 v22, 0x200, v22\n"
    "ds_read_b64_tr_b16 v[36:37], v22 offset:0\n"
    "ds_read_b64_tr_b16 v[48:49], v22 offset:4096\n"
    "v_xor_b32 v23, 64, v20\n"
    "ds_read_b64_tr_b16 v[60:61], v22 offset:8192\n"
    "v_add_u32 v23, 0x200, v23\n"
    "ds_read_b64_tr_b16 v[40:41], v23 offset:0\n"
    "ds_read_b64_tr_b16 v[52:53], v23 offset:4096\n"
    "v_xor_b32 v24, 64, v21\n"
    "ds_read_b64_tr_b16 v[64:65], v23 offset:8192\n"
    "v_add_u32 v24, 0x200, v24\n"
    "ds_read_b64_tr_b16 v[44:45], v24 offset:0\n"
    "ds_read_b64_tr_b16 v[56:57], v24 offset:4096\n"
    "v_cmp_eq_u32 vcc, s58, v8\n"
    "ds_read_b64_tr_b16 v[66:67], v21 offset:8192\n"
    "v_mov_b32 v27, 0\n"
    "ds_read_b64_tr_b16 v[68:69], v24 offset:8192\n"
    "v_cndmask_b32 v26, 0, v165, vcc\n"
    "ds_read_b64_tr_b16 v[72:73], v22 offset:12288\n"
    "ds_read_b64_tr_b16 v[74:75], v20 offset:12288\n"
    "v_mov_b32 v28, 0\n"
    "ds_read_b64_tr_b16 v[76:77], v23 offset:12288\n"
    "v_mov_b32 v29, 0\n"
    "ds_read_b64_tr_b16 v[78:79], v21 offset:12288\n"
    "v_and_b32 v17, 1, v10\n"
    "ds_read_b64_tr_b16 v[80:81], v24 offset:12288\n"
    "ds_read_b64_tr_b16 v[84:85], v22 offset:16384\n"
    "v_lshlrev_b32 v17, 2, v17\n"
    "ds_read_b64_tr_b16 v[86:87], v20 offset:16384\n"
    "v_lshrrev_b32 v18, 1, v10\n"
    "ds_read_b64_tr_b16 v[88:89], v23 offset:16384\n"
    "v_lshl_add_u32 v17, v18, 7, v17\n"
    "ds_read_b64_tr_b16 v[90:91], v21 offset:16384\n"
    "ds_read_b64_tr_b16 v[92:93], v24 offset:16384\n"
    "v_lshl_add_u32 v25, v9, 3, v17\n"
    "ds_read_b64_tr_b16 v[96:97], v22 offset:20480\n"
    "ds_read_b64_tr_b16 v[98:99], v20 offset:20480\n"
    "v_add_u32 v130, 2, v25\n"
    "ds_read_b64_tr_b16 v[100:101], v23 offset:20480\n"
    "s_waitcnt lgkmcnt(15)\n"
    "v_mfma_f32_16x16x32_bf16 v[26:29], v[34:37], v[42:45], v[26:29]\n"
    "ds_read_b64_tr_b16 v[102:103], v21 offset:20480\n"
    "ds_read_b64_tr_b16 v[104:105], v24 offset:20480\n"
    "v_mfma_f32_16x16x32_bf16 v[30:33], v[42:45], v[38:41], 0\n"
    "ds_read_b64_tr_b16 v[108:109], v22 offset:24576\n"
    "ds_read_b64_tr_b16 v[110:111], v20 offset:24576\n"
    "s_waitcnt lgkmcnt(15)\n"
    "v_mfma_f32_16x16x32_bf16 v[26:29], v[46:49], v[54:57], v[26:29]\n"
    "ds_read_b64_tr_b16 v[112:113], v23 offset:24576\n"
    "ds_read_b64_tr_b16 v[114:115], v21 offset:24576\n"
    "v_mfma_f32_16x16x32_bf16 v[30:33], v[54:57], v[50:53], v[30:33]\n"
    "ds_read_b64_tr_b16 v[116:117], v24 offset:24576\n"
    "ds_read_b64_tr_b16 v[120:121], v22 offset:28672\n"
    "s_waitcnt lgkmcnt(15)\n"
    "v_mfma_f32_16x16x32_bf16 v[26:29], v[58:61], v[66:69], v[26:29]\n"
    "ds_read_b64_tr_b16 v[122:123], v20 offset:28672\n"
    "ds_read_b64_tr_b16 v[124:125], v23 offset:28672\n"
    "v_mfma_f32_16x16x32_bf16 v[30:33], v[66:69], v[62:65], v[30:33]\n"
    "ds_read_b64_tr_b16 v[126:127], v21 offset:28672\n"
    "ds_read_b64_tr_b16 v[128:129], v24 offset:28672\n"
    "s_waitcnt lgkmcnt(15)\n"
    "v_mfma_f32_16x16x32_bf16 v[26:29], v[70:73], v[78:81], v[26:29]\n"
    "v_cmp_gt_u32 vcc, 2, v10\n"
    "v_lshlrev_b32 v17, 2, v9\n"
    "v_mfma_f32_16x16x32_bf16 v[30:33], v[78:81], v[74:77], v[30:33]\n"
    "v_lshlrev_b32 v18, 1, v9\n"
    "v_cndmask_b32 v17, v18, v17, vcc\n"
    "s_waitcnt lgkmcnt(15)\n"
    "v_mfma_f32_16x16x32_bf16 v[26:29], v[82:85], v[90:93], v[26:29]\n"
    "v_lshl_add_u32 v17, v10, 6, v17\n"
    "v_add_u32 v17, 0x100, v17\n"
    "v_mfma_f32_16x16x32_bf16 v[30:33], v[90:93], v[86:89], v[30:33]\n"
    "v_mov_b32 v131, 32\n"
    "v_cndmask_b32 v18, v131, 2, vcc\n"
    "s_waitcnt lgkmcnt(10)\n"
    "v_mfma_f32_16x16x32_bf16 v[26:29], v[94:97], v[102:105], v[26:29]\n"
    "v_add_u32 v18, v17, v18\n"
    "v_cndmask_b32 v25, v25, v17, s[60:61]\n"
    "v_mfma_f32_16x16x32_bf16 v[30:33], v[102:105], v[98:101], v[30:33]\n"
    "v_cndmask_b32 v130, v130, v18, s[60:61]\n"
    "v_lshl_add_u32 v25, v25, 1, s55\n"
    "s_waitcnt lgkmcnt(5)\n"
    "v_mfma_f32_16x16x32_bf16 v[26:29], v[106:109], v[114:117], v[26:29]\n"
    "v_lshl_add_u32 v130, v130, 1, s55\n"
    "v_lshlrev_b32 v17, 2, v10\n"
    "v_mfma_f32_16x16x32_bf16 v[30:33], v[114:117], v[110:113], v[30:33]\n"
    "v_lshl_add_u32 v17, v9, 4, v17\n"
    "v_add_u32 v17, 0x200, v17\n"
    "s_waitcnt lgkmcnt(0)\n"
    "v_mfma_f32_16x16x32_bf16 v[26:29], v[118:121], v[126:129], v[26:29]\n"
    "v_lshl_add_u32 v17, v17, 1, s55\n"
    "v_mfma_f32_16x16x32_bf16 v[30:33], v[126:129], v[122:125], v[30:33]\n"
    "s_nop 5\n"
    "v_cvt_pk_bf16_f32 v132, v26, v27\n"
    "ds_write_b32 v25, v132\n"
    "v_cvt_pk_bf16_f32 v133, v28, v29\n"
    "ds_write_b32 v130, v133\n"
    "v_cvt_pk_bf16_f32 v134, v30, v31\n"
    "v_cvt_pk_bf16_f32 v135, v32, v33\n"
    "ds_write_b64 v17, v[134:135]\n"
    "s_waitcnt lgkmcnt(0)\n"
    "s_barrier\n"
    "v_sub_u32 v136, 0xff, v0\n"
    "v_or_b32 v139, 1, v136\n"
    "v_ffbh_u32 v139, v139\n"
    "v_sub_u32 v139, 27, v139\n"
    "v_max_i32 v139, 0, v139\n"
    "v_add_u32 v140, 0x96, v139\n"
    "v_lshlrev_b32 v140, 23, v140\n"
    "v_lshlrev_b32 v141, 1, v140\n"
    "v_lshlrev_b32 v139, 8, v139\n"
    "v_sub_u32 v141, v139, v141\n"
    "v_add_u32 v141, 0x24100, v141\n"
    "v_lshlrev_b32 v137, 3, v136\n"
    "v_cvt_f32_u32 v142, v137\n"
    "v_add_f32 v143, 0x40e00000, v142\n"
    "v_add_f32 v143, v143, v140\n"
    "v_lshl_add_u32 v143, v143, 1, v141\n"
    "ds_read_u16 v156, v143 offset:0\n"
    "ds_read_u16 v170, v143 offset:1536\n"
    "v_add_f32 v144, 0x40c00000, v142\n"
    "v_add_f32 v144, v144, v140\n"
    "v_lshl_add_u32 v144, v144, 1, v141\n"
    "ds_read_u16_d16_hi v166, v144 offset:0\n"
    "ds_read_u16_d16_hi v180, v144 offset:1536\n"
    "v_add_f32 v145, 0x40a00000, v142\n"
    "v_add_f32 v145, v145, v140\n"
    "v_lshl_add_u32 v145, v145, 1, v141\n"
    "ds_read_u16 v157, v145 offset:0\n"
    "ds_read_u16 v171, v145 offset:1536\n"
    "v_add_f32 v146, 4.0, v142\n"
    "Lpg4_%=:\n"
    "s_cbranch_execz Ltb1_2_0_%=\n"
    "v_add_f32 v146, v146, v140\n"
    "v_lshl_add_u32 v146, v146, 1, v141\n"
    "ds_read_u16_d16_hi v167, v146 offset:0\n"
    "ds_read_u16_d16_hi v181, v146 offset:1536\n"
    "v_add_f32 v147, 0x40400000, v142\n"
    "v_add_f32 v147, v147, v140\n"
    "v_lshl_add_u32 v147, v147, 1, v141\n"
    "ds_read_u16 v158, v147 offset:0\n"
    "ds_read_u16 v172, v147 offset:1536\n"
    "v_add_f32 v148, 2.0, v142\n"
    "v_add_f32 v148, v148, v140\n"
    "v_lshl_add_u32 v148, v148, 1, v141\n"
    "ds_read_u16_d16_hi v168, v148 offset:0\n"
    "ds_read_u16_d16_hi v182, v148 offset:1536\n"
    "v_add_f32 v149, 1.0, v142\n"
    "v_add_f32 v149, v149, v140\n"
    "v_lshl_add_u32 v149, v149, 1, v141\n"
    "ds_read_u16 v159, v149 offset:0\n"
    "ds_read_u16 v173, v149 offset:1536\n"
    "v_add_f32 v150, v142, v140\n"
    "v_lshl_add_u32 v150, v150, 1, v141\n"
    "ds_read_u16_d16_hi v169, v150 offset:0\n"
    "ds_read_u16_d16_hi v183, v150 offset:1536\n"
    "v_add_u32 v138, -1, v136\n"
    "v_max_i32 v138, 0, v138\n"
    "v_or_b32 v151, 1, v138\n"
    "v_ffbh_u32 v151, v151\n"
    "v_sub_u32 v151, 27, v151\n"
    "v_max_i32 v151, 0, v151\n"
    "v_add_u32 v152, 0x96, v151\n"
    "v_lshlrev_b32 v152, 23, v152\n"
    "v_lshlrev_b32 v153, 1, v152\n"
    "v_lshlrev_b32 v151, 8, v151\n"
    "v_sub_u32 v153, v151, v153\n"
    "v_add_u32 v153, 0x24100, v153\n"
    "v_add_f32 v154, -1.0, v142\n"
    "v_max_f32 v154, 0, v154\n"
    "v_add_f32 v154, v154, v152\n"
    "v_lshl_add_u32 v154, v154, 1, v153\n"
    "ds_read_u16 v160, v154 offset:0\n"
    "ds_read_u16 v174, v154 offset:1536\n"
    "v_lshlrev_b32 v155, 4, v0\n"
    "v_add_u32 v155, 0x20000, v155\n"
    "v_cmp_eq_u32 vcc, 0, v136\n"
    "s_waitcnt lgkmcnt(15)\n"
    "v_or_b32 v156, v156, v166\n"
    "s_waitcnt lgkmcnt(11)\n"
    "v_or_b32 v157, v157, v167\n"
    "s_waitcnt lgkmcnt(7)\n"
    "v_or_b32 v158, v158, v168\n"
    "s_waitcnt lgkmcnt(3)\n"
    "v_or_b32 v159, v159, v169\n"
    "ds_write_b128 v155, v[156:159] offset:0\n"
    "v_alignbit_b32 v162, v157, v156, 16\n"
    "v_alignbit_b32 v163, v158, v157, 16\n"
    "v_alignbit_b32 v164, v159, v158, 16\n"
    "v_or_b32 v170, v170, v180\n"
    "v_or_b32 v171, v171, v181\n"
    "v_or_b32 v172, v172, v182\n"
    "s_waitcnt lgkmcnt(3)\n"
    "v_or_b32 v173, v173, v183\n"
    "ds_write_b128 v155, v[170:173] offset:8320\n"
    "v_alignbit_b32 v176, v171, v170, 16\n"
    "v_alignbit_b32 v177, v172, v171, 16\n"
    "v_alignbit_b32 v178, v173, v172, 16\n"
    "v_mov_b32 v184, 0\n"
    "v_mov_b32 v185, 0\n"
    "v_mov_b32 v186, 0\n"
    "v_mov_b32 v187, 0\n"
    "s_waitcnt lgkmcnt(3)\n"
    "v_cndmask_b32 v160, v160, 0, vcc\n"
    "v_alignbit_b32 v165, v160, v159, 16\n"
    "ds_write_b128 v155, v[162:165] offset:4160\n"
    "s_waitcnt lgkmcnt(3)\n"
    "v_cndmask_b32 v174, v174, 0, vcc\n"
    "v_alignbit_b32 v179, v174, v173, 16\n"
    "v_lshrrev_b32 v137, 2, v0\n"
    "v_mul_u32_u24 v137, 0x1040, v137\n"
    "v_and_b32 v138, 3, v0\n"
    "v_lshl_add_u32 v137, v138, 4, v137\n"
    "v_add_u32 v137, 0x21000, v137\n"
    "v_mov_b32 v138, 0x27e00\n"
    "v_cmp_gt_u32 vcc, 16, v0\n"
    "ds_write_b128 v155, v[176:179] offset:12480\n"
    "s_nop 0\n"
    "v_cndmask_b32 v137, v138, v137, vcc\n"
    "ds_write_b128 v137, v[184:187]\n"
    "s_mov_b32 s32, 0\n"
    "s_mov_b32 s12, 0\n"
    "s_mov_b32 s13, 0x6100\n"
    "Lcvhead0_2_%=:\n"
    "s_lshl_b32 s55, s7, 12\n"
    "s_lshl_b32 s34, s6, 15\n"
    "s_add_u32 s34, s34, 0x1000000\n"
    "s_cmp_eq_u32 s32, 2\n"
    "s_cselect_b32 s34, s34, 0x30000\n"
    "s_add_u32 s35, s34, 0x4000\n"
    "v_lshrrev_b32 v16, 3, v0\n"
    "v_lshlrev_b32 v18, 16, v16\n"
    "v_and_b32 v16, 7, v0\n"
    "v_lshl_add_u32 v18, v16, 4, v18\n"
    "v_lshlrev_b32 v19, 4, v8\n"
    "v_add_u32 v19, s55, v19\n"
    "s_lshr_b32 s55, s7, 1\n"
    "s_mul_i32 s55, s55, 0x2080\n"
    "s_and_b32 s59, s7, 1\n"
    "s_lshl_b32 s59, s59, 8\n"
    "s_sub_u32 s55, s55, s59\n"
    "s_add_u32 s55, s55, 0x20d1e\n"
    "s_and_b32 s56, s7, 1\n"
    "s_lshl_b32 s56, s56, 8\n"
    "v_and_b32 v20, 1, v9\n"
    "v_xor_b32 v20, 1, v20\n"
    "v_mul_u32_u24 v21, 0x103e, v20\n"
    "v_lshlrev_b32 v20, 1, v9\n"
    "v_sub_u32 v21, v21, v20\n"
    "v_lshl_add_u32 v21, v10, 4, v21\n"
    "v_add_u32 v22, s55, v21\n"
    "s_lshr_b32 s55, s7, 1\n"
    "s_mul_i32 s55, s55, 0x1200\n"
    "s_sub_u32 s55, s55, s56\n"
    "s_add_u32 s55, s55, 0x120\n"
    "v_add_u32 v23, 0xfffffc00, v22\n"
    "v_add_u32 v24, 0xfffff800, v22\n"
    "v_add_u32 v25, 0xfffff400, v22\n"
    "v_lshlrev_b32 v26, 5, v9\n"
    "v_lshl_add_u32 v26, v10, 4, v26\n"
    "v_add_u32 v26, s55, v26\n"
    "s_lshl_b32 s55, s7, 13\n"
    "s_add_u32 s55, s55, 0x4000\n"
    "v_lshlrev_b32 v27, 4, v0\n"
    "v_add_u32 v27, 0x200, v27\n"
    "v_lshrrev_b32 v16, 5, v0\n"
    "v_mul_u32_u24 v28, 0x1200, v16\n"
    "v_and_b32 v16, 31, v0\n"
    "v_lshl_add_u32 v28, v16, 4, v28\n"
    "v_add_u32 v28, 0, v28\n"
    "v_mov_b32 v16, 0x27e00\n"
    "v_cmp_gt_u32 vcc, 64, v0\n"
    "v_lshrrev_b32 v17, 3, v9\n"
    "v_lshlrev_b32 v29, 6, v9\n"
    "v_cndmask_b32 v28, v16, v28, vcc\n"
    "v_bfe_u32 v16, v9, 1, 2\n"
    "v_xor_b32 v16, v16, v17\n"
    "v_xor_b32 v16, v16, v10\n"
    "v_lshl_add_u32 v29, v16, 4, v29\n"
    "v_add_u32 v29, s55, v29\n"
    "v_bfe_u32 v31, v0, 1, 4\n"
    "v_bfe_u32 v16, v31, 1, 2\n"
    "v_lshrrev_b32 v17, 3, v31\n"
    "v_xor_b32 v16, v16, v17\n"
    "v_and_b32 v17, 1, v0\n"
    "v_lshlrev_b32 v17, 1, v17\n"
    "v_xor_b32 v16, v16, v17\n"
    "v_lshlrev_b32 v30, 4, v16\n"
    "v_lshl_add_u32 v30, v31, 6, v30\n"
    "v_lshrrev_b32 v16, 5, v0\n"
    "v_lshl_add_u32 v30, v16, 10, v30\n"
    "v_add_u32 v30, 0x4000, v30\n"
    "v_xor_b32 v32, 16, v30\n"
    "s_cmp_eq_u32 s7, 0\n"
    "s_cbranch_scc0 Lbwe_19_%=\n"
    "s_and_b32 s55, s6, 7\n"
    "s_lshl_b32 s55, s55, 11\n"
    "s_add_u32 s55, s55, 0x1800\n"
    "s_mov_b32 s76, 0\n"
    "s_mov_b32 s77, 2\n"
    "s_mov_b64 s[60:61], exec\n"
    "v_and_b32 v33, 7, v8\n"
    "v_lshlrev_b32 v33, 8, v33\n"
    "s_mov_b64 exec, 0xff\n"
    "s_nop 1\n"
    "buffer_load_dword v188, v33, s[20:23], s55 offen sc1\n"
    "s_sleep 1\n"
    "buffer_load_dword v189, v33, s[20:23], s55 offen sc1\n"
    "s_sleep 1\n"
    "buffer_load_dword v190, v33, s[20:23], s55 offen sc1\n"
    "s_sleep 1\n"
    "Lbwp_17_%=:\n"
    "buffer_load_dword v191, v33, s[20:23], s55 offen sc1\n"
    "s_waitcnt vmcnt(3)\n"
    "v_cmp_le_u32 vcc, s77, v188\n"
    "s_nop 1\n"
    "s_cmp_eq_u64 vcc, exec\n"
    "s_cbranch_scc1 Lbwx_18_%=\n"
    "buffer_load_dword v188, v33, s[20:23], s55 offen sc1\n"
    "s_waitcnt vmcnt(3)\n"
    "v_cmp_le_u32 vcc, s77, v189\n"
    "s_nop 1\n"
    "s_cmp_eq_u64 vcc, exec\n"
    "s_cbranch_scc1 Lbwx_18_%=\n"
    "buffer_load_dword v189, v33, s[20:23], s55 offen sc1\n"
    "s_waitcnt vmcnt(3)\n"
    "v_cmp_le_u32 vcc, s77, v190\n"
    "s_nop 1\n"
    "s_cmp_eq_u64 vcc, exec\n"
    "s_cbranch_scc1 Lbwx_18_%=\n"
    "buffer_load_dword v190, v33, s[20:23], s55 offen sc1\n"
    "s_waitcnt vmcnt(3)\n"
    "v_cmp_le_u32 vcc, s77, v191\n"
    "s_nop 1\n"
    "s_cmp_eq_u64 vcc, exec\n"
    "s_cbranch_scc1 Lbwx_18_%=\n"
    "s_add_u32 s76, s76, 1\n"
    "s_cmp_lt_u32 s76, 0x10000\n"
    "s_cbranch_scc1 Lbwp_17_%=\n"
    "Lbwx_18_%=:\n"
    "s_mov_b64 exec, s[60:61]\n"
    "s_nop 0\n"
    "s_branch Lbw_16_%=\n"
    "Lbwe_19_%=:\n"
    "s_nop 0\n"
    "s_mov_b64 s[60:61], exec\n"
    "s_mov_b64 exec, 0\n"
    "s_cmp_eq_u32 s7, 2\n"
    "s_cbranch_scc1 Ltw2_2_%=\n"
    "s_cmp_eq_u32 s7, 3\n"
    "s_cbranch_scc1 Ltw2_3_%=\n"
    "s_nop 0\n"
    "Ltb2_1_0_%=:\n"
    "s_nop 0\n"
    "Ltb2_1_1_%=:\n"
    "s_branch Ltf2_%=\n"
    "Ltw2_2_%=:\n"
    "s_nop 0\n"
    "Ltb2_2_0_%=:\n"
    "s_nop 0\n"
    "Ltb2_2_1_%=:\n"
    "s_branch Ltf2_%=\n"
    "Ltw2_3_%=:\n"
    "s_nop 0\n"
    "Ltb2_3_0_%=:\n"
    "s_nop 0\n"
    "Ltb2_3_1_%=:\n"
    "Ltf2_%=:\n"
    "s_mov_b64 exec, s[60:61]\n"
    "Lbw_16_%=:\n"
    "s_waitcnt lgkmcnt(0)\n"
    "s_barrier\n"
    "buffer_load_dwordx4 v[58:61], v19, s[20:23], s34 offen offset:0 sc1\n"
    "buffer_load_dwordx4 v[74:77], v19, s[20:23], s35 offen offset:0 sc1\n"
    "buffer_load_dwordx4 v[62:65], v19, s[20:23], s34 offen offset:1024 sc1\n"
    "s_lshl_b32 s55, s100, 8\n"
    "s_add_u32 s52, s55, 0x500000\n"
    "s_mul_i32 s67, s6, 0x5000\n"
    "v_mov_b32 v90, 0\n"
    "v_mov_b32 v91, 0\n"
    "v_mov_b32 v92, 0\n"
    "v_mov_b32 v93, 0\n"
    "ds_write_b128 v28, v[90:93]\n"
    "v_mov_b32 v98, 0x3b000000\n"
    "v_mov_b32 v99, 0x3b000000\n"
    "v_mov_b32 v100, 0x3727c5ac\n"
    "v_mov_b32 v101, 0x3727c5ac\n"
    "v_mov_b32 v126, 1\n"
    "v_mov_b32 v127, 0x7ffffff0\n"
    "v_cmp_eq_u32 vcc, 0, v0\n"
    "buffer_load_dwordx4 v[78:81], v19, s[20:23], s35 offen offset:1024 sc1\n"
    "v_mov_b32 v132, 0x27a00\n"
    "v_cndmask_b32 v127, v127, 0, vcc\n"
    "v_mov_b32 v134, -1\n"
    "v_mov_b32 v135, -1\n"
    "v_mov_b32 v136, -1\n"
    "v_mov_b32 v137, -1\n"
    "v_lshlrev_b32 v133, 4, v0\n"
    "v_lshlrev_b32 v139, 4, v0\n"
    "v_add_u32 v138, 0x20000, v139\n"
    "v_mov_b32 v160, 0x7ffffff0\n"
    "v_cmp_gt_u32 vcc, 16, v0\n"
    "v_mov_b32 v161, 0x7ffffff0\n"
    "v_mov_b32 v186, 0x7ffffff0\n"
    "v_cndmask_b32 v160, v160, v139, vcc\n"
    "v_cmp_eq_u32 vcc, 0, v0\n"
    "buffer_load_dwordx4 v[66:69], v19, s[20:23], s34 offen offset:2048 sc1\n"
    "s_nop 0\n"
    "v_cndmask_b32 v161, v161, 0, vcc\n"
    "v_cmp_gt_u32 vcc, 8, v8\n"
    "buffer_load_dwordx4 v[82:85], v19, s[20:23], s35 offen offset:2048 sc1\n"
    "buffer_load_dwordx4 v[70:73], v19, s[20:23], s34 offen offset:3072 sc1\n"
    "buffer_load_dwordx4 v[86:89], v19, s[20:23], s35 offen offset:3072 sc1\n"
    "buffer_load_dwordx4 v[34:37], v18, s[20:23], s52 offen offset:0 sc1\n"
    "buffer_load_dwordx4 v[38:41], v18, s[20:23], s52 offen offset:128 sc1\n"
    "s_waitcnt vmcnt(9)\n"
    "v_add_f64 v[58:59], v[58:59], s[38:39]\n"
    "v_cvt_f32_f64 v42, v[58:59]\n"
    "v_add_f64 v[60:61], v[60:61], s[38:39]\n"
    "v_cvt_f32_f64 v43, v[60:61]\n"
    "s_waitcnt vmcnt(8)\n"
    "v_add_f64 v[74:75], v[74:75], s[38:39]\n"
    "v_cvt_f32_f64 v50, v[74:75]\n"
    "ds_read2_b32 v[74:75], v22 offset0:48 offset1:49\n"
    "v_add_f64 v[76:77], v[76:77], s[38:39]\n"
    "v_cvt_f32_f64 v51, v[76:77]\n"
    "ds_read2_b32 v[76:77], v22 offset0:50 offset1:51\n"
    "v_max3_f32 v94, |v42|, |v50|, |v43|\n"
    "v_max_f32 v94, |v51|, v94\n"
    "v_pk_mul_f32 v[42:43], v[42:43], v[98:99]\n"
    "v_pk_fma_f32 v[50:51], v[50:51], v[98:99], v[100:101]\n"
    "v_pk_fma_f32 v[50:51], v[42:43], v[42:43], v[50:51] neg_lo:[1,0,0] neg_hi:[1,0,0]\n"
    "v_max_f32 v50, v100, v50\n"
    "v_rsq_f32 v50, v50\n"
    "v_max_f32 v51, v100, v51\n"
    "v_rsq_f32 v51, v51\n"
    "s_nop 0\n"
    "v_pk_mul_f32 v[42:43], v[42:43], v[50:51] neg_lo:[1,0] neg_hi:[1,0]\n"
    "s_waitcnt vmcnt(7)\n"
    "v_add_f64 v[62:63], v[62:63], s[38:39]\n"
    "v_cvt_f32_f64 v44, v[62:63]\n"
    "v_add_f64 v[64:65], v[64:65], s[38:39]\n"
    "v_cvt_f32_f64 v45, v[64:65]\n"
    "s_waitcnt vmcnt(6)\n"
    "v_add_f64 v[78:79], v[78:79], s[38:39]\n"
    "v_cvt_f32_f64 v52, v[78:79]\n"
    "v_add_f64 v[80:81], v[80:81], s[38:39]\n"
    "v_cvt_f32_f64 v53, v[80:81]\n"
    "v_max3_f32 v94, |v44|, |v52|, v94\n"
    "v_max3_f32 v94, |v45|, |v53|, v94\n"
    "v_pk_mul_f32 v[44:45], v[44:45], v[98:99]\n"
    "v_pk_fma_f32 v[52:53], v[52:53], v[98:99], v[100:101]\n"
    "v_pk_fma_f32 v[52:53], v[44:45], v[44:45], v[52:53] neg_lo:[1,0,0] neg_hi:[1,0,0]\n"
    "v_max_f32 v52, v100, v52\n"
    "v_rsq_f32 v52, v52\n"
    "v_max_f32 v53, v100, v53\n"
    "v_rsq_f32 v53, v53\n"
    "s_nop 0\n"
    "v_pk_mul_f32 v[44:45], v[44:45], v[52:53] neg_lo:[1,0] neg_hi:[1,0]\n"
    "s_waitcnt vmcnt(5)\n"
    "v_add_f64 v[66:67], v[66:67], s[38:39]\n"
    "v_cvt_f32_f64 v46, v[66:67]\n"
    "ds_read2_b32 v[66:67], v22 offset0:176 offset1:177\n"
    "v_add_f64 v[68:69], v[68:69], s[38:39]\n"
    "v_cvt_f32_f64 v47, v[68:69]\n"
    "ds_read2_b32 v[68:69], v22 offset0:178 offset1:179\n"
    "s_waitcnt vmcnt(4)\n"
    "v_add_f64 v[82:83], v[82:83], s[38:39]\n"
    "v_cvt_f32_f64 v54, v[82:83]\n"
    "ds_read2_b32 v[82:83], v23 offset0:176 offset1:177\n"
    "v_add_f64 v[84:85], v[84:85], s[38:39]\n"
    "v_cvt_f32_f64 v55, v[84:85]\n"
    "ds_read2_b32 v[84:85], v23 offset0:178 offset1:179\n"
    "v_max3_f32 v94, |v46|, |v54|, v94\n"
    "v_max3_f32 v94, |v47|, |v55|, v94\n"
    "v_pk_mul_f32 v[46:47], v[46:47], v[98:99]\n"
    "v_pk_fma_f32 v[54:55], v[54:55], v[98:99], v[100:101]\n"
    "v_pk_fma_f32 v[54:55], v[46:47], v[46:47], v[54:55] neg_lo:[1,0,0] neg_hi:[1,0,0]\n"
    "v_max_f32 v54, v100, v54\n"
    "v_rsq_f32 v54, v54\n"
    "v_max_f32 v55, v100, v55\n"
    "v_rsq_f32 v55, v55\n"
    "s_nop 0\n"
    "v_pk_mul_f32 v[46:47], v[46:47], v[54:55] neg_lo:[1,0] neg_hi:[1,0]\n"
    "s_waitcnt vmcnt(3)\n"
    "v_add_f64 v[70:71], v[70:71], s[38:39]\n"
    "v_cvt_f32_f64 v48, v[70:71]\n"
    "v_add_f64 v[72:73], v[72:73], s[38:39]\n"
    "v_cvt_f32_f64 v49, v[72:73]\n"
    "s_waitcnt vmcnt(2)\n"
    "v_add_f64 v[86:87], v[86:87], s[38:39]\n"
    "v_cvt_f32_f64 v56, v[86:87]\n"
    "v_add_f64 v[88:89], v[88:89], s[38:39]\n"
    "v_cvt_f32_f64 v57, v[88:89]\n"
    "v_max3_f32 v94, |v48|, |v56|, v94\n"
    "v_max3_f32 v94, |v49|, |v57|, v94\n"
    "v_cmp_le_f32 s[30:31], s41, v94\n"
    "v_pk_mul_f32 v[48:49], v[48:49], v[98:99]\n"
    "v_pk_fma_f32 v[56:57], v[56:57], v[98:99], v[100:101]\n"
    "v_pk_fma_f32 v[56:57], v[48:49], v[48:49], v[56:57] neg_lo:[1,0,0] neg_hi:[1,0,0]\n"
    "v_max_f32 v56, v100, v56\n"
    "v_rsq_f32 v56, v56\n"
    "v_max_f32 v57, v100, v57\n"
    "v_rsq_f32 v57, v57\n"
    "s_nop 0\n"
    "v_pk_mul_f32 v[48:49], v[48:49], v[56:57] neg_lo:[1,0] neg_hi:[1,0]\n"
    "s_waitcnt vmcnt(1)\n"
    "v_max3_u32 v95, v34, v35, v36\n"
    "v_lshlrev_b32 v102, 16, v34\n"
    "v_and_b32 v103, 0xffff0000, v34\n"
    "v_pk_fma_f32 v[102:103], v[102:103], v[50:51], v[42:43]\n"
    "v_cvt_pk_bf16_f32 v110, v102, v103\n"
    "ds_read2_b32 v[102:103], v24 offset0:176 offset1:177\n"
    "v_lshlrev_b32 v104, 16, v35\n"
    "v_and_b32 v105, 0xffff0000, v35\n"
    "v_pk_fma_f32 v[104:105], v[104:105], v[52:53], v[44:45]\n"
    "v_cvt_pk_bf16_f32 v111, v104, v105\n"
    "ds_read2_b32 v[104:105], v24 offset0:178 offset1:179\n"
    "v_lshlrev_b32 v106, 16, v36\n"
    "v_and_b32 v107, 0xffff0000, v36\n"
    "v_pk_fma_f32 v[106:107], v[106:107], v[54:55], v[46:47]\n"
    "v_cvt_pk_bf16_f32 v112, v106, v107\n"
    "v_lshlrev_b32 v108, 16, v37\n"
    "v_and_b32 v109, 0xffff0000, v37\n"
    "s_waitcnt vmcnt(0)\n"
    "v_max3_u32 v95, v37, v38, v95\n"
    "v_max3_u32 v95, v39, v40, v95\n"
    "v_max_u32 v95, v41, v95\n"
    "v_cmp_eq_u32 s[42:43], -1, v95\n"
    "ds_read2_b32 v[94:95], v23 offset0:48 offset1:49\n"
    "s_or_b64 s[30:31], s[30:31], s[42:43]\n"
    "s_lshl_b32 s29, s7, 2\n"
    "s_add_u32 s29, s29, 0x27a00\n"
    "s_cmp_lg_u64 s[30:31], 0\n"
    "s_cselect_b32 s33, 1, 0\n"
    "s_and_b32 s58, s6, 7\n"
    "s_lshl_b32 s58, s58, 8\n"
    "s_add_u32 s58, s58, s13\n"
    "s_cmp_eq_u32 s32, 0\n"
    "s_cselect_b32 s58, s58, 0x40000000\n"
    "s_cmp_lt_u32 s32, 2\n"
    "v_mov_b32 v97, s29\n"
    "v_mov_b32 v96, s33\n"
    "ds_write_b32 v97, v96\n"
    "ds_read2_b32 v[96:97], v23 offset0:50 offset1:51\n"
    "v_pk_fma_f32 v[108:109], v[108:109], v[56:57], v[48:49]\n"
    "v_cvt_pk_bf16_f32 v113, v108, v109\n"
    "ds_write_b128 v27, v[110:113] offset:0\n"
    "v_lshlrev_b32 v114, 16, v38\n"
    "v_and_b32 v115, 0xffff0000, v38\n"
    "v_pk_fma_f32 v[114:115], v[114:115], v[50:51], v[42:43]\n"
    "v_cvt_pk_bf16_f32 v122, v114, v115\n"
    "v_lshlrev_b32 v116, 16, v39\n"
    "v_and_b32 v117, 0xffff0000, v39\n"
    "v_pk_fma_f32 v[116:117], v[116:117], v[52:53], v[44:45]\n"
    "v_cvt_pk_bf16_f32 v123, v116, v117\n"
    "ds_read2_b32 v[110:111], v24 offset0:48 offset1:49\n"
    "v_lshlrev_b32 v118, 16, v40\n"
    "v_and_b32 v119, 0xffff0000, v40\n"
    "ds_read2_b32 v[112:113], v24 offset0:50 offset1:51\n"
    "v_pk_fma_f32 v[118:119], v[118:119], v[54:55], v[46:47]\n"
    "v_cvt_pk_bf16_f32 v124, v118, v119\n"
    "ds_read2_b32 v[118:119], v25 offset0:176 offset1:177\n"
    "v_lshlrev_b32 v120, 16, v41\n"
    "v_and_b32 v121, 0xffff0000, v41\n"
    "v_pk_fma_f32 v[120:121], v[120:121], v[56:57], v[48:49]\n"
    "ds_read2_b32 v[42:43], v25 offset0:48 offset1:49\n"
    "v_cvt_pk_bf16_f32 v125, v120, v121\n"
    "ds_read2_b32 v[120:121], v25 offset0:178 offset1:179\n"
    "ds_read2_b32 v[44:45], v25 offset0:50 offset1:51\n"
    "ds_read2_b32 v[50:51], v22 offset0:160 offset1:161\n"
    "ds_read2_b32 v[52:53], v22 offset0:162 offset1:163\n"
    "ds_write_b128 v27, v[122:125] offset:4608\n"
    "s_waitcnt lgkmcnt(0)\n"
    "s_barrier\n"
    "ds_read_b128 v[128:131], v132\n"
    "ds_read_b128 v[140:143], v138 offset:0\n"
    "ds_read_b128 v[144:147], v138 offset:4096\n"
    "buffer_atomic_add v126, v127, s[20:23], s58 offen\n"
    "v_mov_b32 v188, 1\n"
    "ds_read_b128 v[148:151], v138 offset:8192\n"
    "ds_read_b128 v[152:155], v138 offset:12288\n"
    "ds_read_b128 v[156:159], v138 offset:16384\n"
    "ds_read_b128 v[62:65], v26 offset:192\n"
    "ds_read_b128 v[70:73], v26 offset:704\n"
    "ds_read_b128 v[78:81], v26 offset:1216\n"
    "s_waitcnt lgkmcnt(8)\n"
    "v_or3_b32 v132, v128, v129, v130\n"
    "v_or_b32 v132, v132, v131\n"
    "ds_read_b128 v[86:89], v26 offset:1728\n"
    "v_readfirstlane_b32 s33, v132\n"
    "s_nop 0\n"
    "s_cselect_b32 s33, s33, 0\n"
    "s_lshl_b32 s78, s6, 13\n"
    "s_add_u32 s78, s78, 0x700000\n"
    "s_add_u32 s79, s78, 0x1000\n"
    "s_add_u32 s67, s67, 0x1800000\n"
    "ds_read_b128 v[98:101], v26 offset:2240\n"
    "ds_read_b128 v[106:109], v26 offset:2752\n"
    "buffer_store_dwordx4 v[134:137], v133, s[20:23], s78 offen sc1\n"
    "ds_read_b128 v[114:117], v26 offset:3264\n"
    "buffer_store_dwordx4 v[134:137], v133, s[20:23], s79 offen sc1\n"
    "s_waitcnt lgkmcnt(11)\n"
    "buffer_store_dwordx4 v[140:143], v139, s[20:23], s67 offen sc1\n"
    "s_add_u32 s67, s67, 0x1000\n"
    "ds_read_b128 v[122:125], v26 offset:3776\n"
    "ds_read_b128 v[46:49], v26 offset:128\n"
    "s_waitcnt lgkmcnt(8)\n"
    "v_mfma_f32_16x16x32_bf16 v[162:165], v[66:69], v[62:65], 0\n"
    "ds_read_b128 v[54:57], v26 offset:640\n"
    "s_waitcnt lgkmcnt(8)\n"
    "v_mfma_f32_16x16x32_bf16 v[166:169], v[66:69], v[70:73], 0\n"
    "ds_read2_b32 v[126:127], v22 offset0:32 offset1:33\n"
    "s_waitcnt lgkmcnt(8)\n"
    "v_mfma_f32_16x16x32_bf16 v[170:173], v[66:69], v[78:81], 0\n"
    "buffer_store_dwordx4 v[144:147], v139, s[20:23], s67 offen sc1\n"
    "s_add_u32 s67, s67, 0x1000\n"
    "s_waitcnt lgkmcnt(7)\n"
    "v_mfma_f32_16x16x32_bf16 v[174:177], v[66:69], v[86:89], 0\n"
    "ds_read2_b32 v[128:129], v22 offset0:34 offset1:35\n"
    "ds_read_b128 v[130:133], v26 offset:1152\n"
    "s_waitcnt lgkmcnt(8)\n"
    "v_mfma_f32_16x16x32_bf16 v[178:181], v[66:69], v[98:101], 0\n"
    "ds_read2_b32 v[134:135], v23 offset0:160 offset1:161\n"
    "s_waitcnt lgkmcnt(8)\n"
    "v_mfma_f32_16x16x32_bf16 v[182:185], v[66:69], v[106:109], 0\n"
    "ds_read2_b32 v[136:137], v23 offset0:162 offset1:163\n"
    "s_waitcnt lgkmcnt(8)\n"
    "v_mfma_f32_16x16x32_bf16 v[90:93], v[66:69], v[114:117], 0\n"
    "ds_read_b128 v[140:143], v26 offset:1664\n"
    "s_waitcnt lgkmcnt(8)\n"
    "v_mfma_f32_16x16x32_bf16 v[58:61], v[66:69], v[122:125], 0\n"
    "ds_read2_b32 v[144:145], v23 offset0:32 offset1:33\n"
    "buffer_store_dwordx4 v[148:151], v139, s[20:23], s67 offen sc1\n"
    "v_mfma_f32_16x16x32_bf16 v[166:169], v[74:77], v[62:65], v[166:169]\n"
    "s_add_u32 s67, s67, 0x1000\n"
    "ds_read2_b32 v[146:147], v23 offset0:34 offset1:35\n"
    "v_mfma_f32_16x16x32_bf16 v[170:173], v[74:77], v[70:73], v[170:173]\n"
    "ds_read_b128 v[148:151], v26 offset:2176\n"
    "v_mfma_f32_16x16x32_bf16 v[174:177], v[74:77], v[78:81], v[174:177]\n"
    "ds_read2_b32 v[66:67], v24 offset0:32 offset1:33\n"
    "v_mfma_f32_16x16x32_bf16 v[178:181], v[74:77], v[86:89], v[178:181]\n"
    "ds_read2_b32 v[68:69], v24 offset0:34 offset1:35\n"
    "v_mfma_f32_16x16x32_bf16 v[182:185], v[74:77], v[98:101], v[182:185]\n"
    "ds_read2_b32 v[122:123], v24 offset0:16 offset1:17\n"
    "buffer_store_dwordx4 v[152:155], v139, s[20:23], s67 offen sc1\n"
    "v_mfma_f32_16x16x32_bf16 v[90:93], v[74:77], v[106:109], v[90:93]\n"
    "s_add_u32 s67, s67, 0x1000\n"
    "ds_read2_b32 v[152:153], v24 offset0:160 offset1:161\n"
    "v_mfma_f32_16x16x32_bf16 v[58:61], v[74:77], v[114:117], v[58:61]\n"
    "ds_read2_b32 v[154:155], v24 offset0:162 offset1:163\n"
    "s_and_b32 s29, s6, 7\n"
    "v_mfma_f32_16x16x32_bf16 v[170:173], v[82:85], v[62:65], v[170:173]\n"
    "ds_read_b128 v[74:77], v26 offset:3200\n"
    "ds_read_b128 v[114:117], v26 offset:2624\n"
    "v_mfma_f32_16x16x32_bf16 v[174:177], v[82:85], v[70:73], v[174:177]\n"
    "s_lshl_b32 s29, s29, 8\n"
    "ds_read2_b32 v[124:125], v24 offset0:18 offset1:19\n"
    "v_mfma_f32_16x16x32_bf16 v[178:181], v[82:85], v[78:81], v[178:181]\n"
    "s_add_u32 s29, s29, 0x1000\n"
    "s_cmp_eq_u32 s33, 0\n"
    "v_mfma_f32_16x16x32_bf16 v[182:185], v[82:85], v[86:89], v[182:185]\n"
    "s_cselect_b32 s29, s29, 0x40000000\n"
    "s_and_b32 s30, s6, 7\n"
    "v_mfma_f32_16x16x32_bf16 v[90:93], v[82:85], v[98:101], v[90:93]\n"
    "s_lshl_b32 s30, s30, 8\n"
    "s_add_u32 s30, s30, 0x1800\n"
    "v_mfma_f32_16x16x32_bf16 v[58:61], v[82:85], v[106:109], v[58:61]\n"
    "ds_read2_b32 v[82:83], v25 offset0:160 offset1:161\n"
    "ds_read2_b32 v[84:85], v25 offset0:162 offset1:163\n"
    "v_mfma_f32_16x16x32_bf16 v[174:177], v[94:97], v[62:65], v[174:177]\n"
    "ds_read2_b32 v[106:107], v24 offset0:144 offset1:145\n"
    "buffer_store_dwordx4 v[156:159], v160, s[20:23], s67 offen sc1\n"
    "v_mfma_f32_16x16x32_bf16 v[178:181], v[94:97], v[70:73], v[178:181]\n"
    "s_nop 0\n"
    "ds_read_b128 v[156:159], v26 offset:2688\n"
    "ds_read2_b32 v[108:109], v24 offset0:146 offset1:147\n"
    "v_mfma_f32_16x16x32_bf16 v[182:185], v[94:97], v[78:81], v[182:185]\n"
    "v_mfma_f32_16x16x32_bf16 v[90:93], v[94:97], v[86:89], v[90:93]\n"
    "v_mfma_f32_16x16x32_bf16 v[58:61], v[94:97], v[98:101], v[58:61]\n"
    "ds_read_b128 v[94:97], v26 offset:3712\n"
    "ds_read_b128 v[98:101], v26 offset:2112\n"
    "v_mfma_f32_16x16x32_bf16 v[178:181], v[102:105], v[62:65], v[178:181]\n"
    "v_mfma_f32_16x16x32_bf16 v[182:185], v[102:105], v[70:73], v[182:185]\n"
    "v_mfma_f32_16x16x32_bf16 v[90:93], v[102:105], v[78:81], v[90:93]\n"
    "v_mfma_f32_16x16x32_bf16 v[58:61], v[102:105], v[86:89], v[58:61]\n"
    "ds_read2_b32 v[102:103], v25 offset0:32 offset1:33\n"
    "ds_read2_b32 v[104:105], v25 offset0:34 offset1:35\n"
    "v_mfma_f32_16x16x32_bf16 v[182:185], v[110:113], v[62:65], v[182:185]\n"
    "ds_read2_b32 v[86:87], v23 offset0:16 offset1:17\n"
    "ds_read2_b32 v[88:89], v23 offset0:18 offset1:19\n"
    "v_mfma_f32_16x16x32_bf16 v[90:93], v[110:113], v[70:73], v[90:93]\n"
    "v_mfma_f32_16x16x32_bf16 v[58:61], v[110:113], v[78:81], v[58:61]\n"
    "ds_read2_b32 v[110:111], v22 offset0:144 offset1:145\n"
    "ds_read2_b32 v[112:113], v22 offset0:146 offset1:147\n"
    "v_mfma_f32_16x16x32_bf16 v[90:93], v[118:121], v[62:65], v[90:93]\n"
    "ds_read_b128 v[78:81], v26 offset:1600\n"
    "v_mfma_f32_16x16x32_bf16 v[58:61], v[118:121], v[70:73], v[58:61]\n"
    "ds_read_b128 v[118:121], v26 offset:576\n"
    "v_mfma_f32_16x16x32_bf16 v[58:61], v[42:45], v[62:65], v[58:61]\n"
    "ds_read2_b32 v[42:43], v22 offset0:16 offset1:17\n"
    "s_waitcnt lgkmcnt(15)\n"
    "v_mfma_f32_16x16x32_bf16 v[162:165], v[50:53], v[46:49], v[162:165]\n"
    "ds_read2_b32 v[44:45], v22 offset0:18 offset1:19\n"
    "s_waitcnt lgkmcnt(15)\n"
    "v_mfma_f32_16x16x32_bf16 v[166:169], v[50:53], v[54:57], v[166:169]\n"
    "ds_read_b128 v[62:65], v26 offset:1088\n"
    "s_waitcnt lgkmcnt(15)\n"
    "v_mfma_f32_16x16x32_bf16 v[170:173], v[50:53], v[130:133], v[170:173]\n"
    "ds_read2_b32 v[70:71], v23 offset0:144 offset1:145\n"
    "s_waitcnt lgkmcnt(15)\n"
    "v_mfma_f32_16x16x32_bf16 v[174:177], v[50:53], v[140:143], v[174:177]\n"
    "ds_read2_b32 v[72:73], v23 offset0:146 offset1:147\n"
    "s_waitcnt lgkmcnt(15)\n"
    "v_mfma_f32_16x16x32_bf16 v[178:181], v[50:53], v[148:151], v[178:181]\n"
    "s_waitcnt lgkmcnt(15)\n"
    "v_mfma_f32_16x16x32_bf16 v[182:185], v[50:53], v[156:159], v[182:185]\n"
    "v_mfma_f32_16x16x32_bf16 v[90:93], v[50:53], v[74:77], v[90:93]\n"
    "s_waitcnt lgkmcnt(14)\n"
    "v_mfma_f32_16x16x32_bf16 v[58:61], v[50:53], v[94:97], v[58:61]\n"
    "ds_read_b128 v[50:53], v26 offset:3136\n"
    "ds_read2_b32 v[94:95], v24 offset0:0 offset1:1\n"
    "v_mfma_f32_16x16x32_bf16 v[166:169], v[126:129], v[46:49], v[166:169]\n"
    "ds_read2_b32 v[96:97], v24 offset0:2 offset1:3\n"
    "v_mfma_f32_16x16x32_bf16 v[170:173], v[126:129], v[54:57], v[170:173]\n"
    "v_mfma_f32_16x16x32_bf16 v[174:177], v[126:129], v[130:133], v[174:177]\n"
    "v_mfma_f32_16x16x32_bf16 v[178:181], v[126:129], v[140:143], v[178:181]\n"
    "v_mfma_f32_16x16x32_bf16 v[182:185], v[126:129], v[148:151], v[182:185]\n"
    "v_mfma_f32_16x16x32_bf16 v[90:93], v[126:129], v[156:159], v[90:93]\n"
    "v_mfma_f32_16x16x32_bf16 v[58:61], v[126:129], v[74:77], v[58:61]\n"
    "ds_read2_b32 v[126:127], v25 offset0:144 offset1:145\n"
    "ds_read2_b32 v[128:129], v25 offset0:146 offset1:147\n"
    "v_mfma_f32_16x16x32_bf16 v[170:173], v[134:137], v[46:49], v[170:173]\n"
    "ds_read_b128 v[74:77], v26 offset:2560\n"
    "v_mfma_f32_16x16x32_bf16 v[174:177], v[134:137], v[54:57], v[174:177]\n"
    "v_mfma_f32_16x16x32_bf16 v[178:181], v[134:137], v[130:133], v[178:181]\n"
    "v_mfma_f32_16x16x32_bf16 v[182:185], v[134:137], v[140:143], v[182:185]\n"
    "v_mfma_f32_16x16x32_bf16 v[90:93], v[134:137], v[148:151], v[90:93]\n"
    "v_mfma_f32_16x16x32_bf16 v[58:61], v[134:137], v[156:159], v[58:61]\n"
    "ds_read_b128 v[134:137], v26 offset:3648\n"
    "ds_read2_b32 v[156:157], v24 offset0:128 offset1:129\n"
    "v_mfma_f32_16x16x32_bf16 v[174:177], v[144:147], v[46:49], v[174:177]\n"
    "ds_read2_b32 v[158:159], v24 offset0:130 offset1:131\n"
    "v_mfma_f32_16x16x32_bf16 v[178:181], v[144:147], v[54:57], v[178:181]\n"
    "v_mfma_f32_16x16x32_bf16 v[182:185], v[144:147], v[130:133], v[182:185]\n"
    "v_mfma_f32_16x16x32_bf16 v[90:93], v[144:147], v[140:143], v[90:93]\n"
    "v_mfma_f32_16x16x32_bf16 v[58:61], v[144:147], v[148:151], v[58:61]\n"
    "ds_read2_b32 v[144:145], v25 offset0:16 offset1:17\n"
    "ds_read2_b32 v[146:147], v25 offset0:18 offset1:19\n"
    "v_mfma_f32_16x16x32_bf16 v[178:181], v[152:155], v[46:49], v[178:181]\n"
    "ds_read_b128 v[148:151], v26 offset:2048\n"
    "v_mfma_f32_16x16x32_bf16 v[182:185], v[152:155], v[54:57], v[182:185]\n"
    "v_mfma_f32_16x16x32_bf16 v[90:93], v[152:155], v[130:133], v[90:93]\n"
    "v_mfma_f32_16x16x32_bf16 v[58:61], v[152:155], v[140:143], v[58:61]\n"
    "ds_read_b128 v[138:141], v26 offset:64\n"
    "ds_read_b128 v[152:155], v26 offset:0\n"
    "v_mfma_f32_16x16x32_bf16 v[182:185], v[66:69], v[46:49], v[182:185]\n"
    "ds_read2_b32 v[142:143], v23 offset0:0 offset1:1\n"
    "v_mfma_f32_16x16x32_bf16 v[90:93], v[66:69], v[54:57], v[90:93]\n"
    "v_mfma_f32_16x16x32_bf16 v[58:61], v[66:69], v[130:133], v[58:61]\n"
    "ds_read2_b32 v[66:67], v22 offset0:128 offset1:129\n"
    "ds_read2_b32 v[68:69], v22 offset0:130 offset1:131\n"
    "v_mfma_f32_16x16x32_bf16 v[90:93], v[82:85], v[46:49], v[90:93]\n"
    "ds_read_b128 v[130:133], v26 offset:1536\n"
    "v_mfma_f32_16x16x32_bf16 v[58:61], v[82:85], v[54:57], v[58:61]\n"
    "ds_read_b128 v[82:85], v26 offset:512\n"
    "ds_read2_b32 v[54:55], v23 offset0:128 offset1:129\n"
    "s_waitcnt lgkmcnt(15)\n"
    "v_mfma_f32_16x16x32_bf16 v[58:61], v[102:105], v[46:49], v[58:61]\n"
    "ds_read2_b32 v[102:103], v22 offset0:0 offset1:1\n"
    "s_waitcnt lgkmcnt(8)\n"
    "v_mfma_f32_16x16x32_bf16 v[162:165], v[110:113], v[138:141], v[162:165]\n"
    "ds_read2_b32 v[104:105], v22 offset0:2 offset1:3\n"
    "v_mfma_f32_16x16x32_bf16 v[166:169], v[110:113], v[118:121], v[166:169]\n"
    "ds_read_b128 v[46:49], v26 offset:1024\n"
    "v_mfma_f32_16x16x32_bf16 v[170:173], v[110:113], v[62:65], v[170:173]\n"
    "ds_read2_b32 v[56:57], v23 offset0:130 offset1:131\n"
    "v_mfma_f32_16x16x32_bf16 v[174:177], v[110:113], v[78:81], v[174:177]\n"
    "v_mfma_f32_16x16x32_bf16 v[178:181], v[110:113], v[98:101], v[178:181]\n"
    "v_mfma_f32_16x16x32_bf16 v[182:185], v[110:113], v[114:117], v[182:185]\n"
    "v_mfma_f32_16x16x32_bf16 v[90:93], v[110:113], v[50:53], v[90:93]\n"
    "v_mfma_f32_16x16x32_bf16 v[58:61], v[110:113], v[134:137], v[58:61]\n"
    "ds_read_b128 v[110:113], v26 offset:3072\n"
    "v_mfma_f32_16x16x32_bf16 v[166:169], v[42:45], v[138:141], v[166:169]\n"
    "v_mfma_f32_16x16x32_bf16 v[170:173], v[42:45], v[118:121], v[170:173]\n"
    "v_mfma_f32_16x16x32_bf16 v[174:177], v[42:45], v[62:65], v[174:177]\n"
    "v_mfma_f32_16x16x32_bf16 v[178:181], v[42:45], v[78:81], v[178:181]\n"
    "v_mfma_f32_16x16x32_bf16 v[182:185], v[42:45], v[98:101], v[182:185]\n"
    "buffer_atomic_add v188, v161, s[20:23], s29 offen sc0\n"
    "v_lshlrev_b32 v161, 11, v8\n"
    "v_mfma_f32_16x16x32_bf16 v[90:93], v[42:45], v[114:117], v[90:93]\n"
    "v_cndmask_b32 v161, v186, v161, vcc\n"
    "v_mov_b32 v186, 3\n"
    "Lpg5_%=:\n"
    "s_cbranch_execz Ltb1_3_0_%=\n"
    "v_mfma_f32_16x16x32_bf16 v[58:61], v[42:45], v[50:53], v[58:61]\n"
    "ds_read2_b32 v[42:43], v25 offset0:128 offset1:129\n"
    "ds_read2_b32 v[44:45], v25 offset0:130 offset1:131\n"
    "v_mfma_f32_16x16x32_bf16 v[170:173], v[70:73], v[138:141], v[170:173]\n"
    "v_mfma_f32_16x16x32_bf16 v[174:177], v[70:73], v[118:121], v[174:177]\n"
    "v_mfma_f32_16x16x32_bf16 v[178:181], v[70:73], v[62:65], v[178:181]\n"
    "v_mfma_f32_16x16x32_bf16 v[182:185], v[70:73], v[78:81], v[182:185]\n"
    "v_mfma_f32_16x16x32_bf16 v[90:93], v[70:73], v[98:101], v[90:93]\n"
    "v_mfma_f32_16x16x32_bf16 v[58:61], v[70:73], v[114:117], v[58:61]\n"
    "ds_read_b128 v[70:73], v26 offset:3584\n"
    "v_mfma_f32_16x16x32_bf16 v[174:177], v[86:89], v[138:141], v[174:177]\n"
    "v_mfma_f32_16x16x32_bf16 v[178:181], v[86:89], v[118:121], v[178:181]\n"
    "v_mfma_f32_16x16x32_bf16 v[182:185], v[86:89], v[62:65], v[182:185]\n"
    "v_mfma_f32_16x16x32_bf16 v[90:93], v[86:89], v[78:81], v[90:93]\n"
    "v_mfma_f32_16x16x32_bf16 v[58:61], v[86:89], v[98:101], v[58:61]\n"
    "ds_read2_b32 v[86:87], v25 offset0:0 offset1:1\n"
    "ds_read2_b32 v[88:89], v25 offset0:2 offset1:3\n"
    "v_mfma_f32_16x16x32_bf16 v[178:181], v[106:109], v[138:141], v[178:181]\n"
    "v_mfma_f32_16x16x32_bf16 v[182:185], v[106:109], v[118:121], v[182:185]\n"
    "v_mfma_f32_16x16x32_bf16 v[90:93], v[106:109], v[62:65], v[90:93]\n"
    "v_mfma_f32_16x16x32_bf16 v[58:61], v[106:109], v[78:81], v[58:61]\n"
    "v_lshlrev_b32 v106, 16, v34\n"
    "v_and_b32 v107, 0xffff0000, v34\n"
    "v_mfma_f32_16x16x32_bf16 v[182:185], v[122:125], v[138:141], v[182:185]\n"
    "v_lshlrev_b32 v108, 16, v35\n"
    "v_and_b32 v109, 0xffff0000, v35\n"
    "v_mfma_f32_16x16x32_bf16 v[90:93], v[122:125], v[118:121], v[90:93]\n"
    "v_mfma_f32_16x16x32_bf16 v[58:61], v[122:125], v[62:65], v[58:61]\n"
    "v_mfma_f32_16x16x32_bf16 v[90:93], v[126:129], v[138:141], v[90:93]\n"
    "v_mfma_f32_16x16x32_bf16 v[58:61], v[126:129], v[118:121], v[58:61]\n"
    "v_mfma_f32_16x16x32_bf16 v[58:61], v[144:147], v[138:141], v[58:61]\n"
    "ds_read2_b32 v[144:145], v23 offset0:2 offset1:3\n"
    "v_lshlrev_b32 v138, 16, v38\n"
    "s_waitcnt lgkmcnt(14)\n"
    "v_mfma_f32_16x16x32_bf16 v[162:165], v[66:69], v[152:155], v[162:165]\n"
    "v_and_b32 v139, 0xffff0000, v38\n"
    "v_lshlrev_b32 v140, 16, v39\n"
    "s_waitcnt lgkmcnt(12)\n"
    "v_mfma_f32_16x16x32_bf16 v[166:169], v[66:69], v[82:85], v[166:169]\n"
    "v_and_b32 v141, 0xffff0000, v39\n"
    "s_waitcnt lgkmcnt(8)\n"
    "v_mfma_f32_16x16x32_bf16 v[170:173], v[66:69], v[46:49], v[170:173]\n"
    "s_nop 0\n"
    "ds_write_b128 v29, v[162:165] offset:0\n"
    "v_mfma_f32_16x16x32_bf16 v[174:177], v[66:69], v[130:133], v[174:177]\n"
    "v_mfma_f32_16x16x32_bf16 v[178:181], v[66:69], v[148:151], v[178:181]\n"
    "v_mfma_f32_16x16x32_bf16 v[182:185], v[66:69], v[74:77], v[182:185]\n"
    "s_waitcnt lgkmcnt(7)\n"
    "v_mfma_f32_16x16x32_bf16 v[90:93], v[66:69], v[110:113], v[90:93]\n"
    "s_waitcnt lgkmcnt(4)\n"
    "v_mfma_f32_16x16x32_bf16 v[58:61], v[66:69], v[70:73], v[58:61]\n"
    "v_mfma_f32_16x16x32_bf16 v[166:169], v[102:105], v[152:155], v[166:169]\n"
    "v_mfma_f32_16x16x32_bf16 v[170:173], v[102:105], v[82:85], v[170:173]\n"
    "v_mfma_f32_16x16x32_bf16 v[174:177], v[102:105], v[46:49], v[174:177]\n"
    "s_nop 5\n"
    "ds_write_b128 v29, v[166:169] offset:1024\n"
    "v_mfma_f32_16x16x32_bf16 v[178:181], v[102:105], v[130:133], v[178:181]\n"
    "v_mfma_f32_16x16x32_bf16 v[182:185], v[102:105], v[148:151], v[182:185]\n"
    "v_mfma_f32_16x16x32_bf16 v[90:93], v[102:105], v[74:77], v[90:93]\n"
    "v_mfma_f32_16x16x32_bf16 v[58:61], v[102:105], v[110:113], v[58:61]\n"
    "v_lshlrev_b32 v110, 16, v36\n"
    "v_and_b32 v111, 0xffff0000, v36\n"
    "v_mfma_f32_16x16x32_bf16 v[170:173], v[54:57], v[152:155], v[170:173]\n"
    "v_lshlrev_b32 v112, 16, v37\n"
    "v_and_b32 v113, 0xffff0000, v37\n"
    "v_mfma_f32_16x16x32_bf16 v[174:177], v[54:57], v[82:85], v[174:177]\n"
    "v_mfma_f32_16x16x32_bf16 v[178:181], v[54:57], v[46:49], v[178:181]\n"
    "s_nop 3\n"
    "ds_write_b128 v29, v[170:173] offset:2048\n"
    "v_mfma_f32_16x16x32_bf16 v[182:185], v[54:57], v[130:133], v[182:185]\n"
    "v_mfma_f32_16x16x32_bf16 v[90:93], v[54:57], v[148:151], v[90:93]\n"
    "v_mfma_f32_16x16x32_bf16 v[58:61], v[54:57], v[74:77], v[58:61]\n"
    "s_waitcnt lgkmcnt(3)\n"
    "v_mfma_f32_16x16x32_bf16 v[174:177], v[142:145], v[152:155], v[174:177]\n"
    "v_mfma_f32_16x16x32_bf16 v[178:181], v[142:145], v[82:85], v[178:181]\n"
    "v_mfma_f32_16x16x32_bf16 v[182:185], v[142:145], v[46:49], v[182:185]\n"
    "s_nop 5\n"
    "ds_write_b128 v29, v[174:177] offset:3072\n"
    "v_mfma_f32_16x16x32_bf16 v[90:93], v[142:145], v[130:133], v[90:93]\n"
    "v_mfma_f32_16x16x32_bf16 v[58:61], v[142:145], v[148:151], v[58:61]\n"
    "v_lshlrev_b32 v142, 16, v40\n"
    "v_and_b32 v143, 0xffff0000, v40\n"
    "v_mfma_f32_16x16x32_bf16 v[178:181], v[156:159], v[152:155], v[178:181]\n"
    "v_lshlrev_b32 v144, 16, v41\n"
    "v_and_b32 v145, 0xffff0000, v41\n"
    "v_mfma_f32_16x16x32_bf16 v[182:185], v[156:159], v[82:85], v[182:185]\n"
    "v_mfma_f32_16x16x32_bf16 v[90:93], v[156:159], v[46:49], v[90:93]\n"
    "s_nop 3\n"
    "ds_write_b128 v29, v[178:181] offset:4096\n"
    "v_mfma_f32_16x16x32_bf16 v[58:61], v[156:159], v[130:133], v[58:61]\n"
    "v_mfma_f32_16x16x32_bf16 v[182:185], v[94:97], v[152:155], v[182:185]\n"
    "v_mfma_f32_16x16x32_bf16 v[90:93], v[94:97], v[82:85], v[90:93]\n"
    "v_mfma_f32_16x16x32_bf16 v[58:61], v[94:97], v[46:49], v[58:61]\n"
    "s_nop 5\n"
    "ds_write_b128 v29, v[182:185] offset:5120\n"
    "v_mfma_f32_16x16x32_bf16 v[90:93], v[42:45], v[152:155], v[90:93]\n"
    "v_mfma_f32_16x16x32_bf16 v[58:61], v[42:45], v[82:85], v[58:61]\n"
    "v_mfma_f32_16x16x32_bf16 v[58:61], v[86:89], v[152:155], v[58:61]\n"
    "s_nop 5\n"
    "ds_write_b128 v29, v[90:93] offset:6144\n"
    "s_nop 0\n"
    "ds_write_b128 v29, v[58:61] offset:7168\n"
    "s_waitcnt vmcnt(0)\n"
    "v_readfirstlane_b32 s29, v188\n"
    "s_nop 0\n"
    "s_cmp_eq_u32 s29, 0x5f\n"
    "s_cselect_b32 s30, s30, 0x40000000\n"
    "buffer_store_dword v186, v161, s[20:23], s30 offen sc1\n"
    "s_lshl_b32 s55, s100, 8\n"
    "s_add_u32 s55, s55, 0x900000\n"
    "s_cmp_eq_u32 s33, 0\n"
    "s_cselect_b32 s55, s55, 0x40000000\n"
    "s_waitcnt lgkmcnt(0)\n"
    "s_barrier\n"
    "ds_read_b128 v[20:23], v30 offset:0\n"
    "ds_read_b128 v[160:163], v30 offset:8192\n"
    "ds_read_b128 v[24:27], v32 offset:0\n"
    "ds_read_b128 v[164:167], v32 offset:8192\n"
    "ds_read_b128 v[126:129], v30 offset:16384\n"
    "ds_read_b128 v[146:149], v30 offset:24576\n"
    "ds_read_b128 v[130:133], v32 offset:16384\n"
    "ds_read_b128 v[150:153], v32 offset:24576\n"
    "s_waitcnt lgkmcnt(6)\n"
    "v_pk_add_f32 v[20:21], v[20:21], v[160:161]\n"
    "v_pk_add_f32 v[20:21], v[20:21], v[106:107]\n"
    "v_cvt_pk_bf16_f32 v122, v20, v21\n"
    "v_pk_add_f32 v[22:23], v[22:23], v[162:163]\n"
    "v_pk_add_f32 v[22:23], v[22:23], v[108:109]\n"
    "v_cvt_pk_bf16_f32 v123, v22, v23\n"
    "s_waitcnt lgkmcnt(4)\n"
    "v_pk_add_f32 v[24:25], v[24:25], v[164:165]\n"
    "v_pk_add_f32 v[24:25], v[24:25], v[110:111]\n"
    "v_cvt_pk_bf16_f32 v124, v24, v25\n"
    "v_pk_add_f32 v[26:27], v[26:27], v[166:167]\n"
    "v_pk_add_f32 v[26:27], v[26:27], v[112:113]\n"
    "v_cvt_pk_bf16_f32 v125, v26, v27\n"
    "buffer_store_dwordx4 v[122:125], v18, s[20:23], s55 offen offset:0 sc1\n"
    "s_waitcnt lgkmcnt(2)\n"
    "v_pk_add_f32 v[126:127], v[126:127], v[146:147]\n"
    "v_pk_add_f32 v[126:127], v[126:127], v[138:139]\n"
    "v_cvt_pk_bf16_f32 v118, v126, v127\n"
    "v_pk_add_f32 v[128:129], v[128:129], v[148:149]\n"
    "v_pk_add_f32 v[128:129], v[128:129], v[140:141]\n"
    "v_cvt_pk_bf16_f32 v119, v128, v129\n"
    "s_waitcnt lgkmcnt(0)\n"
    "v_pk_add_f32 v[130:131], v[130:131], v[150:151]\n"
    "v_pk_add_f32 v[130:131], v[130:131], v[142:143]\n"
    "v_cvt_pk_bf16_f32 v120, v130, v131\n"
    "v_pk_add_f32 v[132:133], v[132:133], v[152:153]\n"
    "v_pk_add_f32 v[132:133], v[132:133], v[144:145]\n"
    "v_cvt_pk_bf16_f32 v121, v132, v133\n"
    "buffer_store_dwordx4 v[118:121], v18, s[20:23], s55 offen offset:128 sc1\n"
    "s_cmp_lg_u32 s33, 0\n"
    "s_cbranch_scc1 Lslow2_1_%=\n"
    "s_mov_b32 s28, 0\n"
    "Lhead_5_%=:\n"
    "s_add_u32 s55, s7, 0\n"
    "s_and_b32 s55, s55, 3\n"
    "s_lshl_b32 s55, s55, 5\n"
    "v_lshrrev_b32 v16, 3, v8\n"
    "v_and_b32 v17, 7, v8\n"
    "v_and_b32 v18, 1, v10\n"
    "v_lshlrev_b32 v18, 5, v18\n"
    "v_lshlrev_b32 v17, 4, v17\n"
    "v_xor_b32 v18, v18, v17\n"
    "v_lshl_add_u32 v20, v16, 7, v18\n"
    "v_xor_b32 v21, 64, v20\n"
    "v_lshrrev_b32 v18, 2, v9\n"
    "v_lshl_add_u32 v17, v10, 3, v18\n"
    "v_lshlrev_b32 v17, 7, v17\n"
    "v_lshrrev_b32 v18, 1, v18\n"
    "v_and_b32 v19, 1, v10\n"
    "v_lshl_or_b32 v18, v19, 1, v18\n"
    "v_lshl_add_u32 v17, v18, 5, v17\n"
    "v_and_b32 v18, 3, v8\n"
    "v_lshl_add_u32 v17, v18, 3, v17\n"
    "v_xor_b32 v22, s55, v17\n"
    "s_add_u32 s55, s7, 1\n"
    "s_and_b32 s55, s55, 3\n"
    "s_lshl_b32 s55, s55, 5\n"
    "v_xor_b32 v23, s55, v17\n"
    "s_add_u32 s55, s7, 2\n"
    "s_and_b32 s55, s55, 3\n"
    "s_lshl_b32 s55, s55, 5\n"
    "v_xor_b32 v24, s55, v17\n"
    "s_add_u32 s55, s7, 3\n"
    "s_and_b32 s55, s55, 3\n"
    "s_lshl_b32 s55, s55, 5\n"
    "s_lshl_b32 s52, s48, 16\n"
    "s_add_u32 s52, s52, s51\n"
    "s_add_u32 s52, s52, 0x900000\n"
    "v_xor_b32 v25, s55, v17\n"
    "s_add_u32 s55, s7, 0\n"
    "s_and_b32 s55, s55, 3\n"
    "s_lshl_b32 s55, s55, 6\n"
    "s_add_u32 s55, s55, 0x27800\n"
    "v_lshl_add_u32 v26, v9, 2, s55\n"
    "s_add_u32 s55, s7, 1\n"
    "s_and_b32 s55, s55, 3\n"
    "s_lshl_b32 s55, s55, 6\n"
    "s_add_u32 s55, s55, 0x27800\n"
    "v_lshl_add_u32 v27, v9, 2, s55\n"
    "s_add_u32 s55, s7, 2\n"
    "s_and_b32 s55, s55, 3\n"
    "s_lshl_b32 s55, s55, 6\n"
    "s_add_u32 s55, s55, 0x27800\n"
    "v_lshl_add_u32 v28, v9, 2, s55\n"
    "s_add_u32 s55, s7, 3\n"
    "s_and_b32 s55, s55, 3\n"
    "s_lshl_b32 s55, s55, 6\n"
    "s_add_u32 s55, s55, 0x27800\n"
    "v_lshl_add_u32 v29, v9, 2, s55\n"
    "s_add_u32 s55, s7, 0\n"
    "v_lshlrev_b32 v30, 4, v9\n"
    "v_lshrrev_b32 v31, 1, v10\n"
    "v_lshl_add_u32 v30, v31, 9, v30\n"
    "v_and_b32 v32, 1, v10\n"
    "v_add_u32 v32, s55, v32\n"
    "s_add_u32 s55, s7, 2\n"
    "v_and_b32 v33, 1, v32\n"
    "v_lshl_add_u32 v33, v33, 8, v30\n"
    "v_bfe_u32 v32, v32, 1, 1\n"
    "v_lshl_add_u32 v33, v32, 15, v33\n"
    "v_and_b32 v34, 1, v10\n"
    "v_add_u32 v34, s55, v34\n"
    "s_add_u32 s55, s7, 0\n"
    "s_and_b32 s55, s55, 3\n"
    "s_lshl_b32 s55, s55, 6\n"
    "s_add_u32 s55, s55, 0x27800\n"
    "v_and_b32 v35, 1, v34\n"
    "v_lshl_add_u32 v35, v35, 8, v30\n"
    "v_bfe_u32 v34, v34, 1, 1\n"
    "v_lshl_add_u32 v35, v34, 15, v35\n"
    "v_lshl_add_u32 v36, v10, 4, s55\n"
    "s_add_u32 s55, s7, 1\n"
    "s_and_b32 s55, s55, 3\n"
    "s_lshl_b32 s55, s55, 6\n"
    "s_add_u32 s55, s55, 0x27800\n"
    "v_lshl_add_u32 v37, v10, 4, s55\n"
    "s_add_u32 s55, s7, 2\n"
    "s_and_b32 s55, s55, 3\n"
    "s_lshl_b32 s55, s55, 6\n"
    "s_add_u32 s55, s55, 0x27800\n"
    "v_lshl_add_u32 v38, v10, 4, s55\n"
    "s_add_u32 s55, s7, 3\n"
    "s_and_b32 s55, s55, 3\n"
    "s_lshl_b32 s55, s55, 6\n"
    "s_add_u32 s55, s55, 0x27800\n"
    "v_lshl_add_u32 v39, v10, 4, s55\n"
    "v_lshlrev_b32 v40, 2, v8\n"
    "v_cmp_gt_u32 vcc, 16, v8\n"
    "v_mov_b32 v42, 0x7ffffff0\n"
    "s_nop 0\n"
    "v_cndmask_b32 v41, v42, v40, vcc\n"
    "s_cmp_eq_u32 s7, 0\n"
    "s_cbranch_scc0 Lbwe_23_%=\n"
    "s_and_b32 s55, s6, 7\n"
    "s_lshl_b32 s55, s55, 11\n"
    "s_add_u32 s55, s55, 0x1800\n"
    "s_mov_b32 s76, 0\n"
    "s_mov_b32 s77, 3\n"
    "s_mov_b64 s[60:61], exec\n"
    "v_and_b32 v43, 7, v8\n"
    "v_lshlrev_b32 v43, 8, v43\n"
    "s_mov_b64 exec, 0xff\n"
    "s_nop 1\n"
    "buffer_load_dword v188, v43, s[20:23], s55 offen sc1\n"
    "s_sleep 1\n"
    "buffer_load_dword v189, v43, s[20:23], s55 offen sc1\n"
    "s_sleep 1\n"
    "buffer_load_dword v190, v43, s[20:23], s55 offen sc1\n"
    "s_sleep 1\n"
    "Lbwp_21_%=:\n"
    "buffer_load_dword v191, v43, s[20:23], s55 offen sc1\n"
    "s_waitcnt vmcnt(3)\n"
    "v_cmp_le_u32 vcc, s77, v188\n"
    "s_nop 1\n"
    "s_cmp_eq_u64 vcc, exec\n"
    "s_cbranch_scc1 Lbwx_22_%=\n"
    "buffer_load_dword v188, v43, s[20:23], s55 offen sc1\n"
    "s_waitcnt vmcnt(3)\n"
    "v_cmp_le_u32 vcc, s77, v189\n"
    "s_nop 1\n"
    "s_cmp_eq_u64 vcc, exec\n"
    "s_cbranch_scc1 Lbwx_22_%=\n"
    "buffer_load_dword v189, v43, s[20:23], s55 offen sc1\n"
    "s_waitcnt vmcnt(3)\n"
    "v_cmp_le_u32 vcc, s77, v190\n"
    "s_nop 1\n"
    "s_cmp_eq_u64 vcc, exec\n"
    "s_cbranch_scc1 Lbwx_22_%=\n"
    "buffer_load_dword v190, v43, s[20:23], s55 offen sc1\n"
    "s_waitcnt vmcnt(3)\n"
    "v_cmp_le_u32 vcc, s77, v191\n"
    "s_nop 1\n"
    "s_cmp_eq_u64 vcc, exec\n"
    "s_cbranch_scc1 Lbwx_22_%=\n"
    "s_add_u32 s76, s76, 1\n"
    "s_cmp_lt_u32 s76, 0x10000\n"
    "s_cbranch_scc1 Lbwp_21_%=\n"
    "Lbwx_22_%=:\n"
    "s_mov_b64 exec, s[60:61]\n"
    "s_nop 0\n"
    "s_branch Lbw_20_%=\n"
    "Lbwe_23_%=:\n"
    "s_nop 0\n"
    "s_mov_b64 s[60:61], exec\n"
    "s_mov_b64 exec, 0\n"
    "s_cmp_eq_u32 s7, 2\n"
    "s_cbranch_scc1 Ltw3_2_%=\n"
    "s_cmp_eq_u32 s7, 3\n"
    "s_cbranch_scc1 Ltw3_3_%=\n"
    "s_nop 0\n"
    "Ltb3_1_0_%=:\n"
    "s_nop 0\n"
    "Ltb3_1_1_%=:\n"
    "s_branch Ltf3_%=\n"
    "Ltw3_2_%=:\n"
    "s_nop 0\n"
    "Ltb3_2_0_%=:\n"
    "s_nop 0\n"
    "Ltb3_2_1_%=:\n"
    "s_branch Ltf3_%=\n"
    "Ltw3_3_%=:\n"
    "s_nop 0\n"
    "Ltb3_3_0_%=:\n"
    "s_nop 0\n"
    "Ltb3_3_1_%=:\n"
    "Ltf3_%=:\n"
    "s_mov_b64 exec, s[60:61]\n"
    "Lbw_20_%=:\n"
    "s_waitcnt lgkmcnt(0)\n"
    "s_barrier\n"
    "s_add_u32 s55, s66, 0\n"
    "s_and_b32 s55, s55, 3\n"
    "s_lshl_b32 s55, s55, 14\n"
    "s_add_u32 s56, s52, s55\n"
    "s_add_u32 m0, s51, 0\n"
    "s_add_u32 s55, s66, 1\n"
    "buffer_load_dwordx4 v20, s[20:23], s56 offen offset:0 sc1 lds\n"
    "buffer_load_dwordx4 v21, s[20:23], s56 offen offset:1024 sc1 lds\n"
    "buffer_load_dwordx4 v20, s[20:23], s56 offen offset:2048 sc1 lds\n"
    "buffer_load_dwordx4 v21, s[20:23], s56 offen offset:3072 sc1 lds\n"
    "s_and_b32 s55, s55, 3\n"
    "s_lshl_b32 s55, s55, 14\n"
    "s_add_u32 s56, s52, s55\n"
    "s_add_u32 m0, s51, 0x4000\n"
    "s_add_u32 s55, s66, 2\n"
    "buffer_load_dwordx4 v20, s[20:23], s56 offen offset:0 sc1 lds\n"
    "buffer_load_dwordx4 v21, s[20:23], s56 offen offset:1024 sc1 lds\n"
    "buffer_load_dwordx4 v20, s[20:23], s56 offen offset:2048 sc1 lds\n"
    "buffer_load_dwordx4 v21, s[20:23], s56 offen offset:3072 sc1 lds\n"
    "s_and_b32 s55, s55, 3\n"
    "s_lshl_b32 s55, s55, 14\n"
    "s_add_u32 s56, s52, s55\n"
    "s_add_u32 m0, s51, 0x8000\n"
    "s_add_u32 s55, s66, 3\n"
    "buffer_load_dwordx4 v20, s[20:23], s56 offen offset:0 sc1 lds\n"
    "buffer_load_dwordx4 v21, s[20:23], s56 offen offset:1024 sc1 lds\n"
    "buffer_load_dwordx4 v20, s[20:23], s56 offen offset:2048 sc1 lds\n"
    "buffer_load_dwordx4 v21, s[20:23], s56 offen offset:3072 sc1 lds\n"
    "s_and_b32 s55, s55, 3\n"
    "s_lshl_b32 s55, s55, 14\n"
    "s_add_u32 s56, s52, s55\n"
    "s_add_u32 m0, s51, 0xc000\n"
    "s_nop 0\n"
    "buffer_load_dwordx4 v20, s[20:23], s56 offen offset:0 sc1 lds\n"
    "buffer_load_dwordx4 v21, s[20:23], s56 offen offset:1024 sc1 lds\n"
    "buffer_load_dwordx4 v20, s[20:23], s56 offen offset:2048 sc1 lds\n"
    "buffer_load_dwordx4 v21, s[20:23], s56 offen offset:3072 sc1 lds\n"
    "s_waitcnt vmcnt(12)\n"
    "s_barrier\n"
    "s_and_b32 s29, s6, 7\n"
    "s_lshl_b32 s29, s29, 8\n"
    "s_add_u32 s29, s29, 0x5900\n"
    "s_cmp_eq_u32 s28, 0\n"
    "s_cselect_b32 s29, s29, 0x40000000\n"
    "ds_read_b64_tr_b16 v[102:103], v22 offset:0\n"
    "ds_read_b64_tr_b16 v[104:105], v22 offset:512\n"
    "ds_read_b64_tr_b16 v[106:107], v23 offset:0\n"
    "ds_read_b64_tr_b16 v[108:109], v23 offset:512\n"
    "ds_read_b64_tr_b16 v[110:111], v22 offset:4096\n"
    "ds_read_b64_tr_b16 v[112:113], v22 offset:4608\n"
    "ds_read_b64_tr_b16 v[114:115], v23 offset:4096\n"
    "ds_read_b64_tr_b16 v[116:117], v23 offset:4608\n"
    "ds_read_b64_tr_b16 v[118:119], v22 offset:8192\n"
    "s_mov_b32 s55, 0x3b000000\n"
    "ds_read_b64_tr_b16 v[120:121], v22 offset:8704\n"
    "s_lshl_b32 s56, s7, 6\n"
    "ds_read_b64_tr_b16 v[122:123], v23 offset:8192\n"
    "ds_read_b64_tr_b16 v[124:125], v23 offset:8704\n"
    "s_add_u32 s56, s56, 0x27800\n"
    "ds_read_b64_tr_b16 v[126:127], v22 offset:12288\n"
    "s_lshl_b32 s74, s50, 10\n"
    "ds_read_b64_tr_b16 v[128:129], v22 offset:12800\n"
    "ds_read_b64_tr_b16 v[130:131], v23 offset:12288\n"
    "s_add_u32 s74, s74, 0x1a000\n"
    "ds_read_b64_tr_b16 v[132:133], v23 offset:12800\n"
    "s_lshl_b32 s54, s50, 7\n"
    "s_add_u32 s54, s54, 0x22000\n"
    "v_mov_b32 v100, 1\n"
    "v_mov_b32 v101, 0x7ffffff0\n"
    "s_waitcnt lgkmcnt(14)\n"
    "v_mfma_f32_16x16x32_bf16 v[92:95], v[102:105], v[12:15], 0\n"
    "v_cmp_eq_u32 vcc, 0, v0\n"
    "v_and_b32 v186, 1, v9\n"
    "v_mfma_f32_16x16x32_bf16 v[96:99], v[102:105], v[102:105], 0\n"
    "v_cndmask_b32 v101, v101, 0, vcc\n"
    "buffer_atomic_add v100, v101, s[20:23], s29 offen\n"
    "v_mfma_f32_16x16x32_bf16 v[44:47], v[102:105], a[0:3], 0\n"
    "v_cmp_ne_u32 s[60:61], 0, v186\n"
    "v_and_b32 v186, 2, v9\n"
    "v_mfma_f32_16x16x32_bf16 v[60:63], v[102:105], a[64:67], 0\n"
    "v_cmp_ne_u32 s[62:63], 0, v186\n"
    "v_lshrrev_b32 v186, 2, v9\n"
    "v_mfma_f32_16x16x32_bf16 v[76:79], a[128:131], v[102:105], 0\n"
    "v_cmp_eq_u32 s[64:65], v186, v10\n"
    "v_lshl_add_u32 v31, v9, 2, s56\n"
    "s_waitcnt lgkmcnt(12)\n"
    "v_mfma_f32_16x16x32_bf16 v[48:51], v[106:109], a[0:3], 0\n"
    "v_mov_b32 v186, 0x27e00\n"
    "v_cndmask_b32 v31, v186, v31, s[64:65]\n"
    "v_mfma_f32_16x16x32_bf16 v[64:67], v[106:109], a[64:67], 0\n"
    "v_mfma_f32_16x16x32_bf16 v[80:83], a[128:131], v[106:109], 0\n"
    "s_waitcnt lgkmcnt(10)\n"
    "v_mfma_f32_16x16x32_bf16 v[92:95], v[110:113], v[12:15], v[92:95]\n"
    "v_mfma_f32_16x16x32_bf16 v[96:99], v[110:113], v[110:113], v[96:99]\n"
    "v_mfma_f32_16x16x32_bf16 v[44:47], v[110:113], a[4:7], v[44:47]\n"
    "v_mfma_f32_16x16x32_bf16 v[60:63], v[110:113], a[68:71], v[60:63]\n"
    "v_mfma_f32_16x16x32_bf16 v[76:79], a[132:135], v[110:113], v[76:79]\n"
    "s_waitcnt lgkmcnt(8)\n"
    "v_mfma_f32_16x16x32_bf16 v[48:51], v[114:117], a[4:7], v[48:51]\n"
    "v_mfma_f32_16x16x32_bf16 v[64:67], v[114:117], a[68:71], v[64:67]\n"
    "s_waitcnt vmcnt(9)\n"
    "s_barrier\n"
    "v_mfma_f32_16x16x32_bf16 v[80:83], a[132:135], v[114:117], v[80:83]\n"
    "ds_read_b64_tr_b16 v[134:135], v22 offset:16384\n"
    "ds_read_b64_tr_b16 v[136:137], v22 offset:16896\n"
    "s_waitcnt lgkmcnt(8)\n"
    "v_mfma_f32_16x16x32_bf16 v[92:95], v[118:121], v[12:15], v[92:95]\n"
    "ds_read_b64_tr_b16 v[138:139], v23 offset:16384\n"
    "ds_read_b64_tr_b16 v[140:141], v23 offset:16896\n"
    "v_mfma_f32_16x16x32_bf16 v[96:99], v[118:121], v[118:121], v[96:99]\n"
    "ds_read_b64_tr_b16 v[142:143], v22 offset:20480\n"
    "ds_read_b64_tr_b16 v[144:145], v22 offset:20992\n"
    "v_mfma_f32_16x16x32_bf16 v[44:47], v[118:121], a[8:11], v[44:47]\n"
    "ds_read_b64_tr_b16 v[146:147], v23 offset:20480\n"
    "ds_read_b64_tr_b16 v[148:149], v23 offset:20992\n"
    "v_mfma_f32_16x16x32_bf16 v[60:63], v[118:121], a[72:75], v[60:63]\n"
    "ds_read_b64_tr_b16 v[150:151], v22 offset:24576\n"
    "ds_read_b64_tr_b16 v[152:153], v22 offset:25088\n"
    "v_mfma_f32_16x16x32_bf16 v[76:79], a[136:139], v[118:121], v[76:79]\n"
    "ds_read_b64_tr_b16 v[154:155], v23 offset:24576\n"
    "ds_read_b64_tr_b16 v[156:157], v23 offset:25088\n"
    "s_waitcnt lgkmcnt(15)\n"
    "v_mfma_f32_16x16x32_bf16 v[48:51], v[122:125], a[8:11], v[48:51]\n"
    "ds_read_b64_tr_b16 v[158:159], v22 offset:28672\n"
    "ds_read_b64_tr_b16 v[160:161], v22 offset:29184\n"
    "v_mfma_f32_16x16x32_bf16 v[64:67], v[122:125], a[72:75], v[64:67]\n"
    "ds_read_b64_tr_b16 v[162:163], v23 offset:28672\n"
    "ds_read_b64_tr_b16 v[164:165], v23 offset:29184\n"
    "v_mfma_f32_16x16x32_bf16 v[80:83], a[136:139], v[122:125], v[80:83]\n"
    "s_waitcnt lgkmcnt(15)\n"
    "v_mfma_f32_16x16x32_bf16 v[92:95], v[126:129], v[12:15], v[92:95]\n"
    "v_mfma_f32_16x16x32_bf16 v[96:99], v[126:129], v[126:129], v[96:99]\n"
    "v_mfma_f32_16x16x32_bf16 v[44:47], v[126:129], a[12:15], v[44:47]\n"
    "v_mfma_f32_16x16x32_bf16 v[60:63], v[126:129], a[76:79], v[60:63]\n"
    "v_mfma_f32_16x16x32_bf16 v[76:79], a[140:143], v[126:129], v[76:79]\n"
    "s_waitcnt lgkmcnt(15)\n"
    "v_mfma_f32_16x16x32_bf16 v[48:51], v[130:133], a[12:15], v[48:51]\n"
    "v_mfma_f32_16x16x32_bf16 v[64:67], v[130:133], a[76:79], v[64:67]\n"
    "v_mfma_f32_16x16x32_bf16 v[80:83], a[140:143], v[130:133], v[80:83]\n"
    "s_waitcnt lgkmcnt(14)\n"
    "v_mfma_f32_16x16x32_bf16 v[92:95], v[134:137], v[12:15], v[92:95]\n"
    "v_mfma_f32_16x16x32_bf16 v[96:99], v[134:137], v[134:137], v[96:99]\n"
    "v_mfma_f32_16x16x32_bf16 v[44:47], v[134:137], a[16:19], v[44:47]\n"
    "v_mfma_f32_16x16x32_bf16 v[60:63], v[134:137], a[80:83], v[60:63]\n"
    "v_mfma_f32_16x16x32_bf16 v[76:79], a[144:147], v[134:137], v[76:79]\n"
    "s_waitcnt lgkmcnt(12)\n"
    "v_mfma_f32_16x16x32_bf16 v[48:51], v[138:141], a[16:19], v[48:51]\n"
    "v_mfma_f32_16x16x32_bf16 v[64:67], v[138:141], a[80:83], v[64:67]\n"
    "v_mfma_f32_16x16x32_bf16 v[80:83], a[144:147], v[138:141], v[80:83]\n"
    "s_waitcnt lgkmcnt(10)\n"
    "v_mfma_f32_16x16x32_bf16 v[92:95], v[142:145], v[12:15], v[92:95]\n"
    "v_mfma_f32_16x16x32_bf16 v[96:99], v[142:145], v[142:145], v[96:99]\n"
    "v_mfma_f32_16x16x32_bf16 v[44:47], v[142:145], a[20:23], v[44:47]\n"
    "v_mfma_f32_16x16x32_bf16 v[60:63], v[142:145], a[84:87], v[60:63]\n"
    "v_mfma_f32_16x16x32_bf16 v[76:79], a[148:151], v[142:145], v[76:79]\n"
    "s_waitcnt lgkmcnt(8)\n"
    "v_mfma_f32_16x16x32_bf16 v[48:51], v[146:149], a[20:23], v[48:51]\n"
    "v_mfma_f32_16x16x32_bf16 v[64:67], v[146:149], a[84:87], v[64:67]\n"
    "s_waitcnt vmcnt(5)\n"
    "s_barrier\n"
    "v_mfma_f32_16x16x32_bf16 v[80:83], a[148:151], v[146:149], v[80:83]\n"
    "ds_read_b64_tr_b16 v[166:167], v22 offset:32768\n"
    "ds_read_b64_tr_b16 v[168:169], v22 offset:33280\n"
    "s_waitcnt lgkmcnt(8)\n"
    "v_mfma_f32_16x16x32_bf16 v[92:95], v[150:153], v[12:15], v[92:95]\n"
    "ds_read_b64_tr_b16 v[170:171], v23 offset:32768\n"
    "ds_read_b64_tr_b16 v[172:173], v23 offset:33280\n"
    "v_mfma_f32_16x16x32_bf16 v[96:99], v[150:153], v[150:153], v[96:99]\n"
    "ds_read_b64_tr_b16 v[174:175], v22 offset:36864\n"
    "ds_read_b64_tr_b16 v[176:177], v22 offset:37376\n"
    "v_mfma_f32_16x16x32_bf16 v[44:47], v[150:153], a[24:27], v[44:47]\n"
    "ds_read_b64_tr_b16 v[178:179], v23 offset:36864\n"
    "ds_read_b64_tr_b16 v[180:181], v23 offset:37376\n"
    "v_mfma_f32_16x16x32_bf16 v[60:63], v[150:153], a[88:91], v[60:63]\n"
    "ds_read_b64_tr_b16 v[182:183], v22 offset:40960\n"
    "ds_read_b64_tr_b16 v[184:185], v22 offset:41472\n"
    "v_mfma_f32_16x16x32_bf16 v[76:79], a[152:155], v[150:153], v[76:79]\n"
    "ds_read_b64_tr_b16 v[16:17], v23 offset:40960\n"
    "ds_read_b64_tr_b16 v[18:19], v23 offset:41472\n"
    "s_waitcnt lgkmcnt(15)\n"
    "v_mfma_f32_16x16x32_bf16 v[48:51], v[154:157], a[24:27], v[48:51]\n"
    "ds_read_b64_tr_b16 v[100:101], v22 offset:45056\n"
    "ds_read_b64_tr_b16 v[102:103], v22 offset:45568\n"
    "v_mfma_f32_16x16x32_bf16 v[64:67], v[154:157], a[88:91], v[64:67]\n"
    "ds_read_b64_tr_b16 v[104:105], v23 offset:45056\n"
    "ds_read_b64_tr_b16 v[106:107], v23 offset:45568\n"
    "v_mfma_f32_16x16x32_bf16 v[80:83], a[152:155], v[154:157], v[80:83]\n"
    "s_waitcnt lgkmcnt(15)\n"
    "v_mfma_f32_16x16x32_bf16 v[92:95], v[158:161], v[12:15], v[92:95]\n"
    "v_mfma_f32_16x16x32_bf16 v[96:99], v[158:161], v[158:161], v[96:99]\n"
    "v_mfma_f32_16x16x32_bf16 v[44:47], v[158:161], a[28:31], v[44:47]\n"
    "v_mfma_f32_16x16x32_bf16 v[60:63], v[158:161], a[92:95], v[60:63]\n"
    "v_mfma_f32_16x16x32_bf16 v[76:79], a[156:159], v[158:161], v[76:79]\n"
    "s_waitcnt lgkmcnt(15)\n"
    "v_mfma_f32_16x16x32_bf16 v[48:51], v[162:165], a[28:31], v[48:51]\n"
    "v_mfma_f32_16x16x32_bf16 v[64:67], v[162:165], a[92:95], v[64:67]\n"
    "v_mfma_f32_16x16x32_bf16 v[80:83], a[156:159], v[162:165], v[80:83]\n"
    "s_waitcnt lgkmcnt(14)\n"
    "v_mfma_f32_16x16x32_bf16 v[92:95], v[166:169], v[12:15], v[92:95]\n"
    "v_mfma_f32_16x16x32_bf16 v[96:99], v[166:169], v[166:169], v[96:99]\n"
    "v_mfma_f32_16x16x32_bf16 v[44:47], v[166:169], a[32:35], v[44:47]\n"
    "v_mfma_f32_16x16x32_bf16 v[60:63], v[166:169], a[96:99], v[60:63]\n"
    "v_mfma_f32_16x16x32_bf16 v[76:79], a[160:163], v[166:169], v[76:79]\n"
    "s_waitcnt lgkmcnt(12)\n"
    "v_mfma_f32_16x16x32_bf16 v[48:51], v[170:173], a[32:35], v[48:51]\n"
    "v_mfma_f32_16x16x32_bf16 v[64:67], v[170:173], a[96:99], v[64:67]\n"
    "v_mfma_f32_16x16x32_bf16 v[80:83], a[160:163], v[170:173], v[80:83]\n"
    "s_waitcnt lgkmcnt(10)\n"
    "v_mfma_f32_16x16x32_bf16 v[92:95], v[174:177], v[12:15], v[92:95]\n"
    "v_mfma_f32_16x16x32_bf16 v[96:99], v[174:177], v[174:177], v[96:99]\n"
    "v_mfma_f32_16x16x32_bf16 v[44:47], v[174:177], a[36:39], v[44:47]\n"
    "v_mfma_f32_16x16x32_bf16 v[60:63], v[174:177], a[100:103], v[60:63]\n"
    "v_mfma_f32_16x16x32_bf16 v[76:79], a[164:167], v[174:177], v[76:79]\n"
    "s_waitcnt lgkmcnt(8)\n"
    "v_mfma_f32_16x16x32_bf16 v[48:51], v[178:181], a[36:39], v[48:51]\n"
    "v_mfma_f32_16x16x32_bf16 v[64:67], v[178:181], a[100:103], v[64:67]\n"
    "s_waitcnt vmcnt(1)\n"
    "s_barrier\n"
    "v_mfma_f32_16x16x32_bf16 v[80:83], a[164:167], v[178:181], v[80:83]\n"
    "ds_read_b64_tr_b16 v[108:109], v22 offset:49152\n"
    "ds_read_b64_tr_b16 v[110:111], v22 offset:49664\n"
    "s_waitcnt lgkmcnt(8)\n"
    "v_mfma_f32_16x16x32_bf16 v[92:95], v[182:185], v[12:15], v[92:95]\n"
    "ds_read_b64_tr_b16 v[112:113], v23 offset:49152\n"
    "ds_read_b64_tr_b16 v[114:115], v23 offset:49664\n"
    "v_mfma_f32_16x16x32_bf16 v[96:99], v[182:185], v[182:185], v[96:99]\n"
    "ds_read_b64_tr_b16 v[116:117], v22 offset:53248\n"
    "ds_read_b64_tr_b16 v[118:119], v22 offset:53760\n"
    "v_mfma_f32_16x16x32_bf16 v[44:47], v[182:185], a[40:43], v[44:47]\n"
    "ds_read_b64_tr_b16 v[120:121], v23 offset:53248\n"
    "ds_read_b64_tr_b16 v[122:123], v23 offset:53760\n"
    "v_mfma_f32_16x16x32_bf16 v[60:63], v[182:185], a[104:107], v[60:63]\n"
    "ds_read_b64_tr_b16 v[124:125], v22 offset:57344\n"
    "ds_read_b64_tr_b16 v[126:127], v22 offset:57856\n"
    "v_mfma_f32_16x16x32_bf16 v[76:79], a[168:171], v[182:185], v[76:79]\n"
    "ds_read_b64_tr_b16 v[128:129], v23 offset:57344\n"
    "ds_read_b64_tr_b16 v[130:131], v23 offset:57856\n"
    "s_waitcnt lgkmcnt(15)\n"
    "v_mfma_f32_16x16x32_bf16 v[48:51], v[16:19], a[40:43], v[48:51]\n"
    "ds_read_b64_tr_b16 v[132:133], v22 offset:61440\n"
    "ds_read_b64_tr_b16 v[134:135], v22 offset:61952\n"
    "v_mfma_f32_16x16x32_bf16 v[64:67], v[16:19], a[104:107], v[64:67]\n"
    "ds_read_b64_tr_b16 v[136:137], v23 offset:61440\n"
    "ds_read_b64_tr_b16 v[138:139], v23 offset:61952\n"
    "v_mfma_f32_16x16x32_bf16 v[80:83], a[168:171], v[16:19], v[80:83]\n"
    "ds_read_b64_tr_b16 v[140:141], v24 offset:0\n"
    "ds_read_b64_tr_b16 v[142:143], v24 offset:512\n"
    "s_waitcnt lgkmcnt(15)\n"
    "v_mfma_f32_16x16x32_bf16 v[92:95], v[100:103], v[12:15], v[92:95]\n"
    "ds_read_b64_tr_b16 v[144:145], v24 offset:4096\n"
    "ds_read_b64_tr_b16 v[146:147], v24 offset:4608\n"
    "v_mfma_f32_16x16x32_bf16 v[96:99], v[100:103], v[100:103], v[96:99]\n"
    "ds_read_b64_tr_b16 v[148:149], v24 offset:8192\n"
    "ds_read_b64_tr_b16 v[150:151], v24 offset:8704\n"
    "v_mfma_f32_16x16x32_bf16 v[44:47], v[100:103], a[44:47], v[44:47]\n"
    "ds_read_b64_tr_b16 v[152:153], v24 offset:12288\n"
    "ds_read_b64_tr_b16 v[154:155], v24 offset:12800\n"
    "v_mfma_f32_16x16x32_bf16 v[60:63], v[100:103], a[108:111], v[60:63]\n"
    "ds_read_b64_tr_b16 v[156:157], v24 offset:16384\n"
    "ds_read_b64_tr_b16 v[158:159], v24 offset:16896\n"
    "v_mfma_f32_16x16x32_bf16 v[76:79], a[172:175], v[100:103], v[76:79]\n"
    "ds_read_b64_tr_b16 v[160:161], v24 offset:20480\n"
    "ds_read_b64_tr_b16 v[162:163], v24 offset:20992\n"
    "s_waitcnt lgkmcnt(15)\n"
    "v_mfma_f32_16x16x32_bf16 v[48:51], v[104:107], a[44:47], v[48:51]\n"
    "ds_read_b64_tr_b16 v[164:165], v24 offset:24576\n"
    "ds_read_b64_tr_b16 v[166:167], v24 offset:25088\n"
    "v_mfma_f32_16x16x32_bf16 v[64:67], v[104:107], a[108:111], v[64:67]\n"
    "ds_read_b64_tr_b16 v[168:169], v24 offset:28672\n"
    "ds_read_b64_tr_b16 v[170:171], v24 offset:29184\n"
    "v_mfma_f32_16x16x32_bf16 v[80:83], a[172:175], v[104:107], v[80:83]\n"
    "ds_read_b64_tr_b16 v[172:173], v24 offset:32768\n"
    "ds_read_b64_tr_b16 v[174:175], v24 offset:33280\n"
    "s_waitcnt lgkmcnt(15)\n"
    "v_mfma_f32_16x16x32_bf16 v[92:95], v[108:111], v[12:15], v[92:95]\n"
    "ds_read_b64_tr_b16 v[176:177], v24 offset:36864\n"
    "ds_read_b64_tr_b16 v[178:179], v24 offset:37376\n"
    "v_mfma_f32_16x16x32_bf16 v[96:99], v[108:111], v[108:111], v[96:99]\n"
    "ds_read_b64_tr_b16 v[180:181], v24 offset:40960\n"
    "ds_read_b64_tr_b16 v[182:183], v24 offset:41472\n"
    "v_mfma_f32_16x16x32_bf16 v[44:47], v[108:111], a[48:51], v[44:47]\n"
    "ds_read_b64_tr_b16 v[184:185], v24 offset:45056\n"
    "v_mfma_f32_16x16x32_bf16 v[60:63], v[108:111], a[112:115], v[60:63]\n"
    "v_mfma_f32_16x16x32_bf16 v[76:79], a[176:179], v[108:111], v[76:79]\n"
    "s_waitcnt lgkmcnt(15)\n"
    "v_mfma_f32_16x16x32_bf16 v[48:51], v[112:115], a[48:51], v[48:51]\n"
    "v_mfma_f32_16x16x32_bf16 v[64:67], v[112:115], a[112:115], v[64:67]\n"
    "v_mfma_f32_16x16x32_bf16 v[80:83], a[176:179], v[112:115], v[80:83]\n"
    "s_waitcnt lgkmcnt(15)\n"
    "v_mfma_f32_16x16x32_bf16 v[92:95], v[116:119], v[12:15], v[92:95]\n"
    "v_mfma_f32_16x16x32_bf16 v[96:99], v[116:119], v[116:119], v[96:99]\n"
    "v_mfma_f32_16x16x32_bf16 v[44:47], v[116:119], a[52:55], v[44:47]\n"
    "v_mfma_f32_16x16x32_bf16 v[60:63], v[116:119], a[116:119], v[60:63]\n"
    "v_mfma_f32_16x16x32_bf16 v[76:79], a[180:183], v[116:119], v[76:79]\n"
    "s_waitcnt lgkmcnt(15)\n"
    "v_mfma_f32_16x16x32_bf16 v[48:51], v[120:123], a[52:55], v[48:51]\n"
    "v_mfma_f32_16x16x32_bf16 v[64:67], v[120:123], a[116:119], v[64:67]\n"
    "v_mfma_f32_16x16x32_bf16 v[80:83], a[180:183], v[120:123], v[80:83]\n"
    "s_waitcnt lgkmcnt(15)\n"
    "v_mfma_f32_16x16x32_bf16 v[92:95], v[124:127], v[12:15], v[92:95]\n"
    "v_mfma_f32_16x16x32_bf16 v[96:99], v[124:127], v[124:127], v[96:99]\n"
    "v_mfma_f32_16x16x32_bf16 v[44:47], v[124:127], a[56:59], v[44:47]\n"
    "v_mfma_f32_16x16x32_bf16 v[60:63], v[124:127], a[120:123], v[60:63]\n"
    "v_mfma_f32_16x16x32_bf16 v[76:79], a[184:187], v[124:127], v[76:79]\n"
    "s_waitcnt lgkmcnt(15)\n"
    "v_mfma_f32_16x16x32_bf16 v[48:51], v[128:131], a[56:59], v[48:51]\n"
    "v_mfma_f32_16x16x32_bf16 v[64:67], v[128:131], a[120:123], v[64:67]\n"
    "v_mfma_f32_16x16x32_bf16 v[80:83], a[184:187], v[128:131], v[80:83]\n"
    "Lpg6_%=:\n"
    "s_cbranch_execz Ltb1_1_1_%=\n"
    "s_waitcnt lgkmcnt(15)\n"
    "v_mfma_f32_16x16x32_bf16 v[92:95], v[132:135], v[12:15], v[92:95]\n"
    "v_mfma_f32_16x16x32_bf16 v[96:99], v[132:135], v[132:135], v[96:99]\n"
    "v_mfma_f32_16x16x32_bf16 v[44:47], v[132:135], a[60:63], v[44:47]\n"
    "s_nop 5\n"
    "v_cndmask_b32 v187, v92, v93, s[60:61]\n"
    "v_cndmask_b32 v32, v94, v95, s[60:61]\n"
    "v_mfma_f32_16x16x32_bf16 v[60:63], v[132:135], a[124:127], v[60:63]\n"
    "v_cndmask_b32 v34, v187, v32, s[62:63]\n"
    "v_cndmask_b32 v187, v96, v97, s[60:61]\n"
    "v_mfma_f32_16x16x32_bf16 v[76:79], a[188:191], v[132:135], v[76:79]\n"
    "v_cndmask_b32 v32, v98, v99, s[60:61]\n"
    "v_cndmask_b32 v30, v187, v32, s[62:63]\n"
    "s_waitcnt lgkmcnt(15)\n"
    "v_mfma_f32_16x16x32_bf16 v[48:51], v[136:139], a[60:63], v[48:51]\n"
    "v_mul_f32 v34, 0x3b000000, v34\n"
    "v_mul_f32 v187, v34, v34\n"
    "v_mfma_f32_16x16x32_bf16 v[64:67], v[136:139], a[124:127], v[64:67]\n"
    "v_fma_f32 v30, v30, s55, -v187\n"
    "ds_read_b64_tr_b16 v[186:187], v24 offset:45568\n"
    "v_mfma_f32_16x16x32_bf16 v[80:83], a[188:191], v[136:139], v[80:83]\n"
    "v_max_f32 v30, 0, v30\n"
    "v_cvt_pk_bf16_f32 v34, 0, v34\n"
    "s_waitcnt lgkmcnt(15)\n"
    "v_mfma_f32_16x16x32_bf16 v[52:55], v[140:143], a[0:3], 0\n"
    "v_cvt_pk_bf16_f32 v30, 0, v30\n"
    "v_add_f32 v30, 0x3727c5ac, v30\n"
    "v_mfma_f32_16x16x32_bf16 v[68:71], v[140:143], a[64:67], 0\n"
    "v_cvt_pk_bf16_f32 v30, 0, v30\n"
    "v_sqrt_f32 v30, v30\n"
    "v_mfma_f32_16x16x32_bf16 v[84:87], a[128:131], v[140:143], 0\n"
    "ds_read_b64_tr_b16 v[140:141], v24 offset:49152\n"
    "ds_read_b64_tr_b16 v[142:143], v24 offset:49664\n"
    "s_waitcnt lgkmcnt(15)\n"
    "v_mfma_f32_16x16x32_bf16 v[52:55], v[144:147], a[4:7], v[52:55]\n"
    "v_cvt_pk_bf16_f32 v30, 0, v30\n"
    "v_rcp_f32 v30, v30\n"
    "v_mfma_f32_16x16x32_bf16 v[68:71], v[144:147], a[68:71], v[68:71]\n"
    "ds_write_b32 v31, v30\n"
    "ds_write_b32 v31, v34 offset:256\n"
    "v_mfma_f32_16x16x32_bf16 v[84:87], a[132:135], v[144:147], v[84:87]\n"
    "ds_read_b64_tr_b16 v[144:145], v24 offset:53248\n"
    "ds_read_b64_tr_b16 v[146:147], v24 offset:53760\n"
    "s_waitcnt lgkmcnt(15)\n"
    "v_mfma_f32_16x16x32_bf16 v[52:55], v[148:151], a[8:11], v[52:55]\n"
    "v_mfma_f32_16x16x32_bf16 v[68:71], v[148:151], a[72:75], v[68:71]\n"
    "v_mfma_f32_16x16x32_bf16 v[84:87], a[136:139], v[148:151], v[84:87]\n"
    "ds_read_b64_tr_b16 v[148:149], v24 offset:57344\n"
    "ds_read_b64_tr_b16 v[150:151], v24 offset:57856\n"
    "s_waitcnt lgkmcnt(15)\n"
    "v_mfma_f32_16x16x32_bf16 v[52:55], v[152:155], a[12:15], v[52:55]\n"
    "v_mfma_f32_16x16x32_bf16 v[68:71], v[152:155], a[76:79], v[68:71]\n"
    "v_mfma_f32_16x16x32_bf16 v[84:87], a[140:143], v[152:155], v[84:87]\n"
    "ds_read_b64_tr_b16 v[152:153], v24 offset:61440\n"
    "ds_read_b64_tr_b16 v[154:155], v24 offset:61952\n"
    "s_waitcnt lgkmcnt(6)\n"
    "s_barrier\n"
    "v_mfma_f32_16x16x32_bf16 v[52:55], v[156:159], a[16:19], v[52:55]\n"
    "ds_read_b128 v[104:107], v36\n"
    "ds_read_b128 v[108:111], v36 offset:256\n"
    "v_mfma_f32_16x16x32_bf16 v[68:71], v[156:159], a[80:83], v[68:71]\n"
    "ds_read_b128 v[124:127], v37 offset:256\n"
    "v_mfma_f32_16x16x32_bf16 v[84:87], a[144:147], v[156:159], v[84:87]\n"
    "ds_read_b64_tr_b16 v[156:157], v25 offset:0\n"
    "ds_read_b64_tr_b16 v[158:159], v25 offset:512\n"
    "v_mfma_f32_16x16x32_bf16 v[52:55], v[160:163], a[20:23], v[52:55]\n"
    "v_mfma_f32_16x16x32_bf16 v[68:71], v[160:163], a[84:87], v[68:71]\n"
    "v_mfma_f32_16x16x32_bf16 v[84:87], a[148:151], v[160:163], v[84:87]\n"
    "ds_read_b64_tr_b16 v[160:161], v25 offset:4096\n"
    "ds_read_b64_tr_b16 v[162:163], v25 offset:4608\n"
    "v_mfma_f32_16x16x32_bf16 v[52:55], v[164:167], a[24:27], v[52:55]\n"
    "v_mfma_f32_16x16x32_bf16 v[68:71], v[164:167], a[88:91], v[68:71]\n"
    "v_mfma_f32_16x16x32_bf16 v[84:87], a[152:155], v[164:167], v[84:87]\n"
    "ds_read_b64_tr_b16 v[164:165], v25 offset:8192\n"
    "ds_read_b64_tr_b16 v[166:167], v25 offset:8704\n"
    "v_mfma_f32_16x16x32_bf16 v[52:55], v[168:171], a[28:31], v[52:55]\n"
    "s_waitcnt lgkmcnt(7)\n"
    "v_fma_f32 v116, -v108, v2, v44\n"
    "v_fma_f32 v117, -v109, v2, v45\n"
    "v_mfma_f32_16x16x32_bf16 v[68:71], v[168:171], a[92:95], v[68:71]\n"
    "ds_read_b128 v[42:45], v37\n"
    "v_fma_f32 v118, -v110, v2, v46\n"
    "v_mfma_f32_16x16x32_bf16 v[84:87], a[156:159], v[168:171], v[84:87]\n"
    "ds_read_b64_tr_b16 v[168:169], v25 offset:12288\n"
    "ds_read_b64_tr_b16 v[170:171], v25 offset:12800\n"
    "v_mfma_f32_16x16x32_bf16 v[52:55], v[172:175], a[32:35], v[52:55]\n"
    "v_fma_f32 v119, -v111, v2, v47\n"
    "v_fma_f32 v120, v116, v104, s68\n"
    "v_mfma_f32_16x16x32_bf16 v[68:71], v[172:175], a[96:99], v[68:71]\n"
    "v_fma_f32 v121, v117, v105, s68\n"
    "v_fma_f32 v122, v118, v106, s68\n"
    "v_mfma_f32_16x16x32_bf16 v[84:87], a[160:163], v[172:175], v[84:87]\n"
    "ds_read_b64_tr_b16 v[172:173], v25 offset:16384\n"
    "ds_read_b64_tr_b16 v[174:175], v25 offset:16896\n"
    "v_mfma_f32_16x16x32_bf16 v[52:55], v[176:179], a[36:39], v[52:55]\n"
    "v_fma_f32 v123, v119, v107, s68\n"
    "v_fma_f32 v116, v120, s70, v1\n"
    "v_mfma_f32_16x16x32_bf16 v[68:71], v[176:179], a[100:103], v[68:71]\n"
    "v_fma_f32 v117, v121, s70, v1\n"
    "v_fma_f32 v118, v122, s70, v1\n"
    "v_mfma_f32_16x16x32_bf16 v[84:87], a[164:167], v[176:179], v[84:87]\n"
    "ds_read_b64_tr_b16 v[176:177], v25 offset:20480\n"
    "ds_read_b64_tr_b16 v[178:179], v25 offset:20992\n"
    "v_mfma_f32_16x16x32_bf16 v[52:55], v[180:183], a[40:43], v[52:55]\n"
    "v_fma_f32 v119, v123, s70, v1\n"
    "v_exp_f32 v116, v116 clamp\n"
    "v_mfma_f32_16x16x32_bf16 v[68:71], v[180:183], a[104:107], v[68:71]\n"
    "v_exp_f32 v117, v117 clamp\n"
    "v_mfma_f32_16x16x32_bf16 v[84:87], a[168:171], v[180:183], v[84:87]\n"
    "ds_read_b64_tr_b16 v[180:181], v25 offset:24576\n"
    "ds_read_b64_tr_b16 v[182:183], v25 offset:25088\n"
    "v_mfma_f32_16x16x32_bf16 v[52:55], v[184:187], a[44:47], v[52:55]\n"
    "v_exp_f32 v118, v118 clamp\n"
    "v_mfma_f32_16x16x32_bf16 v[68:71], v[184:187], a[108:111], v[68:71]\n"
    "v_exp_f32 v119, v119 clamp\n"
    "v_mfma_f32_16x16x32_bf16 v[84:87], a[172:175], v[184:187], v[84:87]\n"
    "ds_read_b64_tr_b16 v[184:185], v25 offset:28672\n"
    "ds_read_b64_tr_b16 v[186:187], v25 offset:29184\n"
    "v_mfma_f32_16x16x32_bf16 v[52:55], v[140:143], a[48:51], v[52:55]\n"
    "v_add_f32 v116, s72, v116\n"
    "v_add_f32 v117, s72, v117\n"
    "v_mfma_f32_16x16x32_bf16 v[68:71], v[140:143], a[112:115], v[68:71]\n"
    "v_add_f32 v118, s72, v118\n"
    "v_add_f32 v119, s72, v119\n"
    "v_mfma_f32_16x16x32_bf16 v[84:87], a[176:179], v[140:143], v[84:87]\n"
    "ds_read_b64_tr_b16 v[140:141], v25 offset:32768\n"
    "ds_read_b64_tr_b16 v[142:143], v25 offset:33280\n"
    "v_mfma_f32_16x16x32_bf16 v[52:55], v[144:147], a[52:55], v[52:55]\n"
    "v_max_f32 v116, v120, v116\n"
    "v_max_f32 v117, v121, v117\n"
    "v_mfma_f32_16x16x32_bf16 v[68:71], v[144:147], a[116:119], v[68:71]\n"
    "v_max_f32 v118, v122, v118\n"
    "v_max_f32 v119, v123, v119\n"
    "v_mfma_f32_16x16x32_bf16 v[84:87], a[180:183], v[144:147], v[84:87]\n"
    "ds_read_b64_tr_b16 v[144:145], v25 offset:36864\n"
    "ds_read_b64_tr_b16 v[146:147], v25 offset:37376\n"
    "v_mfma_f32_16x16x32_bf16 v[52:55], v[148:151], a[56:59], v[52:55]\n"
    "v_cvt_pk_bf16_f32 v96, v116, v117\n"
    "v_cvt_pk_bf16_f32 v97, v118, v119\n"
    "v_mfma_f32_16x16x32_bf16 v[68:71], v[148:151], a[120:123], v[68:71]\n"
    "v_fma_f32 v112, -v108, v3, v60\n"
    "v_fma_f32 v113, -v109, v3, v61\n"
    "v_mfma_f32_16x16x32_bf16 v[84:87], a[184:187], v[148:151], v[84:87]\n"
    "ds_read_b64_tr_b16 v[148:149], v25 offset:40960\n"
    "ds_read_b64_tr_b16 v[150:151], v25 offset:41472\n"
    "v_mfma_f32_16x16x32_bf16 v[52:55], v[152:155], a[60:63], v[52:55]\n"
    "v_fma_f32 v114, -v110, v3, v62\n"
    "v_fma_f32 v115, -v111, v3, v63\n"
    "v_mfma_f32_16x16x32_bf16 v[68:71], v[152:155], a[124:127], v[68:71]\n"
    "v_mul_f32 v112, v112, v104\n"
    "v_mul_f32 v113, v113, v105\n"
    "v_mfma_f32_16x16x32_bf16 v[84:87], a[188:191], v[152:155], v[84:87]\n"
    "ds_read_b64_tr_b16 v[152:153], v25 offset:45056\n"
    "ds_read_b64_tr_b16 v[154:155], v25 offset:45568\n"
    "s_waitcnt lgkmcnt(15)\n"
    "v_mfma_f32_16x16x32_bf16 v[56:59], v[156:159], a[0:3], 0\n"
    "v_mul_f32 v114, v114, v106\n"
    "v_mul_f32 v115, v115, v107\n"
    "v_mfma_f32_16x16x32_bf16 v[72:75], v[156:159], a[64:67], 0\n"
    "ds_read_b128 v[104:107], v38 offset:256\n"
    "v_cvt_pk_bf16_f32 v100, v112, v113\n"
    "v_mfma_f32_16x16x32_bf16 v[88:91], a[128:131], v[156:159], 0\n"
    "ds_read_b64_tr_b16 v[156:157], v25 offset:49152\n"
    "ds_read_b64_tr_b16 v[158:159], v25 offset:49664\n"
    "s_waitcnt lgkmcnt(15)\n"
    "v_mfma_f32_16x16x32_bf16 v[56:59], v[160:163], a[4:7], v[56:59]\n"
    "v_cvt_pk_bf16_f32 v101, v114, v115\n"
    "ds_read_b128 v[112:115], v38\n"
    "v_mfma_f32_16x16x32_bf16 v[72:75], v[160:163], a[68:71], v[72:75]\n"
    "v_fma_f32 v132, -v124, v2, v48\n"
    "v_fma_f32 v133, -v125, v2, v49\n"
    "v_mfma_f32_16x16x32_bf16 v[88:91], a[132:135], v[160:163], v[88:91]\n"
    "ds_read_b64_tr_b16 v[160:161], v25 offset:53248\n"
    "ds_read_b64_tr_b16 v[162:163], v25 offset:53760\n"
    "s_waitcnt lgkmcnt(15)\n"
    "v_mfma_f32_16x16x32_bf16 v[56:59], v[164:167], a[8:11], v[56:59]\n"
    "v_fma_f32 v134, -v126, v2, v50\n"
    "v_fma_f32 v135, -v127, v2, v51\n"
    "v_mfma_f32_16x16x32_bf16 v[72:75], v[164:167], a[72:75], v[72:75]\n"
    "s_waitcnt lgkmcnt(15)\n"
    "v_fma_f32 v136, v132, v42, s68\n"
    "v_fma_f32 v137, v133, v43, s68\n"
    "v_mfma_f32_16x16x32_bf16 v[88:91], a[136:139], v[164:167], v[88:91]\n"
    "ds_read_b64_tr_b16 v[164:165], v25 offset:57344\n"
    "ds_read_b64_tr_b16 v[166:167], v25 offset:57856\n"
    "s_waitcnt lgkmcnt(15)\n"
    "v_mfma_f32_16x16x32_bf16 v[56:59], v[168:171], a[12:15], v[56:59]\n"
    "v_fma_f32 v138, v134, v44, s68\n"
    "v_fma_f32 v139, v135, v45, s68\n"
    "v_mfma_f32_16x16x32_bf16 v[72:75], v[168:171], a[76:79], v[72:75]\n"
    "v_fma_f32 v132, v136, s70, v1\n"
    "v_fma_f32 v133, v137, s70, v1\n"
    "v_mfma_f32_16x16x32_bf16 v[88:91], a[140:143], v[168:171], v[88:91]\n"
    "ds_read_b64_tr_b16 v[168:169], v25 offset:61440\n"
    "ds_read_b64_tr_b16 v[170:171], v25 offset:61952\n"
    "s_waitcnt lgkmcnt(15)\n"
    "v_mfma_f32_16x16x32_bf16 v[56:59], v[172:175], a[16:19], v[56:59]\n"
    "v_fma_f32 v134, v138, s70, v1\n"
    "v_fma_f32 v135, v139, s70, v1\n"
    "v_mfma_f32_16x16x32_bf16 v[72:75], v[172:175], a[80:83], v[72:75]\n"
    "v_exp_f32 v132, v132 clamp\n"
    "v_mfma_f32_16x16x32_bf16 v[88:91], a[144:147], v[172:175], v[88:91]\n"
    "v_exp_f32 v133, v133 clamp\n"
    "s_waitcnt lgkmcnt(15)\n"
    "v_mfma_f32_16x16x32_bf16 v[56:59], v[176:179], a[20:23], v[56:59]\n"
    "v_exp_f32 v134, v134 clamp\n"
    "v_mfma_f32_16x16x32_bf16 v[72:75], v[176:179], a[84:87], v[72:75]\n"
    "v_exp_f32 v135, v135 clamp\n"
    "v_mfma_f32_16x16x32_bf16 v[88:91], a[148:151], v[176:179], v[88:91]\n"
    "v_add_f32 v132, s72, v132\n"
    "v_add_f32 v133, s72, v133\n"
    "s_waitcnt lgkmcnt(15)\n"
    "v_mfma_f32_16x16x32_bf16 v[56:59], v[180:183], a[24:27], v[56:59]\n"
    "v_add_f32 v134, s72, v134\n"
    "v_add_f32 v135, s72, v135\n"
    "v_mfma_f32_16x16x32_bf16 v[72:75], v[180:183], a[88:91], v[72:75]\n"
    "v_max_f32 v132, v136, v132\n"
    "v_max_f32 v133, v137, v133\n"
    "v_mfma_f32_16x16x32_bf16 v[88:91], a[152:155], v[180:183], v[88:91]\n"
    "v_max_f32 v134, v138, v134\n"
    "v_max_f32 v135, v139, v135\n"
    "s_waitcnt lgkmcnt(15)\n"
    "v_mfma_f32_16x16x32_bf16 v[56:59], v[184:187], a[28:31], v[56:59]\n"
    "v_cvt_pk_bf16_f32 v98, v132, v133\n"
    "v_cvt_pk_bf16_f32 v99, v134, v135\n"
    "v_mfma_f32_16x16x32_bf16 v[72:75], v[184:187], a[92:95], v[72:75]\n"
    "v_fma_f32 v128, -v124, v3, v64\n"
    "v_fma_f32 v129, -v125, v3, v65\n"
    "v_mfma_f32_16x16x32_bf16 v[92:95], v[12:15], v[96:99], 0\n"
    "v_fma_f32 v130, -v126, v3, v66\n"
    "v_fma_f32 v131, -v127, v3, v67\n"
    "v_mfma_f32_16x16x32_bf16 v[88:91], a[156:159], v[184:187], v[88:91]\n"
    "v_mul_f32 v128, v128, v42\n"
    "v_mul_f32 v129, v129, v43\n"
    "s_waitcnt lgkmcnt(15)\n"
    "v_mfma_f32_16x16x32_bf16 v[56:59], v[140:143], a[32:35], v[56:59]\n"
    "v_mul_f32 v130, v130, v44\n"
    "v_mul_f32 v131, v131, v45\n"
    "v_mfma_f32_16x16x32_bf16 v[72:75], v[140:143], a[96:99], v[72:75]\n"
    "v_cvt_pk_bf16_f32 v102, v128, v129\n"
    "v_cvt_pk_bf16_f32 v103, v130, v131\n"
    "v_mfma_f32_16x16x32_bf16 v[88:91], a[160:163], v[140:143], v[88:91]\n"
    "ds_read_b128 v[140:143], v39\n"
    "s_waitcnt lgkmcnt(10)\n"
    "v_fma_f32 v46, -v104, v2, v52\n"
    "v_mfma_f32_16x16x32_bf16 v[16:19], v[100:103], v[96:99], 0\n"
    "v_fma_f32 v47, -v105, v2, v53\n"
    "v_fma_f32 v48, -v106, v2, v54\n"
    "v_mfma_f32_16x16x32_bf16 v[56:59], v[144:147], a[36:39], v[56:59]\n"
    "v_fma_f32 v49, -v107, v2, v55\n"
    "s_waitcnt lgkmcnt(7)\n"
    "v_fma_f32 v60, v46, v112, s68\n"
    "v_mfma_f32_16x16x32_bf16 v[72:75], v[144:147], a[100:103], v[72:75]\n"
    "v_fma_f32 v61, v47, v113, s68\n"
    "v_fma_f32 v62, v48, v114, s68\n"
    "v_mfma_f32_16x16x32_bf16 v[88:91], a[164:167], v[144:147], v[88:91]\n"
    "ds_read_b128 v[144:147], v39 offset:256\n"
    "v_fma_f32 v63, v49, v115, s68\n"
    "v_mfma_f32_16x16x32_bf16 v[56:59], v[148:151], a[40:43], v[56:59]\n"
    "v_fma_f32 v46, v60, s70, v1\n"
    "v_fma_f32 v47, v61, s70, v1\n"
    "v_mfma_f32_16x16x32_bf16 v[72:75], v[148:151], a[104:107], v[72:75]\n"
    "v_fma_f32 v48, v62, s70, v1\n"
    "v_fma_f32 v49, v63, s70, v1\n"
    "v_mfma_f32_16x16x32_bf16 v[88:91], a[168:171], v[148:151], v[88:91]\n"
    "v_exp_f32 v46, v46 clamp\n"
    "v_mfma_f32_16x16x32_bf16 v[56:59], v[152:155], a[44:47], v[56:59]\n"
    "v_exp_f32 v47, v47 clamp\n"
    "v_mfma_f32_16x16x32_bf16 v[72:75], v[152:155], a[108:111], v[72:75]\n"
    "v_exp_f32 v48, v48 clamp\n"
    "v_mfma_f32_16x16x32_bf16 v[88:91], a[172:175], v[152:155], v[88:91]\n"
    "v_exp_f32 v49, v49 clamp\n"
    "v_mfma_f32_16x16x32_bf16 v[56:59], v[156:159], a[48:51], v[56:59]\n"
    "v_add_f32 v46, s72, v46\n"
    "v_add_f32 v47, s72, v47\n"
    "v_mfma_f32_16x16x32_bf16 v[72:75], v[156:159], a[112:115], v[72:75]\n"
    "v_add_f32 v48, s72, v48\n"
    "v_add_f32 v49, s72, v49\n"
    "v_mfma_f32_16x16x32_bf16 v[88:91], a[176:179], v[156:159], v[88:91]\n"
    "v_max_f32 v46, v60, v46\n"
    "v_max_f32 v47, v61, v47\n"
    "s_waitcnt lgkmcnt(6)\n"
    "v_mfma_f32_16x16x32_bf16 v[56:59], v[160:163], a[52:55], v[56:59]\n"
    "v_max_f32 v48, v62, v48\n"
    "v_max_f32 v49, v63, v49\n"
    "v_mfma_f32_16x16x32_bf16 v[72:75], v[160:163], a[116:119], v[72:75]\n"
    "v_cvt_pk_bf16_f32 v116, v46, v47\n"
    "v_cvt_pk_bf16_f32 v117, v48, v49\n"
    "v_mfma_f32_16x16x32_bf16 v[88:91], a[180:183], v[160:163], v[88:91]\n"
    "v_fma_f32 v108, -v104, v3, v68\n"
    "v_fma_f32 v109, -v105, v3, v69\n"
    "s_waitcnt lgkmcnt(4)\n"
    "v_mfma_f32_16x16x32_bf16 v[56:59], v[164:167], a[56:59], v[56:59]\n"
    "v_fma_f32 v110, -v106, v3, v70\n"
    "v_fma_f32 v111, -v107, v3, v71\n"
    "v_mfma_f32_16x16x32_bf16 v[72:75], v[164:167], a[120:123], v[72:75]\n"
    "v_mul_f32 v108, v108, v112\n"
    "v_mul_f32 v109, v109, v113\n"
    "v_mfma_f32_16x16x32_bf16 v[88:91], a[184:187], v[164:167], v[88:91]\n"
    "v_mul_f32 v110, v110, v114\n"
    "v_mul_f32 v111, v111, v115\n"
    "s_waitcnt lgkmcnt(2)\n"
    "v_mfma_f32_16x16x32_bf16 v[56:59], v[168:171], a[60:63], v[56:59]\n"
    "v_cvt_pk_bf16_f32 v120, v108, v109\n"
    "v_cvt_pk_bf16_f32 v121, v110, v111\n"
    "v_mfma_f32_16x16x32_bf16 v[72:75], v[168:171], a[124:127], v[72:75]\n"
    "v_lshlrev_b32 v165, 2, v8\n"
    "v_add_u32 v165, 0x27900, v165\n"
    "v_mfma_f32_16x16x32_bf16 v[88:91], a[188:191], v[168:171], v[88:91]\n"
    "ds_read_b32 v164, v165\n"
    "s_waitcnt lgkmcnt(1)\n"
    "v_pk_fma_f32 v[152:153], v[144:145], v[2:3], v[56:57] op_sel:[0,0,0] op_sel_hi:[1,0,1] neg_lo:[1,0,0] neg_hi:[1,0,0]\n"
    "v_pk_fma_f32 v[156:157], v[152:153], v[140:141], s[68:69]\n"
    "v_pk_fma_f32 v[152:153], v[156:157], s[70:71], s[70:71] op_sel:[0,0,1] op_sel_hi:[1,0,1]\n"
    "v_pk_fma_f32 v[154:155], v[146:147], v[2:3], v[58:59] op_sel:[0,0,0] op_sel_hi:[1,0,1] neg_lo:[1,0,0] neg_hi:[1,0,0]\n"
    "v_pk_fma_f32 v[158:159], v[154:155], v[142:143], s[68:69]\n"
    "v_pk_fma_f32 v[154:155], v[158:159], s[70:71], s[70:71] op_sel:[0,0,1] op_sel_hi:[1,0,1]\n"
    "v_exp_f32 v160, v152 clamp\n"
    "v_exp_f32 v161, v153 clamp\n"
    "v_exp_f32 v162, v154 clamp\n"
    "v_exp_f32 v163, v155 clamp\n"
    "v_pk_add_f32 v[160:161], v[160:161], s[72:73]\n"
    "v_pk_add_f32 v[162:163], v[162:163], s[72:73]\n"
    "v_max_f32 v160, v156, v160\n"
    "v_max_f32 v161, v157, v161\n"
    "v_max_f32 v162, v158, v162\n"
    "v_max_f32 v163, v159, v163\n"
    "v_cvt_pk_bf16_f32 v118, v160, v161\n"
    "v_cvt_pk_bf16_f32 v119, v162, v163\n"
    "v_pk_fma_f32 v[148:149], v[144:145], v[2:3], v[72:73] op_sel:[0,1,0] op_sel_hi:[1,1,1] neg_lo:[1,0,0] neg_hi:[1,0,0]\n"
    "v_pk_mul_f32 v[148:149], v[148:149], v[140:141]\n"
    "v_mfma_f32_16x16x32_bf16 v[92:95], v[12:15], v[116:119], v[92:95]\n"
    "v_cvt_pk_bf16_f32 v122, v148, v149\n"
    "s_waitcnt lgkmcnt(0)\n"
    "v_cmp_u_f32 s[30:31], v164, v164\n"
    "v_pk_fma_f32 v[150:151], v[146:147], v[2:3], v[74:75] op_sel:[0,1,0] op_sel_hi:[1,1,1] neg_lo:[1,0,0] neg_hi:[1,0,0]\n"
    "s_cmp_lg_u64 s[30:31], 0\n"
    "s_cselect_b32 s29, 1, 0\n"
    "s_andn2_b32 s29, s29, s28\n"
    "v_pk_mul_f32 v[150:151], v[150:151], v[142:143]\n"
    "v_cvt_pk_bf16_f32 v123, v150, v151\n"
    "s_nop 1\n"
    "v_mfma_f32_16x16x32_bf16 v[16:19], v[120:123], v[116:119], v[16:19]\n"
    "s_nop 7\n"
    "v_mov_b32 v93, v16\n"
    "v_mov_b32_dpp v16, v18 row_shr:8 row_mask:0xf bank_mask:0xc\n"
    "v_mov_b32 v94, v17\n"
    "v_mov_b32_dpp v18, v93 row_shl:8 row_mask:0xf bank_mask:0x3\n"
    "v_mov_b32_dpp v17, v19 row_shr:8 row_mask:0xf bank_mask:0xc\n"
    "v_mov_b32_dpp v19, v94 row_shl:8 row_mask:0xf bank_mask:0x3\n"
    "v_mov_b32 v93, v16\n"
    "v_mov_b32_dpp v16, v17 row_shr:4 row_mask:0xf bank_mask:0xa\n"
    "v_mov_b32 v94, v18\n"
    "v_mov_b32_dpp v17, v93 row_shl:4 row_mask:0xf bank_mask:0x5\n"
    "v_mov_b32_dpp v18, v19 row_shr:4 row_mask:0xf bank_mask:0xa\n"
    "v_mov_b32_dpp v19, v94 row_shl:4 row_mask:0xf bank_mask:0x5\n"
    "s_cmp_lg_u32 s29, 0\n"
    "s_cbranch_scc1 Lslow_4_%=\n"
    "buffer_atomic_add_f32 v16, v40, s[20:23], s74 offen offset:0\n"
    "buffer_atomic_add_f32 v17, v40, s[20:23], s74 offen offset:256\n"
    "buffer_atomic_add_f32 v18, v40, s[20:23], s74 offen offset:512\n"
    "buffer_atomic_add_f32 v19, v40, s[20:23], s74 offen offset:768\n"
    "buffer_atomic_add_f32 v92, v41, s[20:23], s54 offen\n"
    "s_lshl_b32 s75, s48, 16\n"
    "s_lshl_b32 s74, s50, 10\n"
    "s_add_u32 s75, s75, s74\n"
    "s_add_u32 s75, s75, 0x300000\n"
    "ds_read2_b32 v[20:21], v26 offset0:0 offset1:64\n"
    "ds_read2_b32 v[30:31], v27 offset0:0 offset1:64\n"
    "ds_read2_b32 v[64:65], v28 offset0:0 offset1:64\n"
    "ds_read2_b32 v[74:75], v29 offset0:0 offset1:64\n"
    "s_waitcnt lgkmcnt(3)\n"
    "v_pk_fma_f32 v[40:41], v[4:5], v[20:21], v[76:77] op_sel:[0,1,0] op_sel_hi:[1,1,1] neg_lo:[1,0,0] neg_hi:[1,0,0]\n"
    "v_pk_fma_f32 v[44:45], v[40:41], v[20:21], s[68:69] op_sel:[0,0,0] op_sel_hi:[1,0,1]\n"
    "v_pk_fma_f32 v[40:41], v[44:45], s[70:71], s[70:71] op_sel:[0,0,1] op_sel_hi:[1,0,1]\n"
    "v_pk_fma_f32 v[42:43], v[6:7], v[20:21], v[78:79] op_sel:[0,1,0] op_sel_hi:[1,1,1] neg_lo:[1,0,0] neg_hi:[1,0,0]\n"
    "v_pk_fma_f32 v[46:47], v[42:43], v[20:21], s[68:69] op_sel:[0,0,0] op_sel_hi:[1,0,1]\n"
    "v_pk_fma_f32 v[42:43], v[46:47], s[70:71], s[70:71] op_sel:[0,0,1] op_sel_hi:[1,0,1]\n"
    "v_exp_f32 v48, v40 clamp\n"
    "v_exp_f32 v49, v41 clamp\n"
    "v_exp_f32 v50, v42 clamp\n"
    "v_exp_f32 v51, v43 clamp\n"
    "v_pk_add_f32 v[48:49], v[48:49], s[72:73]\n"
    "v_pk_add_f32 v[50:51], v[50:51], s[72:73]\n"
    "v_max_f32 v48, v44, v48\n"
    "v_max_f32 v49, v45, v49\n"
    "v_max_f32 v50, v46, v50\n"
    "v_max_f32 v51, v47, v51\n"
    "v_cvt_pk_bf16_f32 v16, v48, v49\n"
    "v_cvt_pk_bf16_f32 v17, v50, v51\n"
    "s_waitcnt lgkmcnt(2)\n"
    "v_pk_fma_f32 v[52:53], v[4:5], v[30:31], v[80:81] op_sel:[0,1,0] op_sel_hi:[1,1,1] neg_lo:[1,0,0] neg_hi:[1,0,0]\n"
    "v_pk_fma_f32 v[56:57], v[52:53], v[30:31], s[68:69] op_sel:[0,0,0] op_sel_hi:[1,0,1]\n"
    "v_pk_fma_f32 v[52:53], v[56:57], s[70:71], s[70:71] op_sel:[0,0,1] op_sel_hi:[1,0,1]\n"
    "v_pk_fma_f32 v[54:55], v[6:7], v[30:31], v[82:83] op_sel:[0,1,0] op_sel_hi:[1,1,1] neg_lo:[1,0,0] neg_hi:[1,0,0]\n"
    "v_pk_fma_f32 v[58:59], v[54:55], v[30:31], s[68:69] op_sel:[0,0,0] op_sel_hi:[1,0,1]\n"
    "v_pk_fma_f32 v[54:55], v[58:59], s[70:71], s[70:71] op_sel:[0,0,1] op_sel_hi:[1,0,1]\n"
    "v_exp_f32 v60, v52 clamp\n"
    "v_exp_f32 v61, v53 clamp\n"
    "v_exp_f32 v62, v54 clamp\n"
    "v_exp_f32 v63, v55 clamp\n"
    "v_pk_add_f32 v[60:61], v[60:61], s[72:73]\n"
    "v_pk_add_f32 v[62:63], v[62:63], s[72:73]\n"
    "v_max_f32 v60, v56, v60\n"
    "v_max_f32 v61, v57, v61\n"
    "v_max_f32 v62, v58, v62\n"
    "v_max_f32 v63, v59, v63\n"
    "v_cvt_pk_bf16_f32 v18, v60, v61\n"
    "v_cvt_pk_bf16_f32 v19, v62, v63\n"
    "s_waitcnt lgkmcnt(1)\n"
    "v_pk_fma_f32 v[66:67], v[4:5], v[64:65], v[84:85] op_sel:[0,1,0] op_sel_hi:[1,1,1] neg_lo:[1,0,0] neg_hi:[1,0,0]\n"
    "v_permlane16_swap_b32 v16, v18\n"
    "v_permlane16_swap_b32 v17, v19\n"
    "buffer_store_dwordx4 v[16:19], v33, s[20:23], s75 offen sc1\n"
    "v_pk_fma_f32 v[70:71], v[66:67], v[64:65], s[68:69] op_sel:[0,0,0] op_sel_hi:[1,0,1]\n"
    "v_pk_fma_f32 v[66:67], v[70:71], s[70:71], s[70:71] op_sel:[0,0,1] op_sel_hi:[1,0,1]\n"
    "v_pk_fma_f32 v[68:69], v[6:7], v[64:65], v[86:87] op_sel:[0,1,0] op_sel_hi:[1,1,1] neg_lo:[1,0,0] neg_hi:[1,0,0]\n"
    "v_pk_fma_f32 v[72:73], v[68:69], v[64:65], s[68:69] op_sel:[0,0,0] op_sel_hi:[1,0,1]\n"
    "v_pk_fma_f32 v[68:69], v[72:73], s[70:71], s[70:71] op_sel:[0,0,1] op_sel_hi:[1,0,1]\n"
    "v_exp_f32 v92, v66 clamp\n"
    "v_exp_f32 v93, v67 clamp\n"
    "v_exp_f32 v94, v68 clamp\n"
    "v_exp_f32 v95, v69 clamp\n"
    "v_pk_add_f32 v[92:93], v[92:93], s[72:73]\n"
    "v_pk_add_f32 v[94:95], v[94:95], s[72:73]\n"
    "v_max_f32 v92, v70, v92\n"
    "v_max_f32 v93, v71, v93\n"
    "v_max_f32 v94, v72, v94\n"
    "v_max_f32 v95, v73, v95\n"
    "v_cvt_pk_bf16_f32 v36, v92, v93\n"
    "v_cvt_pk_bf16_f32 v37, v94, v95\n"
    "s_waitcnt lgkmcnt(0)\n"
    "v_pk_fma_f32 v[96:97], v[4:5], v[74:75], v[88:89] op_sel:[0,1,0] op_sel_hi:[1,1,1] neg_lo:[1,0,0] neg_hi:[1,0,0]\n"
    "v_pk_fma_f32 v[100:101], v[96:97], v[74:75], s[68:69] op_sel:[0,0,0] op_sel_hi:[1,0,1]\n"
    "v_pk_fma_f32 v[96:97], v[100:101], s[70:71], s[70:71] op_sel:[0,0,1] op_sel_hi:[1,0,1]\n"
    "v_pk_fma_f32 v[98:99], v[6:7], v[74:75], v[90:91] op_sel:[0,1,0] op_sel_hi:[1,1,1] neg_lo:[1,0,0] neg_hi:[1,0,0]\n"
    "v_pk_fma_f32 v[102:103], v[98:99], v[74:75], s[68:69] op_sel:[0,0,0] op_sel_hi:[1,0,1]\n"
    "v_pk_fma_f32 v[98:99], v[102:103], s[70:71], s[70:71] op_sel:[0,0,1] op_sel_hi:[1,0,1]\n"
    "v_exp_f32 v104, v96 clamp\n"
    "v_exp_f32 v105, v97 clamp\n"
    "v_exp_f32 v106, v98 clamp\n"
    "v_exp_f32 v107, v99 clamp\n"
    "v_pk_add_f32 v[104:105], v[104:105], s[72:73]\n"
    "v_pk_add_f32 v[106:107], v[106:107], s[72:73]\n"
    "v_max_f32 v104, v100, v104\n"
    "v_max_f32 v105, v101, v105\n"
    "v_max_f32 v106, v102, v106\n"
    "v_max_f32 v107, v103, v107\n"
    "v_cvt_pk_bf16_f32 v38, v104, v105\n"
    "v_cvt_pk_bf16_f32 v39, v106, v107\n"
    "s_nop 0\n"
    "v_permlane16_swap_b32 v36, v38\n"
    "v_permlane16_swap_b32 v37, v39\n"
    "buffer_store_dwordx4 v[36:39], v35, s[20:23], s75 offen sc1\n"
    "s_lshr_b32 s55, s24, 1\n"
    "s_lshl_b32 s55, s55, 16\n"
    "s_lshl_b32 s56, s45, 14\n"
    "s_add_u32 s55, s55, s56\n"
    "s_lshl_b32 s56, s7, 12\n"
    "s_add_u32 s55, s55, s56\n"
    "s_and_b32 s56, s24, 1\n"
    "s_lshl_b32 s56, s56, 6\n"
    "s_add_u32 s52, s55, s56\n"
    "s_add_u32 s55, s52, 0x900000\n"
    "v_and_b32 v16, 1, v9\n"
    "v_lshlrev_b32 v16, 4, v16\n"
    "v_mov_b32 v26, 0x3f80\n"
    "v_lshlrev_b32 v26, v16, v26\n"
    "v_lshrrev_b32 v16, 3, v9\n"
    "v_lshrrev_b32 v17, 1, v10\n"
    "v_cmp_eq_u32 s[60:61], v16, v17\n"
    "v_bfe_u32 v27, v9, 1, 2\n"
    "v_and_b32 v16, 1, v10\n"
    "v_cndmask_b32 v26, 0, v26, s[60:61]\n"
    "v_lshl_add_u32 v27, v16, 2, v27\n"
    "v_cmp_eq_u32 vcc, 0, v27\n"
    "v_lshlrev_b32 v28, 7, v9\n"
    "v_and_b32 v16, 1, v10\n"
    "v_cndmask_b32 v18, 0, v26, vcc\n"
    "v_cmp_eq_u32 vcc, 1, v27\n"
    "v_lshl_add_u32 v28, v16, 5, v28\n"
    "v_lshrrev_b32 v16, 1, v10\n"
    "v_cndmask_b32 v19, 0, v26, vcc\n"
    "v_cmp_eq_u32 vcc, 2, v27\n"
    "v_lshl_add_u32 v28, v16, 4, v28\n"
    "buffer_load_dwordx4 v[30:33], v28, s[20:23], s55 offen sc1\n"
    "buffer_load_dwordx4 v[34:37], v28, s[20:23], s55 offen offset:2048 sc1\n"
    "s_lshl_b32 s55, s7, 10\n"
    "s_add_u32 s55, s55, 0x8000\n"
    "v_cndmask_b32 v20, 0, v26, vcc\n"
    "v_cmp_eq_u32 vcc, 3, v27\n"
    "v_and_b32 v39, 31, v8\n"
    "v_lshrrev_b32 v40, 5, v8\n"
    "v_cndmask_b32 v21, 0, v26, vcc\n"
    "v_cmp_eq_u32 vcc, 4, v27\n"
    "v_lshlrev_b32 v29, 4, v8\n"
    "v_lshl_add_u32 v41, v9, 7, s55\n"
    "s_sub_u32 s55, s7, s66\n"
    "s_and_b32 s55, s55, 3\n"
    "s_lshl_b32 s55, s55, 8\n"
    "v_cndmask_b32 v22, 0, v26, vcc\n"
    "v_cmp_eq_u32 vcc, 5, v27\n"
    "v_lshl_add_u32 v41, v10, 4, v41\n"
    "v_mov_b32 v16, 0x7ffffff0\n"
    "v_cndmask_b32 v23, 0, v26, vcc\n"
    "v_cmp_eq_u32 vcc, 6, v27\n"
    "v_mov_b32 v38, v29\n"
    "s_nop 0\n"
    "v_cndmask_b32 v24, 0, v26, vcc\n"
    "v_cmp_eq_u32 vcc, 7, v27\n"
    "s_nop 1\n"
    "v_cndmask_b32 v25, 0, v26, vcc\n"
    "v_cmp_gt_u32 vcc, 8, v9\n"
    "s_nop 1\n"
    "v_cndmask_b32 v41, v16, v41, vcc\n"
    "v_xor_b32 v16, v40, v9\n"
    "v_lshlrev_b32 v42, 4, v16\n"
    "v_lshl_add_u32 v42, v39, 10, v42\n"
    "v_add_u32 v42, s55, v42\n"
    "v_add_u32 v42, 0x18000, v42\n"
    "v_xor_b32 v16, v10, v9\n"
    "v_lshlrev_b32 v16, 4, v16\n"
    "v_lshl_add_u32 v43, v9, 10, v16\n"
    "v_add_u32 v43, 0x18000, v43\n"
    "v_xor_b32 v44, 64, v43\n"
    "v_xor_b32 v45, 0x80, v43\n"
    "v_xor_b32 v46, 0xc0, v43\n"
    "v_lshrrev_b32 v16, 1, v10\n"
    "v_lshlrev_b32 v47, 14, v16\n"
    "v_bfe_u32 v16, v9, 1, 2\n"
    "v_lshl_add_u32 v47, v16, 10, v47\n"
    "v_and_b32 v16, 1, v10\n"
    "v_lshl_add_u32 v47, v16, 5, v47\n"
    "v_lshrrev_b32 v16, 3, v9\n"
    "v_lshl_add_u32 v47, v16, 4, v47\n"
    "v_and_b32 v16, 1, v9\n"
    "v_lshl_add_u32 v47, v16, 3, v47\n"
    "v_mov_b32 v16, v16\n"
    "s_waitcnt vmcnt(2)\n"
    "s_barrier\n"
    "s_cmp_eq_u32 s7, 0\n"
    "s_cbranch_scc0 Larr_24_%=\n"
    "s_and_b32 s55, s6, 7\n"
    "s_lshl_b32 s55, s55, 8\n"
    "s_add_u32 s55, s55, 0x1000\n"
    "s_add_u32 s56, s44, 3\n"
    "v_mov_b32 v188, 1\n"
    "v_mov_b32 v48, 0x7ffffff0\n"
    "v_cmp_eq_u32 vcc, 0, v8\n"
    "v_mov_b32 v49, s56\n"
    "s_lshl_b32 s56, s48, 3\n"
    "s_add_u32 s56, s56, s49\n"
    "s_lshl_b32 s56, s56, 7\n"
    "s_add_u32 s56, s56, 0x8000\n"
    "v_cndmask_b32 v48, v48, 0, vcc\n"
    "buffer_atomic_add v188, v48, s[20:23], s55 offen sc0\n"
    "buffer_store_dword v49, v48, s[20:23], s56 offen sc1\n"
    "Larr_24_%=:\n"
    "s_nop 0\n"
    "s_sleep 8\n"
    "s_lshr_b32 s55, s24, 1\n"
    "s_lshl_b32 s55, s55, 2\n"
    "s_add_u32 s55, s55, s7\n"
    "s_lshl_b32 s55, s55, 8\n"
    "s_add_u32 s75, s55, 0x8000\n"
    "v_and_b32 v50, 1, v8\n"
    "v_lshlrev_b32 v50, 7, v50\n"
    "buffer_load_dword v51, v50, s[20:23], s75 offen sc1\n"
    "s_cmp_eq_u32 s7, 0\n"
    "s_cbranch_scc0 Lrel_25_%=\n"
    "s_waitcnt vmcnt(1)\n"
    "v_readfirstlane_b32 s55, v188\n"
    "s_nop 0\n"
    "s_cmp_eq_u32 s55, 0x7f\n"
    "s_cbranch_scc0 Lrel_25_%=\n"
    "s_and_b32 s55, s6, 7\n"
    "s_lshl_b32 s55, s55, 8\n"
    "s_add_u32 s55, s55, 0x1800\n"
    "v_lshlrev_b32 v52, 11, v8\n"
    "v_mov_b32 v53, 0x7ffffff0\n"
    "v_cmp_gt_u32 vcc, 8, v8\n"
    "s_nop 1\n"
    "v_cndmask_b32 v52, v53, v52, vcc\n"
    "v_mov_b32 v53, 4\n"
    "buffer_store_dword v53, v52, s[20:23], s55 offen sc1\n"
    "Lrel_25_%=:\n"
    "s_add_u32 s77, s44, 3\n"
    "s_waitcnt vmcnt(0)\n"
    "v_cmp_eq_u32 vcc, s77, v51\n"
    "s_nop 0\n"
    "s_cmp_eq_u64 vcc, exec\n"
    "s_cbranch_scc1 Lpqx_27_%=\n"
    "s_mov_b32 s76, 0\n"
    "Lpqp_26_%=:\n"
    "buffer_load_dword v51, v50, s[20:23], s75 offen sc1\n"
    "s_waitcnt vmcnt(0)\n"
    "v_cmp_eq_u32 vcc, s77, v51\n"
    "s_nop 0\n"
    "s_cmp_eq_u64 vcc, exec\n"
    "s_cbranch_scc1 Lpqx_27_%=\n"
    "s_add_u32 s76, s76, 1\n"
    "s_cmp_lt_u32 s76, 0x8000\n"
    "s_cbranch_scc1 Lpqp_26_%=\n"
    "Lpqx_27_%=:\n"
    "s_lshl_b32 s58, s24, 15\n"
    "s_lshl_b32 s55, s7, 13\n"
    "s_add_u32 s58, s58, s55\n"
    "s_add_u32 s58, s58, 0x300000\n"
    "buffer_load_dwordx4 v[54:57], v38, s[20:23], s58 offen offset:0 sc1\n"
    "buffer_load_dwordx4 v[58:61], v38, s[20:23], s58 offen offset:1024 sc1\n"
    "buffer_load_dwordx4 v[62:65], v38, s[20:23], s58 offen offset:2048 sc1\n"
    "buffer_load_dwordx4 v[66:69], v38, s[20:23], s58 offen offset:3072 sc1\n"
    "s_add_u32 s58, s58, 0x1000\n"
    "buffer_load_dwordx4 v[70:73], v38, s[20:23], s58 offen offset:0 sc1\n"
    "buffer_load_dwordx4 v[74:77], v38, s[20:23], s58 offen offset:1024 sc1\n"
    "buffer_load_dwordx4 v[78:81], v38, s[20:23], s58 offen offset:2048 sc1\n"
    "buffer_load_dwordx4 v[82:85], v38, s[20:23], s58 offen offset:3072 sc1\n"
    "s_cmp_eq_u32 s7, 0\n"
    "s_cbranch_scc0 Lbwe_31_%=\n"
    "s_and_b32 s55, s6, 7\n"
    "s_lshl_b32 s55, s55, 11\n"
    "s_add_u32 s55, s55, 0x1800\n"
    "s_mov_b32 s76, 0\n"
    "s_mov_b32 s77, 4\n"
    "s_mov_b64 s[60:61], exec\n"
    "v_and_b32 v86, 7, v8\n"
    "v_lshlrev_b32 v86, 8, v86\n"
    "s_mov_b64 exec, 0xff\n"
    "s_nop 1\n"
    "buffer_load_dword v188, v86, s[20:23], s55 offen sc1\n"
    "s_sleep 1\n"
    "Lpg7_%=:\n"
    "s_cbranch_execz Ltb1_2_1_%=\n"
    "buffer_load_dword v189, v86, s[20:23], s55 offen sc1\n"
    "s_sleep 1\n"
    "buffer_load_dword v190, v86, s[20:23], s55 offen sc1\n"
    "s_sleep 1\n"
    "Lbwp_29_%=:\n"
    "buffer_load_dword v191, v86, s[20:23], s55 offen sc1\n"
    "s_waitcnt vmcnt(3)\n"
    "v_cmp_le_u32 vcc, s77, v188\n"
    "s_nop 1\n"
    "s_cmp_eq_u64 vcc, exec\n"
    "s_cbranch_scc1 Lbwx_30_%=\n"
    "buffer_load_dword v188, v86, s[20:23], s55 offen sc1\n"
    "s_waitcnt vmcnt(3)\n"
    "v_cmp_le_u32 vcc, s77, v189\n"
    "s_nop 1\n"
    "s_cmp_eq_u64 vcc, exec\n"
    "s_cbranch_scc1 Lbwx_30_%=\n"
    "buffer_load_dword v189, v86, s[20:23], s55 offen sc1\n"
    "s_waitcnt vmcnt(3)\n"
    "v_cmp_le_u32 vcc, s77, v190\n"
    "s_nop 1\n"
    "s_cmp_eq_u64 vcc, exec\n"
    "s_cbranch_scc1 Lbwx_30_%=\n"
    "buffer_load_dword v190, v86, s[20:23], s55 offen sc1\n"
    "s_waitcnt vmcnt(3)\n"
    "v_cmp_le_u32 vcc, s77, v191\n"
    "s_nop 1\n"
    "s_cmp_eq_u64 vcc, exec\n"
    "s_cbranch_scc1 Lbwx_30_%=\n"
    "s_add_u32 s76, s76, 1\n"
    "s_cmp_lt_u32 s76, 0x10000\n"
    "s_cbranch_scc1 Lbwp_29_%=\n"
    "Lbwx_30_%=:\n"
    "s_mov_b64 exec, s[60:61]\n"
    "s_nop 0\n"
    "s_branch Lbw_28_%=\n"
    "Lbwe_31_%=:\n"
    "s_mov_b64 s[60:61], exec\n"
    "s_mov_b64 exec, 0\n"
    "s_cmp_eq_u32 s7, 2\n"
    "s_cbranch_scc1 Ltw4_2_%=\n"
    "s_cmp_eq_u32 s7, 3\n"
    "s_cbranch_scc1 Ltw4_3_%=\n"
    "s_branch Lpg8_%=\n"
    "Ltb4_1_0_%=:\n"
    "s_nop 0\n"
    "Ltb4_1_1_%=:\n"
    "s_branch Ltf4_%=\n"
    "Ltw4_2_%=:\n"
    "s_branch Lpg9_%=\n"
    "Ltb4_2_0_%=:\n"
    "s_nop 0\n"
    "Ltb4_2_1_%=:\n"
    "s_branch Ltf4_%=\n"
    "Ltw4_3_%=:\n"
    "s_nop 0\n"
    "Ltb4_3_0_%=:\n"
    "s_nop 0\n"
    "Ltb4_3_1_%=:\n"
    "Ltf4_%=:\n"
    "s_mov_b64 exec, s[60:61]\n"
    "Lbw_28_%=:\n"
    "s_waitcnt lgkmcnt(0)\n"
    "s_barrier\n"
    "s_lshl_b32 s57, s7, 13\n"
    "s_add_u32 s57, s57, 0x1a000\n"
    "s_mov_b32 s56, 0x1a000\n"
    "buffer_load_dwordx4 v[88:91], v41, s[20:23], s56 offen sc1\n"
    "buffer_load_dwordx4 v[92:95], v29, s[20:23], s57 offen offset:0 sc1\n"
    "buffer_load_dwordx4 v[96:99], v29, s[20:23], s57 offen offset:1024 sc1\n"
    "buffer_load_dwordx4 v[100:103], v29, s[20:23], s57 offen offset:2048 sc1\n"
    "buffer_load_dwordx4 v[104:107], v29, s[20:23], s57 offen offset:3072 sc1\n"
    "s_add_u32 s57, s57, 0x1000\n"
    "buffer_load_dwordx4 v[108:111], v29, s[20:23], s57 offen offset:0 sc1\n"
    "buffer_load_dwordx4 v[112:115], v29, s[20:23], s57 offen offset:1024 sc1\n"
    "buffer_load_dwordx4 v[116:119], v29, s[20:23], s57 offen offset:2048 sc1\n"
    "buffer_load_dwordx4 v[120:123], v29, s[20:23], s57 offen offset:3072 sc1\n"
    "s_xor_b32 s55, s6, 8\n"
    "s_mul_i32 s55, s55, 0x5000\n"
    "s_add_u32 s55, s55, 0x1800000\n"
    "s_lshl_b32 s56, s7, 10\n"
    "s_add_u32 s57, s55, s56\n"
    "s_add_u32 m0, s56, 0x10000\n"
    "s_add_u32 s55, s55, 0x1000\n"
    "v_lshlrev_b32 v87, 4, v8\n"
    "buffer_load_dwordx4 v87, s[20:23], s57 offen sc1 lds\n"
    "s_add_u32 s57, s55, s56\n"
    "s_add_u32 m0, s56, 0x11000\n"
    "s_add_u32 s55, s55, 0x1000\n"
    "buffer_load_dwordx4 v87, s[20:23], s57 offen sc1 lds\n"
    "s_add_u32 s57, s55, s56\n"
    "s_add_u32 m0, s56, 0x12000\n"
    "s_add_u32 s55, s55, 0x1000\n"
    "buffer_load_dwordx4 v87, s[20:23], s57 offen sc1 lds\n"
    "s_add_u32 s57, s55, s56\n"
    "s_add_u32 m0, s56, 0x13000\n"
    "s_add_u32 s55, s55, 0x1000\n"
    "buffer_load_dwordx4 v87, s[20:23], s57 offen sc1 lds\n"
    "s_mov_b32 m0, 0x14000\n"
    "s_nop 0\n"
    "buffer_load_dwordx4 v87, s[20:23], s55 offen sc1 lds\n"
    "v_mfma_f32_16x16x32_bf16 v[38:41], v[18:21], v[30:33], 0\n"
    "s_and_b32 s29, s6, 7\n"
    "s_lshl_b32 s29, s29, 8\n"
    "v_mfma_f32_16x16x32_bf16 v[48:51], v[18:21], v[34:37], 0\n"
    "s_add_u32 s29, s29, 0x1000\n"
    "s_add_u32 s55, s52, 0x700000\n"
    "v_mfma_f32_16x16x32_bf16 v[158:161], v[22:25], v[34:37], 0\n"
    "s_lshr_b32 s56, s24, 4\n"
    "s_lshl_b32 s56, s56, 12\n"
    "s_waitcnt vmcnt(13)\n"
    "v_cvt_pk_bf16_f32 v124, v88, v89\n"
    "v_mfma_f32_16x16x32_bf16 v[86:89], v[22:25], v[30:33], 0\n"
    "v_cvt_pk_bf16_f32 v125, v90, v91\n"
    "s_waitcnt vmcnt(12)\n"
    "v_cvt_pk_bf16_f32 v162, v92, v93\n"
    "v_cvt_pk_bf16_f32 v163, v94, v95\n"
    "v_mov_b32_dpp v164, v124 row_newbcast:0 row_mask:0xf bank_mask:0xf\n"
    "v_mov_b32_dpp v165, v125 row_newbcast:0 row_mask:0xf bank_mask:0xf\n"
    "v_xor_b32 v17, 0, v42\n"
    "v_permlane16_swap_b32 v162, v164\n"
    "v_permlane16_swap_b32 v163, v165\n"
    "s_waitcnt vmcnt(11)\n"
    "v_cvt_pk_bf16_f32 v166, v96, v97\n"
    "v_mfma_f32_32x32x16_bf16 v[126:141], v[162:165], v[54:57], 0\n"
    "v_cvt_pk_bf16_f32 v167, v98, v99\n"
    "v_mov_b32_dpp v168, v124 row_newbcast:1 row_mask:0xf bank_mask:0xf\n"
    "v_mov_b32_dpp v169, v125 row_newbcast:1 row_mask:0xf bank_mask:0xf\n"
    "v_xor_b32 v27, 32, v42\n"
    "v_permlane16_swap_b32 v166, v168\n"
    "v_permlane16_swap_b32 v167, v169\n"
    "s_waitcnt vmcnt(10)\n"
    "v_cvt_pk_bf16_f32 v174, v100, v101\n"
    "v_mfma_f32_32x32x16_bf16 v[142:157], v[166:169], v[58:61], 0\n"
    "s_nop 2\n"
    "v_add_f32 v16, s72, v134\n"
    "v_rcp_f32 v16, v16\n"
    "s_nop 0\n"
    "v_mul_f32 v126, v16, v126\n"
    "v_mul_f32 v127, v16, v127\n"
    "v_mul_f32 v128, v16, v128\n"
    "v_mul_f32 v129, v16, v129\n"
    "v_mul_f32 v130, v16, v130\n"
    "v_mul_f32 v131, v16, v131\n"
    "v_mul_f32 v132, v16, v132\n"
    "v_mul_f32 v133, v16, v133\n"
    "v_cvt_pk_bf16_f32 v52, v126, v127\n"
    "v_cvt_pk_bf16_f32 v53, v128, v129\n"
    "v_cvt_pk_bf16_f32 v54, v130, v131\n"
    "v_cvt_pk_bf16_f32 v55, v132, v133\n"
    "ds_write_b128 v17, v[52:55]\n"
    "v_add_f32 v26, s72, v150\n"
    "v_rcp_f32 v26, v26\n"
    "s_nop 0\n"
    "v_mul_f32 v142, v26, v142\n"
    "v_mul_f32 v143, v26, v143\n"
    "v_mul_f32 v144, v26, v144\n"
    "v_mul_f32 v145, v26, v145\n"
    "v_mul_f32 v146, v26, v146\n"
    "v_mul_f32 v147, v26, v147\n"
    "v_mul_f32 v148, v26, v148\n"
    "v_mul_f32 v149, v26, v149\n"
    "v_cvt_pk_bf16_f32 v170, v142, v143\n"
    "v_cvt_pk_bf16_f32 v171, v144, v145\n"
    "v_cvt_pk_bf16_f32 v172, v146, v147\n"
    "v_cvt_pk_bf16_f32 v173, v148, v149\n"
    "ds_write_b128 v27, v[170:173]\n"
    "v_cvt_pk_bf16_f32 v175, v102, v103\n"
    "v_mov_b32_dpp v176, v124 row_newbcast:2 row_mask:0xf bank_mask:0xf\n"
    "v_mov_b32_dpp v177, v125 row_newbcast:2 row_mask:0xf bank_mask:0xf\n"
    "v_xor_b32 v179, 64, v42\n"
    "v_permlane16_swap_b32 v174, v176\n"
    "v_permlane16_swap_b32 v175, v177\n"
    "s_waitcnt vmcnt(9)\n"
    "v_cvt_pk_bf16_f32 v184, v104, v105\n"
    "v_mfma_f32_32x32x16_bf16 v[126:141], v[174:177], v[62:65], 0\n"
    "v_cvt_pk_bf16_f32 v185, v106, v107\n"
    "v_mov_b32_dpp v186, v124 row_newbcast:3 row_mask:0xf bank_mask:0xf\n"
    "v_mov_b32_dpp v187, v125 row_newbcast:3 row_mask:0xf bank_mask:0xf\n"
    "v_xor_b32 v91, 0x60, v42\n"
    "v_permlane16_swap_b32 v184, v186\n"
    "v_permlane16_swap_b32 v185, v187\n"
    "s_waitcnt vmcnt(8)\n"
    "v_cvt_pk_bf16_f32 v170, v108, v109\n"
    "v_mfma_f32_32x32x16_bf16 v[142:157], v[184:187], v[66:69], 0\n"
    "s_nop 2\n"
    "v_add_f32 v178, s72, v134\n"
    "v_rcp_f32 v178, v178\n"
    "s_waitcnt lgkmcnt(0)\n"
    "s_barrier\n"
    "ds_read_b128 v[34:37], v43 offset:0\n"
    "ds_read_b128 v[92:95], v43 offset:16384\n"
    "ds_read_b128 v[162:165], v43 offset:256\n"
    "v_mul_f32 v126, v178, v126\n"
    "v_mul_f32 v127, v178, v127\n"
    "v_mul_f32 v128, v178, v128\n"
    "ds_read_b128 v[56:59], v43 offset:16640\n"
    "v_mul_f32 v129, v178, v129\n"
    "v_mul_f32 v130, v178, v130\n"
    "ds_read_b128 v[52:55], v43 offset:512\n"
    "v_mul_f32 v131, v178, v131\n"
    "v_mul_f32 v132, v178, v132\n"
    "v_mul_f32 v133, v178, v133\n"
    "ds_read_b128 v[96:99], v43 offset:16896\n"
    "v_cvt_pk_bf16_f32 v180, v126, v127\n"
    "v_cvt_pk_bf16_f32 v181, v128, v129\n"
    "ds_read_b128 v[166:169], v43 offset:768\n"
    "v_cvt_pk_bf16_f32 v182, v130, v131\n"
    "v_cvt_pk_bf16_f32 v183, v132, v133\n"
    "v_add_f32 v90, s72, v150\n"
    "ds_read_b128 v[60:63], v43 offset:17152\n"
    "v_rcp_f32 v90, v90\n"
    "ds_write_b128 v179, v[180:183]\n"
    "v_mul_f32 v142, v90, v142\n"
    "s_waitcnt lgkmcnt(8)\n"
    "v_mfma_f32_16x16x32_bf16 v[38:41], v[34:37], a[192:195], v[38:41]\n"
    "v_mul_f32 v143, v90, v143\n"
    "v_mul_f32 v144, v90, v144\n"
    "v_mfma_f32_16x16x32_bf16 v[48:51], v[34:37], v[192:195], v[48:51]\n"
    "v_mul_f32 v145, v90, v145\n"
    "v_mul_f32 v146, v90, v146\n"
    "s_waitcnt lgkmcnt(7)\n"
    "v_mfma_f32_16x16x32_bf16 v[86:89], v[92:95], a[192:195], v[86:89]\n"
    "v_mul_f32 v147, v90, v147\n"
    "v_mul_f32 v148, v90, v148\n"
    "v_mfma_f32_16x16x32_bf16 v[158:161], v[92:95], v[192:195], v[158:161]\n"
    "v_mul_f32 v149, v90, v149\n"
    "v_cvt_pk_bf16_f32 v30, v142, v143\n"
    "s_waitcnt lgkmcnt(6)\n"
    "v_mfma_f32_16x16x32_bf16 v[38:41], v[162:165], a[208:211], v[38:41]\n"
    "v_cvt_pk_bf16_f32 v31, v144, v145\n"
    "v_cvt_pk_bf16_f32 v32, v146, v147\n"
    "v_mfma_f32_16x16x32_bf16 v[48:51], v[162:165], v[208:211], v[48:51]\n"
    "v_cvt_pk_bf16_f32 v33, v148, v149\n"
    "ds_write_b128 v91, v[30:33]\n"
    "s_waitcnt lgkmcnt(6)\n"
    "v_mfma_f32_16x16x32_bf16 v[86:89], v[56:59], a[208:211], v[86:89]\n"
    "v_cvt_pk_bf16_f32 v171, v110, v111\n"
    "v_mov_b32_dpp v172, v124 row_newbcast:4 row_mask:0xf bank_mask:0xf\n"
    "v_mfma_f32_16x16x32_bf16 v[158:161], v[56:59], v[208:211], v[158:161]\n"
    "v_mov_b32_dpp v173, v125 row_newbcast:4 row_mask:0xf bank_mask:0xf\n"
    "v_permlane16_swap_b32 v170, v172\n"
    "s_waitcnt lgkmcnt(5)\n"
    "v_mfma_f32_16x16x32_bf16 v[38:41], v[52:55], a[224:227], v[38:41]\n"
    "v_permlane16_swap_b32 v171, v173\n"
    "v_mfma_f32_16x16x32_bf16 v[48:51], v[52:55], v[224:227], v[48:51]\n"
    "v_xor_b32 v17, 0x80, v42\n"
    "s_waitcnt vmcnt(7)\n"
    "v_cvt_pk_bf16_f32 v174, v112, v113\n"
    "s_waitcnt lgkmcnt(4)\n"
    "v_mfma_f32_16x16x32_bf16 v[86:89], v[96:99], a[224:227], v[86:89]\n"
    "v_cvt_pk_bf16_f32 v175, v114, v115\n"
    "v_mov_b32_dpp v176, v124 row_newbcast:5 row_mask:0xf bank_mask:0xf\n"
    "v_mfma_f32_16x16x32_bf16 v[158:161], v[96:99], v[224:227], v[158:161]\n"
    "v_mov_b32_dpp v177, v125 row_newbcast:5 row_mask:0xf bank_mask:0xf\n"
    "v_permlane16_swap_b32 v174, v176\n"
    "s_waitcnt lgkmcnt(3)\n"
    "v_mfma_f32_16x16x32_bf16 v[38:41], v[166:169], a[240:243], v[38:41]\n"
    "s_waitcnt lgkmcnt(0)\n"
    "s_barrier\n"
    "v_mfma_f32_16x16x32_bf16 v[48:51], v[166:169], v[240:243], v[48:51]\n"
    "ds_read_b128 v[178:181], v44 offset:0\n"
    "ds_read_b128 v[182:185], v44 offset:16384\n"
    "v_mfma_f32_16x16x32_bf16 v[86:89], v[60:63], a[240:243], v[86:89]\n"
    "ds_read_b128 v[104:107], v44 offset:256\n"
    "v_permlane16_swap_b32 v175, v177\n"
    "v_mfma_f32_16x16x32_bf16 v[158:161], v[60:63], v[240:243], v[158:161]\n"
    "ds_read_b128 v[90:93], v44 offset:512\n"
    "ds_read_b128 v[30:33], v44 offset:16896\n"
    "v_mfma_f32_32x32x16_bf16 v[126:141], v[170:173], v[70:73], 0\n"
    "ds_read_b128 v[68:71], v44 offset:16640\n"
    "v_xor_b32 v27, 0xa0, v42\n"
    "s_waitcnt vmcnt(6)\n"
    "v_cvt_pk_bf16_f32 v162, v116, v117\n"
    "ds_read_b128 v[34:37], v44 offset:768\n"
    "v_cvt_pk_bf16_f32 v163, v118, v119\n"
    "v_mfma_f32_32x32x16_bf16 v[142:157], v[174:177], v[74:77], 0\n"
    "ds_read_b128 v[94:97], v44 offset:17152\n"
    "v_mov_b32_dpp v164, v124 row_newbcast:6 row_mask:0xf bank_mask:0xf\n"
    "v_mov_b32_dpp v165, v125 row_newbcast:6 row_mask:0xf bank_mask:0xf\n"
    "s_nop 1\n"
    "v_add_f32 v16, s72, v134\n"
    "v_rcp_f32 v16, v16\n"
    "s_waitcnt lgkmcnt(7)\n"
    "v_mfma_f32_16x16x32_bf16 v[38:41], v[178:181], a[196:199], v[38:41]\n"
    "v_mul_f32 v126, v16, v126\n"
    "v_mul_f32 v127, v16, v127\n"
    "v_mfma_f32_16x16x32_bf16 v[48:51], v[178:181], v[196:199], v[48:51]\n"
    "v_mul_f32 v128, v16, v128\n"
    "v_mul_f32 v129, v16, v129\n"
    "s_waitcnt lgkmcnt(6)\n"
    "v_mfma_f32_16x16x32_bf16 v[86:89], v[182:185], a[196:199], v[86:89]\n"
    "v_mul_f32 v130, v16, v130\n"
    "v_mul_f32 v131, v16, v131\n"
    "v_mfma_f32_16x16x32_bf16 v[158:161], v[182:185], v[196:199], v[158:161]\n"
    "v_mul_f32 v132, v16, v132\n"
    "v_mul_f32 v133, v16, v133\n"
    "s_waitcnt lgkmcnt(5)\n"
    "v_mfma_f32_16x16x32_bf16 v[38:41], v[104:107], a[212:215], v[38:41]\n"
    "v_cvt_pk_bf16_f32 v100, v126, v127\n"
    "v_cvt_pk_bf16_f32 v101, v128, v129\n"
    "v_mfma_f32_16x16x32_bf16 v[48:51], v[104:107], v[212:215], v[48:51]\n"
    "v_cvt_pk_bf16_f32 v102, v130, v131\n"
    "v_cvt_pk_bf16_f32 v103, v132, v133\n"
    "s_waitcnt lgkmcnt(2)\n"
    "v_mfma_f32_16x16x32_bf16 v[86:89], v[68:71], a[212:215], v[86:89]\n"
    "ds_write_b128 v17, v[100:103]\n"
    "v_add_f32 v26, s72, v150\n"
    "v_mfma_f32_16x16x32_bf16 v[158:161], v[68:71], v[212:215], v[158:161]\n"
    "v_rcp_f32 v26, v26\n"
    "v_mfma_f32_16x16x32_bf16 v[38:41], v[90:93], a[228:231], v[38:41]\n"
    "v_mul_f32 v142, v26, v142\n"
    "v_mul_f32 v143, v26, v143\n"
    "v_mfma_f32_16x16x32_bf16 v[48:51], v[90:93], v[228:231], v[48:51]\n"
    "v_mul_f32 v144, v26, v144\n"
    "v_mul_f32 v145, v26, v145\n"
    "v_mfma_f32_16x16x32_bf16 v[86:89], v[30:33], a[228:231], v[86:89]\n"
    "v_mul_f32 v146, v26, v146\n"
    "v_mul_f32 v147, v26, v147\n"
    "v_mfma_f32_16x16x32_bf16 v[158:161], v[30:33], v[228:231], v[158:161]\n"
    "v_mul_f32 v148, v26, v148\n"
    "v_mul_f32 v149, v26, v149\n"
    "s_waitcnt lgkmcnt(2)\n"
    "v_mfma_f32_16x16x32_bf16 v[38:41], v[34:37], a[244:247], v[38:41]\n"
    "v_cvt_pk_bf16_f32 v64, v142, v143\n"
    "v_cvt_pk_bf16_f32 v65, v144, v145\n"
    "v_mfma_f32_16x16x32_bf16 v[48:51], v[34:37], v[244:247], v[48:51]\n"
    "v_cvt_pk_bf16_f32 v66, v146, v147\n"
    "v_cvt_pk_bf16_f32 v67, v148, v149\n"
    "s_waitcnt lgkmcnt(1)\n"
    "v_mfma_f32_16x16x32_bf16 v[86:89], v[94:97], a[244:247], v[86:89]\n"
    "ds_write_b128 v27, v[64:67]\n"
    "v_permlane16_swap_b32 v162, v164\n"
    "v_mfma_f32_16x16x32_bf16 v[158:161], v[94:97], v[244:247], v[158:161]\n"
    "v_permlane16_swap_b32 v163, v165\n"
    "v_xor_b32 v187, 0xc0, v42\n"
    "s_nop 0\n"
    "v_mfma_f32_32x32x16_bf16 v[126:141], v[162:165], v[78:81], 0\n"
    "s_waitcnt vmcnt(5)\n"
    "v_cvt_pk_bf16_f32 v52, v120, v121\n"
    "v_cvt_pk_bf16_f32 v53, v122, v123\n"
    "v_mov_b32_dpp v54, v124 row_newbcast:7 row_mask:0xf bank_mask:0xf\n"
    "v_mov_b32_dpp v55, v125 row_newbcast:7 row_mask:0xf bank_mask:0xf\n"
    "v_xor_b32 v99, 0xe0, v42\n"
    "v_permlane16_swap_b32 v52, v54\n"
    "v_permlane16_swap_b32 v53, v55\n"
    "v_mov_b32 v188, 1\n"
    "s_nop 0\n"
    "v_mfma_f32_32x32x16_bf16 v[142:157], v[52:55], v[82:85], 0\n"
    "s_nop 0\n"
    "v_add_f32 v186, s72, v134\n"
    "v_rcp_f32 v186, v186\n"
    "s_nop 0\n"
    "v_mul_f32 v126, v186, v126\n"
    "s_waitcnt lgkmcnt(0)\n"
    "s_barrier\n"
    "ds_read_b128 v[60:63], v45 offset:0\n"
    "ds_read_b128 v[108:111], v45 offset:16384\n"
    "ds_read_b128 v[170:173], v45 offset:256\n"
    "v_mul_f32 v127, v186, v127\n"
    "v_mul_f32 v128, v186, v128\n"
    "v_mul_f32 v129, v186, v129\n"
    "ds_read_b128 v[72:75], v45 offset:16640\n"
    "v_mul_f32 v130, v186, v130\n"
    "v_mul_f32 v131, v186, v131\n"
    "ds_read_b128 v[100:103], v45 offset:512\n"
    "v_mul_f32 v132, v186, v132\n"
    "v_mul_f32 v133, v186, v133\n"
    "v_cvt_pk_bf16_f32 v56, v126, v127\n"
    "ds_read_b128 v[112:115], v45 offset:16896\n"
    "v_cvt_pk_bf16_f32 v57, v128, v129\n"
    "v_cvt_pk_bf16_f32 v58, v130, v131\n"
    "ds_read_b128 v[174:177], v45 offset:768\n"
    "v_cvt_pk_bf16_f32 v59, v132, v133\n"
    "v_add_f32 v98, s72, v150\n"
    "v_rcp_f32 v98, v98\n"
    "ds_read_b128 v[76:79], v45 offset:17152\n"
    "v_mul_f32 v142, v98, v142\n"
    "ds_write_b128 v187, v[56:59]\n"
    "v_mul_f32 v143, v98, v143\n"
    "s_waitcnt lgkmcnt(8)\n"
    "v_mfma_f32_16x16x32_bf16 v[38:41], v[60:63], a[200:203], v[38:41]\n"
    "v_mul_f32 v144, v98, v144\n"
    "v_mul_f32 v145, v98, v145\n"
    "v_mfma_f32_16x16x32_bf16 v[48:51], v[60:63], v[200:203], v[48:51]\n"
    "v_mul_f32 v146, v98, v146\n"
    "v_mul_f32 v147, v98, v147\n"
    "s_waitcnt lgkmcnt(7)\n"
    "v_mfma_f32_16x16x32_bf16 v[86:89], v[108:111], a[200:203], v[86:89]\n"
    "v_mul_f32 v148, v98, v148\n"
    "v_mul_f32 v149, v98, v149\n"
    "v_mfma_f32_16x16x32_bf16 v[158:161], v[108:111], v[200:203], v[158:161]\n"
    "v_cvt_pk_bf16_f32 v166, v142, v143\n"
    "v_cvt_pk_bf16_f32 v167, v144, v145\n"
    "s_waitcnt lgkmcnt(6)\n"
    "v_mfma_f32_16x16x32_bf16 v[38:41], v[170:173], a[216:219], v[38:41]\n"
    "v_cvt_pk_bf16_f32 v168, v146, v147\n"
    "v_cvt_pk_bf16_f32 v169, v148, v149\n"
    "v_mfma_f32_16x16x32_bf16 v[48:51], v[170:173], v[216:219], v[48:51]\n"
    "ds_write_b128 v99, v[166:169]\n"
    "v_mov_b32 v16, 0x7ffffff0\n"
    "s_waitcnt lgkmcnt(6)\n"
    "v_mfma_f32_16x16x32_bf16 v[86:89], v[72:75], a[216:219], v[86:89]\n"
    "v_cmp_eq_u32 vcc, 0, v0\n"
    "v_mov_b32 v95, 0x7f800000\n"
    "v_mfma_f32_16x16x32_bf16 v[158:161], v[72:75], v[216:219], v[158:161]\n"
    "v_cndmask_b32 v16, v16, 0, vcc\n"
    "s_waitcnt lgkmcnt(5)\n"
    "v_mfma_f32_16x16x32_bf16 v[38:41], v[100:103], a[232:235], v[38:41]\n"
    "v_mfma_f32_16x16x32_bf16 v[48:51], v[100:103], v[232:235], v[48:51]\n"
    "s_waitcnt lgkmcnt(4)\n"
    "v_mfma_f32_16x16x32_bf16 v[86:89], v[112:115], a[232:235], v[86:89]\n"
    "v_mfma_f32_16x16x32_bf16 v[158:161], v[112:115], v[232:235], v[158:161]\n"
    "s_waitcnt lgkmcnt(3)\n"
    "v_mfma_f32_16x16x32_bf16 v[38:41], v[174:177], a[248:251], v[38:41]\n"
    "s_waitcnt lgkmcnt(0)\n"
    "s_barrier\n"
    "buffer_atomic_add v188, v16, s[20:23], s29 offen sc0\n"
    "v_mfma_f32_16x16x32_bf16 v[48:51], v[174:177], v[248:251], v[48:51]\n"
    "ds_read_b128 v[64:67], v46 offset:0\n"
    "ds_read_b128 v[178:181], v46 offset:16384\n"
    "v_mfma_f32_16x16x32_bf16 v[86:89], v[76:79], a[248:251], v[86:89]\n"
    "ds_read_b128 v[182:185], v46 offset:256\n"
    "ds_read_b128 v[104:107], v46 offset:16640\n"
    "v_mfma_f32_16x16x32_bf16 v[158:161], v[76:79], v[248:251], v[158:161]\n"
    "ds_read_b128 v[68:71], v46 offset:512\n"
    "ds_read_b128 v[90:93], v46 offset:16896\n"
    "ds_read_b128 v[30:33], v46 offset:768\n"
    "ds_read_b128 v[34:37], v46 offset:17152\n"
    "s_waitcnt lgkmcnt(7)\n"
    "v_mfma_f32_16x16x32_bf16 v[38:41], v[64:67], a[204:207], v[38:41]\n"
    "v_mfma_f32_16x16x32_bf16 v[48:51], v[64:67], v[204:207], v[48:51]\n"
    "s_waitcnt lgkmcnt(6)\n"
    "v_mfma_f32_16x16x32_bf16 v[86:89], v[178:181], a[204:207], v[86:89]\n"
    "v_mfma_f32_16x16x32_bf16 v[158:161], v[178:181], v[204:207], v[158:161]\n"
    "s_waitcnt lgkmcnt(5)\n"
    "v_mfma_f32_16x16x32_bf16 v[38:41], v[182:185], a[220:223], v[38:41]\n"
    "v_mfma_f32_16x16x32_bf16 v[48:51], v[182:185], v[220:223], v[48:51]\n"
    "s_waitcnt lgkmcnt(4)\n"
    "v_mfma_f32_16x16x32_bf16 v[86:89], v[104:107], a[220:223], v[86:89]\n"
    "v_mfma_f32_16x16x32_bf16 v[158:161], v[104:107], v[220:223], v[158:161]\n"
    "s_waitcnt lgkmcnt(3)\n"
    "v_mfma_f32_16x16x32_bf16 v[38:41], v[68:71], a[236:239], v[38:41]\n"
    "v_mfma_f32_16x16x32_bf16 v[48:51], v[68:71], v[236:239], v[48:51]\n"
    "s_waitcnt lgkmcnt(2)\n"
    "v_mfma_f32_16x16x32_bf16 v[86:89], v[90:93], a[236:239], v[86:89]\n"
    "v_mfma_f32_16x16x32_bf16 v[158:161], v[90:93], v[236:239], v[158:161]\n"
    "s_waitcnt lgkmcnt(1)\n"
    "v_mfma_f32_16x16x32_bf16 v[38:41], v[30:33], a[252:255], v[38:41]\n"
    "v_mfma_f32_16x16x32_bf16 v[48:51], v[30:33], v[252:255], v[48:51]\n"
    "s_waitcnt lgkmcnt(0)\n"
    "v_mfma_f32_16x16x32_bf16 v[86:89], v[34:37], a[252:255], v[86:89]\n"
    "s_nop 4\n"
    "v_cvt_pk_bf16_f32 v96, v38, v39\n"
    "v_cvt_pk_bf16_f32 v97, v40, v41\n"
    "v_mfma_f32_16x16x32_bf16 v[158:161], v[34:37], v[252:255], v[158:161]\n"
    "v_cvt_pk_bf16_f32 v116, v48, v49\n"
    "v_cvt_pk_bf16_f32 v117, v50, v51\n"
    "v_cvt_pk_bf16_f32 v98, v86, v87\n"
    "v_cvt_pk_bf16_f32 v99, v88, v89\n"
    "s_nop 0\n"
    "v_permlane16_swap_b32 v96, v98\n"
    "v_permlane16_swap_b32 v97, v99\n"
    "buffer_store_dwordx4 v[96:99], v28, s[20:23], s55 offen offset:0 sc1\n"
    "s_nop 0\n"
    "v_mfma_f32_16x16x32_bf16 v[164:167], v[96:99], v[18:21], 0\n"
    "v_cvt_pk_bf16_f32 v118, v158, v159\n"
    "v_cvt_pk_bf16_f32 v119, v160, v161\n"
    "v_mfma_f32_16x16x32_bf16 v[56:59], v[96:99], v[22:25], 0\n"
    "v_permlane16_swap_b32 v116, v118\n"
    "v_permlane16_swap_b32 v117, v119\n"
    "buffer_store_dwordx4 v[116:119], v28, s[20:23], s55 offen offset:2048 sc1\n"
    "s_nop 0\n"
    "v_mfma_f32_16x16x32_bf16 v[80:83], v[116:119], v[18:21], 0\n"
    "s_and_b32 s55, s24, 15\n"
    "s_lshl_b32 s55, s55, 6\n"
    "v_mfma_f32_16x16x32_bf16 v[120:123], v[116:119], v[22:25], 0\n"
    "s_add_u32 s56, s56, s55\n"
    "s_add_u32 s56, s56, 0x38000\n"
    "v_pk_add_f32 v[162:163], v[164:165], v[166:167]\n"
    "v_pk_mul_f32 v[186:187], v[164:165], v[164:165]\n"
    "s_nop 0\n"
    "v_pk_add_f32 v[162:163], v[162:163], v[80:81]\n"
    "v_pk_fma_f32 v[186:187], v[166:167], v[166:167], v[186:187]\n"
    "v_pk_add_f32 v[162:163], v[162:163], v[82:83]\n"
    "v_pk_fma_f32 v[186:187], v[80:81], v[80:81], v[186:187]\n"
    "v_pk_fma_f32 v[186:187], v[82:83], v[82:83], v[186:187]\n"
    "v_add_f32 v17, v162, v163\n"
    "v_add_f32 v27, v186, v187\n"
    "v_pk_add_f32 v[52:53], v[56:57], v[58:59]\n"
    "v_pk_mul_f32 v[54:55], v[56:57], v[56:57]\n"
    "v_pk_add_f32 v[52:53], v[52:53], v[120:121]\n"
    "v_pk_fma_f32 v[54:55], v[58:59], v[58:59], v[54:55]\n"
    "v_pk_add_f32 v[52:53], v[52:53], v[122:123]\n"
    "v_pk_fma_f32 v[54:55], v[120:121], v[120:121], v[54:55]\n"
    "v_pk_fma_f32 v[54:55], v[122:123], v[122:123], v[54:55]\n"
    "v_add_f32 v26, v52, v53\n"
    "v_add_f32 v94, v54, v55\n"
    "v_permlane32_swap_b32 v17, v27\n"
    "v_add_f32 v17, v17, v27\n"
    "v_permlane32_swap_b32 v26, v94\n"
    "v_add_f32 v26, v26, v94\n"
    "s_nop 1\n"
    "v_permlane16_swap_b32 v17, v26\n"
    "v_add_f32 v17, v17, v26\n"
    "v_cmp_gt_f32 vcc, s40, |v17|\n"
    "s_nop 1\n"
    "v_cndmask_b32 v17, v95, v17, vcc\n"
    "v_cvt_f64_f32 v[84:85], v17\n"
    "v_add_f64 v[84:85], v[84:85], s[36:37]\n"
    "buffer_atomic_add_f64 v[84:85], v47, s[20:23], s56 offen\n"
    "s_cmp_eq_u32 s7, 0\n"
    "s_cbranch_scc0 Lrel_32_%=\n"
    "s_waitcnt vmcnt(3)\n"
    "v_readfirstlane_b32 s55, v188\n"
    "s_nop 0\n"
    "s_cmp_eq_u32 s55, 0x9f\n"
    "s_cbranch_scc0 Lrel_32_%=\n"
    "s_and_b32 s55, s6, 7\n"
    "s_lshl_b32 s55, s55, 8\n"
    "s_add_u32 s55, s55, 0x1800\n"
    "v_lshlrev_b32 v168, 11, v8\n"
    "v_mov_b32 v169, 0x7ffffff0\n"
    "v_cmp_gt_u32 vcc, 8, v8\n"
    "s_nop 1\n"
    "v_cndmask_b32 v168, v169, v168, vcc\n"
    "v_mov_b32 v169, 5\n"
    "buffer_store_dword v169, v168, s[20:23], s55 offen sc1\n"
    "Lrel_32_%=:\n"
    "s_waitcnt lgkmcnt(0)\n"
    "s_waitcnt vmcnt(4)\n"
    "s_barrier\n"
    "s_and_b32 s67, s100, 1\n"
    "s_lshr_b32 s56, s7, 1\n"
    "s_mov_b32 s57, 0x20000\n"
    "s_cmp_eq_u32 s56, s67\n"
    "s_cselect_b32 s55, s57, 0x10000\n"
    "s_and_b32 s56, s7, 1\n"
    "s_mul_i32 s56, s56, 0x2080\n"
    "s_add_u32 s55, s55, s56\n"
    "s_add_u32 s55, s55, 0xc1e\n"
    "v_and_b32 v60, 1, v9\n"
    "v_xor_b32 v60, 1, v60\n"
    "v_mul_u32_u24 v61, 0x103e, v60\n"
    "v_lshlrev_b32 v60, 1, v9\n"
    "v_sub_u32 v61, v61, v60\n"
    "v_lshl_add_u32 v61, v10, 4, v61\n"
    "v_add_u32 v62, s55, v61\n"
    "ds_read2_b32 a[0:1], v62 offset0:240 offset1:241\n"
    "ds_read2_b32 a[2:3], v62 offset0:242 offset1:243\n"
    "ds_read2_b32 a[4:5], v62 offset0:224 offset1:225\n"
    "v_add_u32 v63, 0xfffffc00, v62\n"
    "v_add_u32 v108, 0xfffff800, v62\n"
    "ds_read2_b32 a[6:7], v62 offset0:226 offset1:227\n"
    "v_add_u32 v109, 0xfffff400, v62\n"
    "ds_read2_b32 a[8:9], v62 offset0:208 offset1:209\n"
    "ds_read2_b32 a[10:11], v62 offset0:210 offset1:211\n"
    "ds_read2_b32 a[12:13], v62 offset0:192 offset1:193\n"
    "ds_read2_b32 a[14:15], v62 offset0:194 offset1:195\n"
    "ds_read2_b32 a[16:17], v62 offset0:176 offset1:177\n"
    "ds_read2_b32 a[18:19], v62 offset0:178 offset1:179\n"
    "ds_read2_b32 a[20:21], v62 offset0:160 offset1:161\n"
    "ds_read2_b32 a[22:23], v62 offset0:162 offset1:163\n"
    "ds_read2_b32 a[24:25], v62 offset0:144 offset1:145\n"
    "ds_read2_b32 a[26:27], v62 offset0:146 offset1:147\n"
    "ds_read2_b32 a[28:29], v62 offset0:128 offset1:129\n"
    "ds_read2_b32 a[30:31], v62 offset0:130 offset1:131\n"
    "ds_read2_b32 a[32:33], v62 offset0:112 offset1:113\n"
    "ds_read2_b32 a[34:35], v62 offset0:114 offset1:115\n"
    "ds_read2_b32 a[36:37], v62 offset0:96 offset1:97\n"
    "ds_read2_b32 a[38:39], v62 offset0:98 offset1:99\n"
    "ds_read2_b32 a[40:41], v62 offset0:80 offset1:81\n"
    "ds_read2_b32 a[42:43], v62 offset0:82 offset1:83\n"
    "ds_read2_b32 a[44:45], v62 offset0:64 offset1:65\n"
    "ds_read2_b32 a[46:47], v62 offset0:66 offset1:67\n"
    "ds_read2_b32 a[48:49], v62 offset0:48 offset1:49\n"
    "ds_read2_b32 a[50:51], v62 offset0:50 offset1:51\n"
    "ds_read2_b32 a[52:53], v62 offset0:32 offset1:33\n"
    "ds_read2_b32 a[54:55], v62 offset0:34 offset1:35\n"
    "ds_read2_b32 a[56:57], v62 offset0:16 offset1:17\n"
    "ds_read2_b32 a[58:59], v62 offset0:18 offset1:19\n"
    "ds_read2_b32 a[60:61], v62 offset0:0 offset1:1\n"
    "ds_read2_b32 a[62:63], v62 offset0:2 offset1:3\n"
    "ds_read2_b32 a[64:65], v63 offset0:240 offset1:241\n"
    "ds_read2_b32 a[66:67], v63 offset0:242 offset1:243\n"
    "ds_read2_b32 a[68:69], v63 offset0:224 offset1:225\n"
    "ds_read2_b32 a[70:71], v63 offset0:226 offset1:227\n"
    "ds_read2_b32 a[72:73], v63 offset0:208 offset1:209\n"
    "ds_read2_b32 a[74:75], v63 offset0:210 offset1:211\n"
    "ds_read2_b32 a[76:77], v63 offset0:192 offset1:193\n"
    "ds_read2_b32 a[78:79], v63 offset0:194 offset1:195\n"
    "ds_read2_b32 a[80:81], v63 offset0:176 offset1:177\n"
    "ds_read2_b32 a[82:83], v63 offset0:178 offset1:179\n"
    "ds_read2_b32 a[84:85], v63 offset0:160 offset1:161\n"
    "ds_read2_b32 a[86:87], v63 offset0:162 offset1:163\n"
    "ds_read2_b32 a[88:89], v63 offset0:144 offset1:145\n"
    "ds_read2_b32 a[90:91], v63 offset0:146 offset1:147\n"
    "ds_read2_b32 a[92:93], v63 offset0:128 offset1:129\n"
    "ds_read2_b32 a[94:95], v63 offset0:130 offset1:131\n"
    "ds_read2_b32 a[96:97], v63 offset0:112 offset1:113\n"
    "ds_read2_b32 a[98:99], v63 offset0:114 offset1:115\n"
    "ds_read2_b32 a[100:101], v63 offset0:96 offset1:97\n"
    "ds_read2_b32 a[102:103], v63 offset0:98 offset1:99\n"
    "ds_read2_b32 a[104:105], v63 offset0:80 offset1:81\n"
    "ds_read2_b32 a[106:107], v63 offset0:82 offset1:83\n"
    "ds_read2_b32 a[108:109], v63 offset0:64 offset1:65\n"
    "ds_read2_b32 a[110:111], v63 offset0:66 offset1:67\n"
    "ds_read2_b32 a[112:113], v63 offset0:48 offset1:49\n"
    "ds_read2_b32 a[114:115], v63 offset0:50 offset1:51\n"
    "ds_read2_b32 a[116:117], v63 offset0:32 offset1:33\n"
    "ds_read2_b32 a[118:119], v63 offset0:34 offset1:35\n"
    "ds_read2_b32 a[120:121], v63 offset0:16 offset1:17\n"
    "ds_read2_b32 a[122:123], v63 offset0:18 offset1:19\n"
    "ds_read2_b32 a[124:125], v63 offset0:0 offset1:1\n"
    "ds_read2_b32 a[126:127], v63 offset0:2 offset1:3\n"
    "ds_read2_b32 a[128:129], v108 offset0:240 offset1:241\n"
    "ds_read2_b32 a[130:131], v108 offset0:242 offset1:243\n"
    "ds_read2_b32 a[132:133], v108 offset0:224 offset1:225\n"
    "ds_read2_b32 a[134:135], v108 offset0:226 offset1:227\n"
    "ds_read2_b32 a[136:137], v108 offset0:208 offset1:209\n"
    "ds_read2_b32 a[138:139], v108 offset0:210 offset1:211\n"
    "ds_read2_b32 a[140:141], v108 offset0:192 offset1:193\n"
    "ds_read2_b32 a[142:143], v108 offset0:194 offset1:195\n"
    "ds_read2_b32 a[144:145], v108 offset0:176 offset1:177\n"
    "ds_read2_b32 a[146:147], v108 offset0:178 offset1:179\n"
    "ds_read2_b32 a[148:149], v108 offset0:160 offset1:161\n"
    "ds_read2_b32 a[150:151], v108 offset0:162 offset1:163\n"
    "ds_read2_b32 a[152:153], v108 offset0:144 offset1:145\n"
    "ds_read2_b32 a[154:155], v108 offset0:146 offset1:147\n"
    "ds_read2_b32 a[156:157], v108 offset0:128 offset1:129\n"
    "ds_read2_b32 a[158:159], v108 offset0:130 offset1:131\n"
    "ds_read2_b32 a[160:161], v108 offset0:112 offset1:113\n"
    "ds_read2_b32 a[162:163], v108 offset0:114 offset1:115\n"
    "ds_read2_b32 a[164:165], v108 offset0:96 offset1:97\n"
    "ds_read2_b32 a[166:167], v108 offset0:98 offset1:99\n"
    "ds_read2_b32 a[168:169], v108 offset0:80 offset1:81\n"
    "ds_read2_b32 a[170:171], v108 offset0:82 offset1:83\n"
    "ds_read2_b32 a[172:173], v108 offset0:64 offset1:65\n"
    "ds_read2_b32 a[174:175], v108 offset0:66 offset1:67\n"
    "s_mov_b32 s32, 0\n"
    "s_mov_b32 s12, 1\n"
    "s_mov_b32 s13, 0x6900\n"
    "Lcvhead1_3_%=:\n"
    "s_load_dwordx2 s[24:25], s[4:5], 0x58\n"
    "s_mov_b32 s26, 0x200000\n"
    "s_mov_b32 s27, 0x20000\n"
    "s_mov_b32 s57, 0x20000\n"
    "v_lshrrev_b32 v16, 3, v0\n"
    "v_lshlrev_b32 v18, 16, v16\n"
    "v_and_b32 v16, 7, v0\n"
    "v_lshl_add_u32 v18, v16, 4, v18\n"
    "v_lshlrev_b32 v19, 4, v8\n"
    "v_and_b32 v16, 7, v9\n"
    "v_lshlrev_b32 v20, 5, v16\n"
    "v_lshrrev_b32 v16, 3, v9\n"
    "v_lshl_add_u32 v20, v16, 9, v20\n"
    "v_lshl_add_u32 v20, v10, 4, v20\n"
    "v_lshlrev_b32 v21, 4, v0\n"
    "v_add_u32 v21, 0x400, v21\n"
    "v_lshlrev_b32 v22, 4, v8\n"
    "v_bfe_u32 v16, v9, 1, 2\n"
    "v_lshrrev_b32 v17, 3, v9\n"
    "v_xor_b32 v16, v16, v17\n"
    "v_xor_b32 v16, v16, v10\n"
    "v_lshlrev_b32 v23, 6, v9\n"
    "v_lshl_add_u32 v23, v16, 4, v23\n"
    "Lpg8_%=:\n"
    "s_cbranch_execz Ltb4_1_0_%=\n"
    "v_lshrrev_b32 v25, 4, v0\n"
    "v_bfe_u32 v16, v25, 1, 2\n"
    "v_lshrrev_b32 v17, 3, v25\n"
    "v_xor_b32 v16, v16, v17\n"
    "v_bfe_u32 v17, v0, 2, 2\n"
    "v_xor_b32 v16, v16, v17\n"
    "v_lshlrev_b32 v24, 4, v16\n"
    "v_lshl_add_u32 v24, v25, 6, v24\n"
    "v_and_b32 v16, 3, v0\n"
    "v_lshl_add_u32 v24, v16, 2, v24\n"
    "v_add_u32 v24, 0x5000, v24\n"
    "v_lshlrev_b32 v26, 1, v8\n"
    "v_lshrrev_b32 v16, 7, v0\n"
    "v_lshlrev_b32 v16, 8, v16\n"
    "v_and_b32 v17, 0x7f, v0\n"
    "v_add_u32 v16, v16, v17\n"
    "v_mov_b32 v28, 0\n"
    "v_mov_b32 v29, 0\n"
    "v_mov_b32 v30, 0\n"
    "v_mov_b32 v31, 0\n"
    "v_mov_b32 v32, 0\n"
    "v_mov_b32 v33, 0\n"
    "v_mov_b32 v34, 0\n"
    "s_waitcnt lgkmcnt(0)\n"
    "s_and_b32 s25, s25, 0xffff\n"
    "s_lshl_b32 s55, s7, 12\n"
    "s_lshl_b32 s34, s6, 15\n"
    "s_add_u32 s34, s34, 0x1000000\n"
    "s_cmp_eq_u32 s32, 2\n"
    "s_cselect_b32 s34, s34, 0x38000\n"
    "s_add_u32 s35, s34, 0x4000\n"
    "s_and_b32 s67, s100, 1\n"
    "s_lshl_b32 s56, s67, 8\n"
    "v_add_u32 v19, s55, v19\n"
    "s_mul_i32 s55, s7, 0x1400\n"
    "s_add_u32 s55, s55, s56\n"
    "s_add_u32 s55, s55, 32\n"
    "v_add_u32 v20, s55, v20\n"
    "s_mul_i32 s55, s7, 0x1400\n"
    "v_add_u32 v22, s55, v22\n"
    "s_lshl_b32 s55, s7, 13\n"
    "s_add_u32 s55, s55, 0x5000\n"
    "v_add_u32 v23, s55, v23\n"
    "s_lshl_b32 s55, s67, 7\n"
    "s_and_b32 s67, s100, 1\n"
    "s_lshr_b32 s56, s7, 1\n"
    "s_cmp_eq_u32 s56, s67\n"
    "v_add_u32 v16, s55, v16\n"
    "s_cselect_b32 s55, s57, 0x10000\n"
    "s_and_b32 s56, s7, 1\n"
    "s_mul_i32 s56, s56, 0x2080\n"
    "s_add_u32 s55, s55, s56\n"
    "s_add_u32 s55, s55, 0xc1e\n"
    "v_lshlrev_b32 v27, 10, v16\n"
    "v_mov_b32 v35, 0\n"
    "v_mov_b32 v36, 0\n"
    "v_mov_b32 v37, 0\n"
    "v_mov_b32 v38, 0\n"
    "v_mov_b32 v39, 0\n"
    "v_mov_b32 v40, 0\n"
    "v_mov_b32 v41, 0\n"
    "v_mov_b32 v42, 0\n"
    "v_mov_b32 v43, 0\n"
    "v_and_b32 v44, 1, v9\n"
    "v_xor_b32 v44, 1, v44\n"
    "v_mul_u32_u24 v45, 0x103e, v44\n"
    "v_lshlrev_b32 v44, 1, v9\n"
    "v_sub_u32 v45, v45, v44\n"
    "v_lshl_add_u32 v45, v10, 4, v45\n"
    "v_add_u32 v46, s55, v45\n"
    "v_add_u32 v47, 0xfffffc00, v46\n"
    "v_add_u32 v48, 0xfffff800, v46\n"
    "v_add_u32 v49, 0xfffff400, v46\n"
    "s_cmp_eq_u32 s7, 0\n"
    "s_cbranch_scc0 Lbwe_36_%=\n"
    "s_and_b32 s55, s6, 7\n"
    "s_lshl_b32 s55, s55, 11\n"
    "s_add_u32 s55, s55, 0x1800\n"
    "s_mov_b32 s76, 0\n"
    "s_mov_b32 s77, 5\n"
    "s_mov_b64 s[60:61], exec\n"
    "v_and_b32 v50, 7, v8\n"
    "v_lshlrev_b32 v50, 8, v50\n"
    "s_mov_b64 exec, 0xff\n"
    "s_nop 1\n"
    "buffer_load_dword v188, v50, s[20:23], s55 offen sc1\n"
    "s_sleep 1\n"
    "buffer_load_dword v189, v50, s[20:23], s55 offen sc1\n"
    "s_sleep 1\n"
    "buffer_load_dword v190, v50, s[20:23], s55 offen sc1\n"
    "s_sleep 1\n"
    "Lbwp_34_%=:\n"
    "buffer_load_dword v191, v50, s[20:23], s55 offen sc1\n"
    "s_waitcnt vmcnt(3)\n"
    "v_cmp_le_u32 vcc, s77, v188\n"
    "s_nop 1\n"
    "s_cmp_eq_u64 vcc, exec\n"
    "s_cbranch_scc1 Lbwx_35_%=\n"
    "buffer_load_dword v188, v50, s[20:23], s55 offen sc1\n"
    "s_waitcnt vmcnt(3)\n"
    "v_cmp_le_u32 vcc, s77, v189\n"
    "s_nop 1\n"
    "s_cmp_eq_u64 vcc, exec\n"
    "s_cbranch_scc1 Lbwx_35_%=\n"
    "buffer_load_dword v189, v50, s[20:23], s55 offen sc1\n"
    "s_waitcnt vmcnt(3)\n"
    "v_cmp_le_u32 vcc, s77, v190\n"
    "s_nop 1\n"
    "s_cmp_eq_u64 vcc, exec\n"
    "s_cbranch_scc1 Lbwx_35_%=\n"
    "buffer_load_dword v190, v50, s[20:23], s55 offen sc1\n"
    "s_waitcnt vmcnt(3)\n"
    "v_cmp_le_u32 vcc, s77, v191\n"
    "s_nop 1\n"
    "s_cmp_eq_u64 vcc, exec\n"
    "s_cbranch_scc1 Lbwx_35_%=\n"
    "s_add_u32 s76, s76, 1\n"
    "s_cmp_lt_u32 s76, 0x10000\n"
    "s_cbranch_scc1 Lbwp_34_%=\n"
    "Lbwx_35_%=:\n"
    "s_mov_b64 exec, s[60:61]\n"
    "s_nop 0\n"
    "s_branch Lbw_33_%=\n"
    "Lbwe_36_%=:\n"
    "s_nop 0\n"
    "s_mov_b64 s[60:61], exec\n"
    "s_mov_b64 exec, 0\n"
    "s_cmp_eq_u32 s7, 2\n"
    "s_cbranch_scc1 Ltw5_2_%=\n"
    "s_cmp_eq_u32 s7, 3\n"
    "s_cbranch_scc1 Ltw5_3_%=\n"
    "s_nop 0\n"
    "Ltb5_1_0_%=:\n"
    "s_nop 0\n"
    "Ltb5_1_1_%=:\n"
    "s_branch Ltf5_%=\n"
    "Ltw5_2_%=:\n"
    "s_nop 0\n"
    "Ltb5_2_0_%=:\n"
    "s_nop 0\n"
    "Ltb5_2_1_%=:\n"
    "s_branch Ltf5_%=\n"
    "Ltw5_3_%=:\n"
    "s_nop 0\n"
    "Ltb5_3_0_%=:\n"
    "s_nop 0\n"
    "Ltb5_3_1_%=:\n"
    "Ltf5_%=:\n"
    "s_mov_b64 exec, s[60:61]\n"
    "Lbw_33_%=:\n"
    "s_waitcnt lgkmcnt(0)\n"
    "s_barrier\n"
    "buffer_load_dwordx4 v[84:87], v19, s[20:23], s34 offen offset:0 sc1\n"
    "buffer_load_dwordx4 v[100:103], v19, s[20:23], s35 offen offset:0 sc1\n"
    "buffer_load_dwordx4 v[88:91], v19, s[20:23], s34 offen offset:1024 sc1\n"
    "s_lshr_b32 s55, s100, 1\n"
    "s_lshl_b32 s55, s55, 9\n"
    "s_add_u32 s52, s55, 0x700000\n"
    "s_lshr_b32 s55, s7, 1\n"
    "s_lshl_b32 s55, s55, 2\n"
    "s_lshl_b32 s56, s67, 1\n"
    "s_add_u32 s55, s55, s56\n"
    "s_and_b32 s56, s7, 1\n"
    "s_add_u32 s55, s55, s56\n"
    "s_lshl_b32 s55, s55, 16\n"
    "s_add_u32 s58, s52, s55\n"
    "v_mov_b32 v116, 0\n"
    "v_mov_b32 v117, 0\n"
    "v_mov_b32 v118, 0\n"
    "v_mov_b32 v119, 0\n"
    "ds_write_b128 v22, v[116:119]\n"
    "buffer_load_dwordx4 v[104:107], v19, s[20:23], s35 offen offset:1024 sc1\n"
    "v_mov_b32 v124, 0x3b000000\n"
    "v_mov_b32 v125, 0x3b000000\n"
    "v_mov_b32 v126, 0x3727c5ac\n"
    "v_mov_b32 v127, 0x3727c5ac\n"
    "v_mov_b32 v123, 1\n"
    "v_mov_b32 v176, 0x7ffffff0\n"
    "v_cmp_eq_u32 vcc, 0, v0\n"
    "s_nop 1\n"
    "v_cndmask_b32 v176, v176, 0, vcc\n"
    "buffer_load_dwordx4 v[92:95], v19, s[20:23], s34 offen offset:2048 sc1\n"
    "buffer_load_dwordx4 v[108:111], v19, s[20:23], s35 offen offset:2048 sc1\n"
    "buffer_load_dwordx4 v[96:99], v19, s[20:23], s34 offen offset:3072 sc1\n"
    "buffer_load_dwordx4 v[112:115], v19, s[20:23], s35 offen offset:3072 sc1\n"
    "buffer_load_dwordx4 v[52:55], v18, s[20:23], s52 offen offset:0 sc1\n"
    "buffer_load_dwordx4 v[56:59], v18, s[20:23], s52 offen offset:128 sc1\n"
    "buffer_load_dwordx4 v[60:63], v18, s[20:23], s52 offen offset:256 sc1\n"
    "s_waitcnt vmcnt(10)\n"
    "v_add_f64 v[84:85], v[84:85], s[38:39]\n"
    "v_cvt_f32_f64 v68, v[84:85]\n"
    "v_add_f64 v[86:87], v[86:87], s[38:39]\n"
    "v_cvt_f32_f64 v69, v[86:87]\n"
    "buffer_load_dwordx4 v[64:67], v18, s[20:23], s52 offen offset:384 sc1\n"
    "ds_read2_b32 a[176:177], v48 offset0:48 offset1:49\n"
    "ds_read2_b32 a[178:179], v48 offset0:50 offset1:51\n"
    "ds_read2_b32 a[180:181], v48 offset0:32 offset1:33\n"
    "ds_read2_b32 a[182:183], v48 offset0:34 offset1:35\n"
    "ds_read2_b32 a[184:185], v48 offset0:16 offset1:17\n"
    "s_waitcnt vmcnt(10)\n"
    "v_add_f64 v[100:101], v[100:101], s[38:39]\n"
    "ds_read2_b32 a[186:187], v48 offset0:18 offset1:19\n"
    "v_cvt_f32_f64 v76, v[100:101]\n"
    "v_add_f64 v[102:103], v[102:103], s[38:39]\n"
    "ds_read2_b32 a[188:189], v48 offset0:0 offset1:1\n"
    "buffer_load_short_d16_hi v28, v26, s[20:23], s58 offen offset:0 sc1\n"
    "v_cvt_f32_f64 v77, v[102:103]\n"
    "ds_read2_b32 a[190:191], v48 offset0:2 offset1:3\n"
    "v_max3_f32 v51, |v68|, |v76|, |v69|\n"
    "v_max_f32 v51, |v77|, v51\n"
    "buffer_load_short_d16_hi v29, v26, s[20:23], s58 offen offset:128 sc1\n"
    "ds_read2_b32 a[192:193], v49 offset0:240 offset1:241\n"
    "v_pk_mul_f32 v[68:69], v[68:69], v[124:125]\n"
    "v_pk_fma_f32 v[76:77], v[76:77], v[124:125], v[126:127]\n"
    "buffer_load_short_d16_hi v30, v26, s[20:23], s58 offen offset:256 sc1\n"
    "ds_read2_b32 a[194:195], v49 offset0:242 offset1:243\n"
    "v_pk_fma_f32 v[76:77], v[68:69], v[68:69], v[76:77] neg_lo:[1,0,0] neg_hi:[1,0,0]\n"
    "ds_read2_b32 a[196:197], v49 offset0:224 offset1:225\n"
    "s_waitcnt vmcnt(12)\n"
    "v_add_f64 v[88:89], v[88:89], s[38:39]\n"
    "buffer_load_short_d16_hi v31, v26, s[20:23], s58 offen offset:384 sc1\n"
    "s_add_u32 s58, s58, 0x80000\n"
    "ds_read2_b32 a[198:199], v49 offset0:226 offset1:227\n"
    "v_cvt_f32_f64 v70, v[88:89]\n"
    "v_add_f64 v[90:91], v[90:91], s[38:39]\n"
    "buffer_load_short_d16_hi v32, v26, s[20:23], s58 offen offset:0 sc1\n"
    "ds_read2_b32 a[200:201], v49 offset0:208 offset1:209\n"
    "v_cvt_f32_f64 v71, v[90:91]\n"
    "ds_read2_b32 a[202:203], v49 offset0:210 offset1:211\n"
    "v_max_f32 v76, v126, v76\n"
    "buffer_load_short_d16_hi v33, v26, s[20:23], s58 offen offset:128 sc1\n"
    "ds_read2_b32 a[204:205], v49 offset0:192 offset1:193\n"
    "v_rsq_f32 v76, v76\n"
    "v_max_f32 v77, v126, v77\n"
    "ds_read2_b32 a[206:207], v49 offset0:194 offset1:195\n"
    "buffer_load_short_d16_hi v34, v26, s[20:23], s58 offen offset:256 sc1\n"
    "s_waitcnt vmcnt(15)\n"
    "v_add_f64 v[104:105], v[104:105], s[38:39]\n"
    "ds_read2_b32 a[208:209], v49 offset0:176 offset1:177\n"
    "v_cvt_f32_f64 v78, v[104:105]\n"
    "v_add_f64 v[106:107], v[106:107], s[38:39]\n"
    "buffer_load_short_d16_hi v35, v26, s[20:23], s58 offen offset:384 sc1\n"
    "s_add_u32 s58, s58, 0x80000\n"
    "ds_read2_b32 a[210:211], v49 offset0:178 offset1:179\n"
    "v_cvt_f32_f64 v79, v[106:107]\n"
    "ds_read2_b32 a[212:213], v49 offset0:160 offset1:161\n"
    "buffer_load_short_d16_hi v36, v26, s[20:23], s58 offen offset:0 sc1\n"
    "v_max3_f32 v51, |v70|, |v78|, v51\n"
    "ds_read2_b32 a[214:215], v49 offset0:162 offset1:163\n"
    "v_max3_f32 v51, |v71|, |v79|, v51\n"
    "v_pk_mul_f32 v[70:71], v[70:71], v[124:125]\n"
    "buffer_load_short_d16_hi v37, v26, s[20:23], s58 offen offset:128 sc1\n"
    "ds_read2_b32 a[216:217], v49 offset0:144 offset1:145\n"
    "s_waitcnt vmcnt(17)\n"
    "v_add_f64 v[92:93], v[92:93], s[38:39]\n"
    "v_cvt_f32_f64 v72, v[92:93]\n"
    "ds_read2_b32 a[218:219], v49 offset0:146 offset1:147\n"
    "buffer_load_short_d16_hi v38, v26, s[20:23], s58 offen offset:256 sc1\n"
    "v_add_f64 v[94:95], v[94:95], s[38:39]\n"
    "ds_read2_b32 a[220:221], v49 offset0:128 offset1:129\n"
    "v_cvt_f32_f64 v73, v[94:95]\n"
    "v_pk_fma_f32 v[78:79], v[78:79], v[124:125], v[126:127]\n"
    "buffer_load_short_d16_hi v39, v26, s[20:23], s58 offen offset:384 sc1\n"
    "s_add_u32 s58, s58, 0x80000\n"
    "ds_read2_b32 a[222:223], v49 offset0:130 offset1:131\n"
    "v_pk_fma_f32 v[78:79], v[70:71], v[70:71], v[78:79] neg_lo:[1,0,0] neg_hi:[1,0,0]\n"
    "buffer_load_short_d16_hi v40, v26, s[20:23], s58 offen offset:0 sc1\n"
    "ds_read2_b32 a[224:225], v49 offset0:112 offset1:113\n"
    "v_rsq_f32 v77, v77\n"
    "ds_read2_b32 a[226:227], v49 offset0:114 offset1:115\n"
    "s_waitcnt vmcnt(19)\n"
    "v_add_f64 v[108:109], v[108:109], s[38:39]\n"
    "buffer_load_short_d16_hi v41, v26, s[20:23], s58 offen offset:128 sc1\n"
    "ds_read2_b32 a[228:229], v49 offset0:96 offset1:97\n"
    "v_cvt_f32_f64 v80, v[108:109]\n"
    "v_add_f64 v[110:111], v[110:111], s[38:39]\n"
    "ds_read2_b32 a[230:231], v49 offset0:98 offset1:99\n"
    "buffer_load_short_d16_hi v42, v26, s[20:23], s58 offen offset:256 sc1\n"
    "v_cvt_f32_f64 v81, v[110:111]\n"
    "ds_read2_b32 a[232:233], v49 offset0:80 offset1:81\n"
    "v_max3_f32 v51, |v72|, |v80|, v51\n"
    "v_max3_f32 v51, |v73|, |v81|, v51\n"
    "buffer_load_short_d16_hi v43, v26, s[20:23], s58 offen offset:384 sc1\n"
    "ds_read2_b32 a[234:235], v49 offset0:82 offset1:83\n"
    "v_pk_mul_f32 v[72:73], v[72:73], v[124:125]\n"
    "v_pk_fma_f32 v[80:81], v[80:81], v[124:125], v[126:127]\n"
    "ds_read2_b32 a[236:237], v49 offset0:64 offset1:65\n"
    "s_waitcnt vmcnt(21)\n"
    "v_add_f64 v[96:97], v[96:97], s[38:39]\n"
    "v_cvt_f32_f64 v74, v[96:97]\n"
    "ds_read2_b32 a[238:239], v49 offset0:66 offset1:67\n"
    "v_add_f64 v[98:99], v[98:99], s[38:39]\n"
    "v_cvt_f32_f64 v75, v[98:99]\n"
    "v_pk_fma_f32 v[80:81], v[72:73], v[72:73], v[80:81] neg_lo:[1,0,0] neg_hi:[1,0,0]\n"
    "ds_read2_b32 a[240:241], v49 offset0:48 offset1:49\n"
    "v_max_f32 v78, v126, v78\n"
    "v_rsq_f32 v78, v78\n"
    "ds_read2_b32 a[242:243], v49 offset0:50 offset1:51\n"
    "v_max_f32 v79, v126, v79\n"
    "v_rsq_f32 v79, v79\n"
    "ds_read2_b32 a[244:245], v49 offset0:32 offset1:33\n"
    "v_max_f32 v80, v126, v80\n"
    "s_waitcnt vmcnt(20)\n"
    "v_add_f64 v[112:113], v[112:113], s[38:39]\n"
    "ds_read2_b32 a[246:247], v49 offset0:34 offset1:35\n"
    "v_cvt_f32_f64 v82, v[112:113]\n"
    "v_add_f64 v[114:115], v[114:115], s[38:39]\n"
    "ds_read2_b32 a[248:249], v49 offset0:16 offset1:17\n"
    "v_cvt_f32_f64 v83, v[114:115]\n"
    "v_max3_f32 v51, |v74|, |v82|, v51\n"
    "ds_read2_b32 a[250:251], v49 offset0:18 offset1:19\n"
    "v_max3_f32 v51, |v75|, |v83|, v51\n"
    "v_cmp_le_f32 s[30:31], s41, v51\n"
    "v_pk_mul_f32 v[74:75], v[74:75], v[124:125]\n"
    "ds_read2_b32 a[252:253], v49 offset0:0 offset1:1\n"
    "v_pk_fma_f32 v[82:83], v[82:83], v[124:125], v[126:127]\n"
    "v_pk_fma_f32 v[82:83], v[74:75], v[74:75], v[82:83] neg_lo:[1,0,0] neg_hi:[1,0,0]\n"
    "ds_read2_b32 a[254:255], v49 offset0:2 offset1:3\n"
    "v_rsq_f32 v80, v80\n"
    "s_waitcnt vmcnt(19)\n"
    "v_max_u32 v120, v52, v53\n"
    "v_max3_u32 v120, v54, v55, v120\n"
    "v_max_f32 v81, v126, v81\n"
    "v_rsq_f32 v81, v81\n"
    "v_max_f32 v82, v126, v82\n"
    "v_rsq_f32 v82, v82\n"
    "v_max_f32 v83, v126, v83\n"
    "v_rsq_f32 v83, v83\n"
    "v_pk_mul_f32 v[68:69], v[68:69], v[76:77] neg_lo:[1,0] neg_hi:[1,0]\n"
    "v_pk_mul_f32 v[70:71], v[70:71], v[78:79] neg_lo:[1,0] neg_hi:[1,0]\n"
    "v_pk_mul_f32 v[72:73], v[72:73], v[80:81] neg_lo:[1,0] neg_hi:[1,0]\n"
    "v_pk_mul_f32 v[74:75], v[74:75], v[82:83] neg_lo:[1,0] neg_hi:[1,0]\n"
    "v_lshlrev_b32 v128, 16, v52\n"
    "v_and_b32 v129, 0xffff0000, v52\n"
    "v_pk_fma_f32 v[128:129], v[128:129], v[76:77], v[68:69]\n"
    "s_waitcnt vmcnt(18)\n"
    "v_max3_u32 v120, v56, v57, v120\n"
    "v_max3_u32 v120, v58, v59, v120\n"
    "v_cvt_pk_bf16_f32 v136, v128, v129\n"
    "v_lshlrev_b32 v130, 16, v53\n"
    "v_and_b32 v131, 0xffff0000, v53\n"
    "v_pk_fma_f32 v[130:131], v[130:131], v[78:79], v[70:71]\n"
    "v_cvt_pk_bf16_f32 v137, v130, v131\n"
    "v_lshlrev_b32 v132, 16, v54\n"
    "v_and_b32 v133, 0xffff0000, v54\n"
    "v_pk_fma_f32 v[132:133], v[132:133], v[80:81], v[72:73]\n"
    "v_cvt_pk_bf16_f32 v138, v132, v133\n"
    "v_lshlrev_b32 v134, 16, v55\n"
    "v_and_b32 v135, 0xffff0000, v55\n"
    "v_pk_fma_f32 v[134:135], v[134:135], v[82:83], v[74:75]\n"
    "v_cvt_pk_bf16_f32 v139, v134, v135\n"
    "ds_write_b128 v21, v[136:139] offset:0\n"
    "v_lshlrev_b32 v140, 16, v56\n"
    "s_waitcnt vmcnt(17)\n"
    "v_max3_u32 v120, v60, v61, v120\n"
    "v_max3_u32 v120, v62, v63, v120\n"
    "v_and_b32 v141, 0xffff0000, v56\n"
    "v_pk_fma_f32 v[140:141], v[140:141], v[76:77], v[68:69]\n"
    "v_cvt_pk_bf16_f32 v148, v140, v141\n"
    "v_lshlrev_b32 v142, 16, v57\n"
    "v_and_b32 v143, 0xffff0000, v57\n"
    "v_pk_fma_f32 v[142:143], v[142:143], v[78:79], v[70:71]\n"
    "v_cvt_pk_bf16_f32 v149, v142, v143\n"
    "v_lshlrev_b32 v144, 16, v58\n"
    "v_and_b32 v145, 0xffff0000, v58\n"
    "v_pk_fma_f32 v[144:145], v[144:145], v[80:81], v[72:73]\n"
    "v_cvt_pk_bf16_f32 v150, v144, v145\n"
    "v_lshlrev_b32 v146, 16, v59\n"
    "v_and_b32 v147, 0xffff0000, v59\n"
    "v_pk_fma_f32 v[146:147], v[146:147], v[82:83], v[74:75]\n"
    "v_cvt_pk_bf16_f32 v151, v146, v147\n"
    "ds_write_b128 v21, v[148:151] offset:5120\n"
    "s_waitcnt vmcnt(16)\n"
    "v_max3_u32 v120, v64, v65, v120\n"
    "v_max3_u32 v120, v66, v67, v120\n"
    "v_cmp_eq_u32 s[42:43], -1, v120\n"
    "v_lshlrev_b32 v152, 16, v60\n"
    "s_or_b64 s[30:31], s[30:31], s[42:43]\n"
    "s_lshl_b32 s29, s7, 2\n"
    "s_add_u32 s29, s29, 0x27a00\n"
    "s_and_b32 s58, s6, 7\n"
    "s_lshl_b32 s58, s58, 8\n"
    "s_add_u32 s58, s58, s13\n"
    "s_cmp_eq_u32 s32, 0\n"
    "s_cselect_b32 s58, s58, 0x40000000\n"
    "s_lshr_b32 s55, s100, 1\n"
    "s_lshl_b32 s55, s55, 3\n"
    "v_mov_b32 v122, s29\n"
    "v_and_b32 v153, 0xffff0000, v60\n"
    "v_pk_fma_f32 v[152:153], v[152:153], v[76:77], v[68:69]\n"
    "v_cvt_pk_bf16_f32 v160, v152, v153\n"
    "v_lshlrev_b32 v154, 16, v61\n"
    "v_and_b32 v155, 0xffff0000, v61\n"
    "v_pk_fma_f32 v[154:155], v[154:155], v[78:79], v[70:71]\n"
    "v_cvt_pk_bf16_f32 v161, v154, v155\n"
    "v_lshlrev_b32 v156, 16, v62\n"
    "v_and_b32 v157, 0xffff0000, v62\n"
    "v_pk_fma_f32 v[156:157], v[156:157], v[80:81], v[72:73]\n"
    "v_cvt_pk_bf16_f32 v162, v156, v157\n"
    "v_lshlrev_b32 v158, 16, v63\n"
    "v_and_b32 v159, 0xffff0000, v63\n"
    "v_pk_fma_f32 v[158:159], v[158:159], v[82:83], v[74:75]\n"
    "v_cvt_pk_bf16_f32 v163, v158, v159\n"
    "ds_write_b128 v21, v[160:163] offset:10240\n"
    "v_lshlrev_b32 v164, 16, v64\n"
    "v_and_b32 v165, 0xffff0000, v64\n"
    "v_pk_fma_f32 v[164:165], v[164:165], v[76:77], v[68:69]\n"
    "v_cvt_pk_bf16_f32 v172, v164, v165\n"
    "v_lshlrev_b32 v166, 16, v65\n"
    "v_and_b32 v167, 0xffff0000, v65\n"
    "v_pk_fma_f32 v[166:167], v[166:167], v[78:79], v[70:71]\n"
    "v_cvt_pk_bf16_f32 v173, v166, v167\n"
    "v_lshlrev_b32 v168, 16, v66\n"
    "v_and_b32 v169, 0xffff0000, v66\n"
    "v_pk_fma_f32 v[168:169], v[168:169], v[80:81], v[72:73]\n"
    "v_cvt_pk_bf16_f32 v174, v168, v169\n"
    "v_lshlrev_b32 v170, 16, v67\n"
    "v_and_b32 v171, 0xffff0000, v67\n"
    "v_pk_fma_f32 v[170:171], v[170:171], v[82:83], v[74:75]\n"
    "v_cvt_pk_bf16_f32 v175, v170, v171\n"
    "ds_write_b128 v21, v[172:175] offset:15360\n"
    "s_waitcnt vmcnt(14)\n"
    "v_max_u32 v177, v28, v29\n"
    "s_waitcnt vmcnt(12)\n"
    "v_max3_u32 v177, v30, v31, v177\n"
    "s_waitcnt vmcnt(10)\n"
    "v_max3_u32 v177, v32, v33, v177\n"
    "v_mov_b32 v186, 0x27a00\n"
    "s_waitcnt vmcnt(8)\n"
    "v_max3_u32 v177, v34, v35, v177\n"
    "s_waitcnt vmcnt(6)\n"
    "v_max3_u32 v177, v36, v37, v177\n"
    "s_waitcnt lgkmcnt(0)\n"
    "s_barrier\n"
    "ds_read_b128 v[192:195], v20 offset:448\n"
    "ds_read_b128 v[196:199], v20 offset:384\n"
    "ds_read_b128 v[200:203], v20 offset:320\n"
    "buffer_atomic_add v123, v176, s[20:23], s58 offen\n"
    "s_waitcnt vmcnt(5)\n"
    "v_max3_u32 v177, v38, v39, v177\n"
    "ds_read_b128 v[204:207], v20 offset:256\n"
    "s_waitcnt vmcnt(3)\n"
    "v_max3_u32 v177, v40, v41, v177\n"
    "ds_read_b128 v[208:211], v20 offset:192\n"
    "ds_read_b128 v[212:215], v20 offset:128\n"
    "s_waitcnt vmcnt(1)\n"
    "v_max3_u32 v177, v42, v43, v177\n"
    "ds_read_b128 v[216:219], v20 offset:64\n"
    "v_cmp_eq_u32 vcc, 0xffff0000, v177\n"
    "s_nop 0\n"
    "s_or_b64 s[30:31], s[30:31], vcc\n"
    "s_cmp_lg_u64 s[30:31], 0\n"
    "s_cselect_b32 s33, 1, 0\n"
    "ds_read_b128 v[220:223], v20 offset:0\n"
    "ds_read_b128 v[224:227], v20 offset:960\n"
    "s_waitcnt lgkmcnt(8)\n"
    "v_mfma_f32_16x16x32_bf16 v[178:181], a[32:35], v[192:195], 0\n"
    "s_add_u32 s56, s55, 0x80000\n"
    "ds_read_b128 v[228:231], v20 offset:896\n"
    "s_waitcnt lgkmcnt(8)\n"
    "v_mfma_f32_16x16x32_bf16 v[178:181], a[36:39], v[196:199], v[178:181]\n"
    "ds_read_b128 v[232:235], v20 offset:832\n"
    "v_mov_b32 v121, s33\n"
    "s_waitcnt lgkmcnt(8)\n"
    "v_mfma_f32_16x16x32_bf16 v[178:181], a[40:43], v[200:203], v[178:181]\n"
    "ds_read_b128 v[236:239], v20 offset:768\n"
    "s_waitcnt lgkmcnt(8)\n"
    "v_mfma_f32_16x16x32_bf16 v[178:181], a[44:47], v[204:207], v[178:181]\n"
    "ds_read_b128 v[240:243], v20 offset:704\n"
    "s_waitcnt lgkmcnt(8)\n"
    "v_mfma_f32_16x16x32_bf16 v[178:181], a[48:51], v[208:211], v[178:181]\n"
    "ds_read_b128 v[244:247], v20 offset:640\n"
    "s_waitcnt lgkmcnt(8)\n"
    "v_mfma_f32_16x16x32_bf16 v[178:181], a[52:55], v[212:215], v[178:181]\n"
    "ds_read_b128 v[248:251], v20 offset:576\n"
    "s_waitcnt lgkmcnt(8)\n"
    "v_mfma_f32_16x16x32_bf16 v[178:181], a[56:59], v[216:219], v[178:181]\n"
    "ds_read_b128 v[252:255], v20 offset:512\n"
    "s_waitcnt lgkmcnt(8)\n"
    "v_mfma_f32_16x16x32_bf16 v[178:181], a[60:63], v[220:223], v[178:181]\n"
    "ds_write_b32 v122, v121\n"
    "s_waitcnt lgkmcnt(8)\n"
    "v_mfma_f32_16x16x32_bf16 v[178:181], a[0:3], v[224:227], v[178:181]\n"
    "s_waitcnt lgkmcnt(7)\n"
    "v_mfma_f32_16x16x32_bf16 v[178:181], a[4:7], v[228:231], v[178:181]\n"
    "s_waitcnt lgkmcnt(6)\n"
    "v_mfma_f32_16x16x32_bf16 v[178:181], a[8:11], v[232:235], v[178:181]\n"
    "s_waitcnt lgkmcnt(5)\n"
    "v_mfma_f32_16x16x32_bf16 v[178:181], a[12:15], v[236:239], v[178:181]\n"
    "s_waitcnt lgkmcnt(4)\n"
    "v_mfma_f32_16x16x32_bf16 v[178:181], a[16:19], v[240:243], v[178:181]\n"
    "s_waitcnt lgkmcnt(3)\n"
    "v_mfma_f32_16x16x32_bf16 v[178:181], a[20:23], v[244:247], v[178:181]\n"
    "s_waitcnt lgkmcnt(2)\n"
    "v_mfma_f32_16x16x32_bf16 v[178:181], a[24:27], v[248:251], v[178:181]\n"
    "s_waitcnt lgkmcnt(1)\n"
    "v_mfma_f32_16x16x32_bf16 v[178:181], a[28:31], v[252:255], v[178:181]\n"
    "v_mfma_f32_16x16x32_bf16 v[182:185], a[96:99], v[192:195], 0\n"
    "v_mfma_f32_16x16x32_bf16 v[182:185], a[100:103], v[196:199], v[182:185]\n"
    "s_nop 5\n"
    "ds_write_b128 v23, v[178:181] offset:0\n"
    "v_mfma_f32_16x16x32_bf16 v[182:185], a[104:107], v[200:203], v[182:185]\n"
    "v_mfma_f32_16x16x32_bf16 v[182:185], a[108:111], v[204:207], v[182:185]\n"
    "v_mfma_f32_16x16x32_bf16 v[182:185], a[112:115], v[208:211], v[182:185]\n"
    "v_mfma_f32_16x16x32_bf16 v[182:185], a[116:119], v[212:215], v[182:185]\n"
    "s_waitcnt lgkmcnt(0)\n"
    "s_barrier\n"
    "v_mfma_f32_16x16x32_bf16 v[182:185], a[120:123], v[216:219], v[182:185]\n"
    "ds_read2st64_b32 v[120:121], v24 offset0:0 offset1:32\n"
    "ds_read2st64_b32 v[122:123], v24 offset0:64 offset1:96\n"
    "v_mfma_f32_16x16x32_bf16 v[182:185], a[124:127], v[220:223], v[182:185]\n"
    "ds_read_b128 v[116:119], v20 offset:1472\n"
    "ds_read_b128 v[84:87], v20 offset:1408\n"
    "v_mfma_f32_16x16x32_bf16 v[182:185], a[64:67], v[224:227], v[182:185]\n"
    "ds_read_b128 v[88:91], v20 offset:1344\n"
    "v_mfma_f32_16x16x32_bf16 v[182:185], a[68:71], v[228:231], v[182:185]\n"
    "ds_read_b128 v[92:95], v20 offset:1280\n"
    "v_mfma_f32_16x16x32_bf16 v[182:185], a[72:75], v[232:235], v[182:185]\n"
    "ds_read_b128 v[96:99], v20 offset:1216\n"
    "v_mfma_f32_16x16x32_bf16 v[182:185], a[76:79], v[236:239], v[182:185]\n"
    "ds_read_b128 v[100:103], v20 offset:1152\n"
    "v_mfma_f32_16x16x32_bf16 v[182:185], a[80:83], v[240:243], v[182:185]\n"
    "ds_read_b128 v[104:107], v20 offset:1088\n"
    "v_mfma_f32_16x16x32_bf16 v[182:185], a[84:87], v[244:247], v[182:185]\n"
    "s_waitcnt lgkmcnt(8)\n"
    "v_add_f32 v120, v120, v28\n"
    "ds_read_b128 v[108:111], v20 offset:1024\n"
    "v_mfma_f32_16x16x32_bf16 v[182:185], a[88:91], v[248:251], v[182:185]\n"
    "v_add_f32 v121, v121, v29\n"
    "ds_read_b128 v[112:115], v20 offset:1984\n"
    "v_mfma_f32_16x16x32_bf16 v[182:185], a[92:95], v[252:255], v[182:185]\n"
    "s_waitcnt lgkmcnt(9)\n"
    "v_add_f32 v122, v122, v30\n"
    "ds_read_b128 v[124:127], v20 offset:1920\n"
    "v_mfma_f32_16x16x32_bf16 v[16:19], a[160:163], v[192:195], 0\n"
    "ds_read_b128 v[128:131], v20 offset:1856\n"
    "v_add_f32 v123, v123, v31\n"
    "v_mfma_f32_16x16x32_bf16 v[16:19], a[164:167], v[196:199], v[16:19]\n"
    "ds_read_b128 v[132:135], v20 offset:1792\n"
    "v_cvt_pk_bf16_f32 v120, v120, v121\n"
    "v_mfma_f32_16x16x32_bf16 v[16:19], a[168:171], v[200:203], v[16:19]\n"
    "ds_read_b128 v[136:139], v20 offset:1728\n"
    "v_cvt_pk_bf16_f32 v121, v122, v123\n"
    "v_mfma_f32_16x16x32_bf16 v[16:19], a[172:175], v[204:207], v[16:19]\n"
    "buffer_store_dwordx2 v[120:121], v27, s[24:27], s55 offen\n"
    "ds_read_b128 v[140:143], v20 offset:1664\n"
    "v_mfma_f32_16x16x32_bf16 v[16:19], a[176:179], v[208:211], v[16:19]\n"
    "ds_read_b128 v[144:147], v20 offset:1600\n"
    "s_add_u32 s55, s56, 0x80000\n"
    "v_mfma_f32_16x16x32_bf16 v[16:19], a[180:183], v[212:215], v[16:19]\n"
    "ds_read_b128 v[148:151], v20 offset:1536\n"
    "s_cmp_lt_u32 s32, 2\n"
    "v_mfma_f32_16x16x32_bf16 v[16:19], a[184:187], v[216:219], v[16:19]\n"
    "v_mfma_f32_16x16x32_bf16 v[16:19], a[188:191], v[220:223], v[16:19]\n"
    "v_mfma_f32_16x16x32_bf16 v[16:19], a[128:131], v[224:227], v[16:19]\n"
    "v_mfma_f32_16x16x32_bf16 v[16:19], a[132:135], v[228:231], v[16:19]\n"
    "v_mfma_f32_16x16x32_bf16 v[16:19], a[136:139], v[232:235], v[16:19]\n"
    "v_mfma_f32_16x16x32_bf16 v[16:19], a[140:143], v[236:239], v[16:19]\n"
    "v_mfma_f32_16x16x32_bf16 v[16:19], a[144:147], v[240:243], v[16:19]\n"
    "v_mfma_f32_16x16x32_bf16 v[16:19], a[148:151], v[244:247], v[16:19]\n"
    "v_mfma_f32_16x16x32_bf16 v[16:19], a[152:155], v[248:251], v[16:19]\n"
    "v_mfma_f32_16x16x32_bf16 v[16:19], a[156:159], v[252:255], v[16:19]\n"
    "v_mfma_f32_16x16x32_bf16 v[50:53], a[224:227], v[192:195], 0\n"
    "ds_read_b128 v[192:195], v20 offset:2496\n"
    "v_mfma_f32_16x16x32_bf16 v[50:53], a[228:231], v[196:199], v[50:53]\n"
    "ds_read_b128 v[196:199], v20 offset:2432\n"
    "v_mfma_f32_16x16x32_bf16 v[50:53], a[232:235], v[200:203], v[50:53]\n"
    "ds_read_b128 v[200:203], v20 offset:2368\n"
    "v_mfma_f32_16x16x32_bf16 v[50:53], a[236:239], v[204:207], v[50:53]\n"
    "ds_read_b128 v[204:207], v20 offset:2304\n"
    "v_mfma_f32_16x16x32_bf16 v[50:53], a[240:243], v[208:211], v[50:53]\n"
    "ds_read_b128 v[208:211], v20 offset:2240\n"
    "v_mfma_f32_16x16x32_bf16 v[50:53], a[244:247], v[212:215], v[50:53]\n"
    "ds_read_b128 v[212:215], v20 offset:2176\n"
    "v_mfma_f32_16x16x32_bf16 v[50:53], a[248:251], v[216:219], v[50:53]\n"
    "ds_read_b128 v[216:219], v20 offset:2112\n"
    "v_mfma_f32_16x16x32_bf16 v[50:53], a[252:255], v[220:223], v[50:53]\n"
    "ds_read_b128 v[220:223], v20 offset:2048\n"
    "v_mfma_f32_16x16x32_bf16 v[50:53], a[192:195], v[224:227], v[50:53]\n"
    "ds_read_b128 v[224:227], v20 offset:3008\n"
    "v_mfma_f32_16x16x32_bf16 v[50:53], a[196:199], v[228:231], v[50:53]\n"
    "ds_read_b128 v[228:231], v20 offset:2944\n"
    "v_mfma_f32_16x16x32_bf16 v[50:53], a[200:203], v[232:235], v[50:53]\n"
    "ds_read_b128 v[232:235], v20 offset:2880\n"
    "v_mfma_f32_16x16x32_bf16 v[50:53], a[204:207], v[236:239], v[50:53]\n"
    "ds_read_b128 v[236:239], v20 offset:2816\n"
    "v_mfma_f32_16x16x32_bf16 v[50:53], a[208:211], v[240:243], v[50:53]\n"
    "ds_read_b128 v[240:243], v20 offset:2752\n"
    "v_mfma_f32_16x16x32_bf16 v[50:53], a[212:215], v[244:247], v[50:53]\n"
    "ds_read_b128 v[244:247], v20 offset:2688\n"
    "v_mfma_f32_16x16x32_bf16 v[50:53], a[216:219], v[248:251], v[50:53]\n"
    "ds_read_b128 v[248:251], v20 offset:2624\n"
    "v_mfma_f32_16x16x32_bf16 v[50:53], a[220:223], v[252:255], v[50:53]\n"
    "ds_read_b128 v[252:255], v20 offset:2560\n"
    "s_waitcnt lgkmcnt(15)\n"
    "v_mfma_f32_16x16x32_bf16 v[182:185], a[32:35], v[116:119], v[182:185]\n"
    "s_waitcnt lgkmcnt(15)\n"
    "v_mfma_f32_16x16x32_bf16 v[182:185], a[36:39], v[84:87], v[182:185]\n"
    "s_waitcnt lgkmcnt(15)\n"
    "v_mfma_f32_16x16x32_bf16 v[182:185], a[40:43], v[88:91], v[182:185]\n"
    "s_waitcnt lgkmcnt(15)\n"
    "v_mfma_f32_16x16x32_bf16 v[182:185], a[44:47], v[92:95], v[182:185]\n"
    "s_waitcnt lgkmcnt(15)\n"
    "v_mfma_f32_16x16x32_bf16 v[182:185], a[48:51], v[96:99], v[182:185]\n"
    "s_waitcnt lgkmcnt(15)\n"
    "v_mfma_f32_16x16x32_bf16 v[182:185], a[52:55], v[100:103], v[182:185]\n"
    "s_waitcnt lgkmcnt(15)\n"
    "v_mfma_f32_16x16x32_bf16 v[182:185], a[56:59], v[104:107], v[182:185]\n"
    "s_waitcnt lgkmcnt(15)\n"
    "v_mfma_f32_16x16x32_bf16 v[182:185], a[60:63], v[108:111], v[182:185]\n"
    "s_waitcnt lgkmcnt(15)\n"
    "v_mfma_f32_16x16x32_bf16 v[182:185], a[0:3], v[112:115], v[182:185]\n"
    "s_waitcnt lgkmcnt(15)\n"
    "v_mfma_f32_16x16x32_bf16 v[182:185], a[4:7], v[124:127], v[182:185]\n"
    "s_waitcnt lgkmcnt(15)\n"
    "v_mfma_f32_16x16x32_bf16 v[182:185], a[8:11], v[128:131], v[182:185]\n"
    "s_waitcnt lgkmcnt(15)\n"
    "v_mfma_f32_16x16x32_bf16 v[182:185], a[12:15], v[132:135], v[182:185]\n"
    "s_waitcnt lgkmcnt(15)\n"
    "v_mfma_f32_16x16x32_bf16 v[182:185], a[16:19], v[136:139], v[182:185]\n"
    "s_waitcnt lgkmcnt(15)\n"
    "v_mfma_f32_16x16x32_bf16 v[182:185], a[20:23], v[140:143], v[182:185]\n"
    "s_waitcnt lgkmcnt(15)\n"
    "v_mfma_f32_16x16x32_bf16 v[182:185], a[24:27], v[144:147], v[182:185]\n"
    "s_waitcnt lgkmcnt(15)\n"
    "v_mfma_f32_16x16x32_bf16 v[182:185], a[28:31], v[148:151], v[182:185]\n"
    "v_mfma_f32_16x16x32_bf16 v[16:19], a[96:99], v[116:119], v[16:19]\n"
    "v_mfma_f32_16x16x32_bf16 v[16:19], a[100:103], v[84:87], v[16:19]\n"
    "s_nop 5\n"
    "ds_write_b128 v23, v[182:185] offset:1024\n"
    "v_mfma_f32_16x16x32_bf16 v[16:19], a[104:107], v[88:91], v[16:19]\n"
    "v_mfma_f32_16x16x32_bf16 v[16:19], a[108:111], v[92:95], v[16:19]\n"
    "v_mfma_f32_16x16x32_bf16 v[16:19], a[112:115], v[96:99], v[16:19]\n"
    "v_mfma_f32_16x16x32_bf16 v[16:19], a[116:119], v[100:103], v[16:19]\n"
    "s_waitcnt lgkmcnt(0)\n"
    "s_barrier\n"
    "v_mfma_f32_16x16x32_bf16 v[16:19], a[120:123], v[104:107], v[16:19]\n"
    "ds_read2st64_b32 v[152:153], v24 offset0:4 offset1:36\n"
    "ds_read2st64_b32 v[154:155], v24 offset0:68 offset1:100\n"
    "v_mfma_f32_16x16x32_bf16 v[16:19], a[124:127], v[108:111], v[16:19]\n"
    "v_mfma_f32_16x16x32_bf16 v[16:19], a[64:67], v[112:115], v[16:19]\n"
    "v_mfma_f32_16x16x32_bf16 v[16:19], a[68:71], v[124:127], v[16:19]\n"
    "v_mfma_f32_16x16x32_bf16 v[16:19], a[72:75], v[128:131], v[16:19]\n"
    "v_mfma_f32_16x16x32_bf16 v[16:19], a[76:79], v[132:135], v[16:19]\n"
    "v_mfma_f32_16x16x32_bf16 v[16:19], a[80:83], v[136:139], v[16:19]\n"
    "v_mfma_f32_16x16x32_bf16 v[16:19], a[84:87], v[140:143], v[16:19]\n"
    "s_waitcnt lgkmcnt(1)\n"
    "v_add_f32 v152, v152, v32\n"
    "v_add_f32 v153, v153, v33\n"
    "v_mfma_f32_16x16x32_bf16 v[16:19], a[88:91], v[144:147], v[16:19]\n"
    "s_waitcnt lgkmcnt(0)\n"
    "v_add_f32 v154, v154, v34\n"
    "v_add_f32 v155, v155, v35\n"
    "v_mfma_f32_16x16x32_bf16 v[16:19], a[92:95], v[148:151], v[16:19]\n"
    "v_cvt_pk_bf16_f32 v152, v152, v153\n"
    "v_cvt_pk_bf16_f32 v153, v154, v155\n"
    "Lpg9_%=:\n"
    "s_cbranch_execz Ltb4_2_0_%=\n"
    "v_mfma_f32_16x16x32_bf16 v[50:53], a[160:163], v[116:119], v[50:53]\n"
    "buffer_store_dwordx2 v[152:153], v27, s[24:27], s56 offen\n"
    "ds_read_b128 v[116:119], v20 offset:3520\n"
    "v_mfma_f32_16x16x32_bf16 v[50:53], a[164:167], v[84:87], v[50:53]\n"
    "ds_read_b128 v[84:87], v20 offset:3456\n"
    "v_mfma_f32_16x16x32_bf16 v[50:53], a[168:171], v[88:91], v[50:53]\n"
    "ds_read_b128 v[88:91], v20 offset:3392\n"
    "v_mfma_f32_16x16x32_bf16 v[50:53], a[172:175], v[92:95], v[50:53]\n"
    "ds_read_b128 v[92:95], v20 offset:3328\n"
    "v_mfma_f32_16x16x32_bf16 v[50:53], a[176:179], v[96:99], v[50:53]\n"
    "ds_read_b128 v[96:99], v20 offset:3264\n"
    "v_mfma_f32_16x16x32_bf16 v[50:53], a[180:183], v[100:103], v[50:53]\n"
    "ds_read_b128 v[100:103], v20 offset:3200\n"
    "v_mfma_f32_16x16x32_bf16 v[50:53], a[184:187], v[104:107], v[50:53]\n"
    "ds_read_b128 v[104:107], v20 offset:3136\n"
    "v_mfma_f32_16x16x32_bf16 v[50:53], a[188:191], v[108:111], v[50:53]\n"
    "ds_read_b128 v[108:111], v20 offset:3072\n"
    "v_mfma_f32_16x16x32_bf16 v[50:53], a[128:131], v[112:115], v[50:53]\n"
    "ds_read_b128 v[112:115], v20 offset:4032\n"
    "v_mfma_f32_16x16x32_bf16 v[50:53], a[132:135], v[124:127], v[50:53]\n"
    "ds_read_b128 v[124:127], v20 offset:3968\n"
    "v_mfma_f32_16x16x32_bf16 v[50:53], a[136:139], v[128:131], v[50:53]\n"
    "ds_read_b128 v[128:131], v20 offset:3904\n"
    "v_mfma_f32_16x16x32_bf16 v[50:53], a[140:143], v[132:135], v[50:53]\n"
    "ds_read_b128 v[132:135], v20 offset:3840\n"
    "v_mfma_f32_16x16x32_bf16 v[50:53], a[144:147], v[136:139], v[50:53]\n"
    "ds_read_b128 v[136:139], v20 offset:3776\n"
    "v_mfma_f32_16x16x32_bf16 v[50:53], a[148:151], v[140:143], v[50:53]\n"
    "ds_read_b128 v[140:143], v20 offset:3712\n"
    "v_mfma_f32_16x16x32_bf16 v[50:53], a[152:155], v[144:147], v[50:53]\n"
    "ds_read_b128 v[144:147], v20 offset:3648\n"
    "v_mfma_f32_16x16x32_bf16 v[50:53], a[156:159], v[148:151], v[50:53]\n"
    "ds_read_b128 v[148:151], v20 offset:3584\n"
    "v_mfma_f32_16x16x32_bf16 v[16:19], a[32:35], v[192:195], v[16:19]\n"
    "v_mfma_f32_16x16x32_bf16 v[16:19], a[36:39], v[196:199], v[16:19]\n"
    "v_mfma_f32_16x16x32_bf16 v[16:19], a[40:43], v[200:203], v[16:19]\n"
    "v_mfma_f32_16x16x32_bf16 v[16:19], a[44:47], v[204:207], v[16:19]\n"
    "v_mfma_f32_16x16x32_bf16 v[16:19], a[48:51], v[208:211], v[16:19]\n"
    "v_mfma_f32_16x16x32_bf16 v[16:19], a[52:55], v[212:215], v[16:19]\n"
    "v_mfma_f32_16x16x32_bf16 v[16:19], a[56:59], v[216:219], v[16:19]\n"
    "v_mfma_f32_16x16x32_bf16 v[16:19], a[60:63], v[220:223], v[16:19]\n"
    "v_mfma_f32_16x16x32_bf16 v[16:19], a[0:3], v[224:227], v[16:19]\n"
    "v_mfma_f32_16x16x32_bf16 v[16:19], a[4:7], v[228:231], v[16:19]\n"
    "v_mfma_f32_16x16x32_bf16 v[16:19], a[8:11], v[232:235], v[16:19]\n"
    "v_mfma_f32_16x16x32_bf16 v[16:19], a[12:15], v[236:239], v[16:19]\n"
    "v_mfma_f32_16x16x32_bf16 v[16:19], a[16:19], v[240:243], v[16:19]\n"
    "v_mfma_f32_16x16x32_bf16 v[16:19], a[20:23], v[244:247], v[16:19]\n"
    "v_mfma_f32_16x16x32_bf16 v[16:19], a[24:27], v[248:251], v[16:19]\n"
    "v_mfma_f32_16x16x32_bf16 v[16:19], a[28:31], v[252:255], v[16:19]\n"
    "v_mfma_f32_16x16x32_bf16 v[50:53], a[96:99], v[192:195], v[50:53]\n"
    "v_mfma_f32_16x16x32_bf16 v[50:53], a[100:103], v[196:199], v[50:53]\n"
    "s_nop 5\n"
    "ds_write_b128 v23, v[16:19] offset:2048\n"
    "v_mfma_f32_16x16x32_bf16 v[50:53], a[104:107], v[200:203], v[50:53]\n"
    "v_mfma_f32_16x16x32_bf16 v[50:53], a[108:111], v[204:207], v[50:53]\n"
    "v_mfma_f32_16x16x32_bf16 v[50:53], a[112:115], v[208:211], v[50:53]\n"
    "v_mfma_f32_16x16x32_bf16 v[50:53], a[116:119], v[212:215], v[50:53]\n"
    "s_waitcnt lgkmcnt(0)\n"
    "s_barrier\n"
    "v_mfma_f32_16x16x32_bf16 v[50:53], a[120:123], v[216:219], v[50:53]\n"
    "ds_read2st64_b32 v[156:157], v24 offset0:8 offset1:40\n"
    "ds_read2st64_b32 v[158:159], v24 offset0:72 offset1:104\n"
    "v_mfma_f32_16x16x32_bf16 v[50:53], a[124:127], v[220:223], v[50:53]\n"
    "v_mfma_f32_16x16x32_bf16 v[50:53], a[64:67], v[224:227], v[50:53]\n"
    "v_mfma_f32_16x16x32_bf16 v[50:53], a[68:71], v[228:231], v[50:53]\n"
    "v_mfma_f32_16x16x32_bf16 v[50:53], a[72:75], v[232:235], v[50:53]\n"
    "v_mfma_f32_16x16x32_bf16 v[50:53], a[76:79], v[236:239], v[50:53]\n"
    "v_mfma_f32_16x16x32_bf16 v[50:53], a[80:83], v[240:243], v[50:53]\n"
    "v_mfma_f32_16x16x32_bf16 v[50:53], a[84:87], v[244:247], v[50:53]\n"
    "s_waitcnt lgkmcnt(1)\n"
    "v_add_f32 v156, v156, v36\n"
    "v_add_f32 v157, v157, v37\n"
    "v_mfma_f32_16x16x32_bf16 v[50:53], a[88:91], v[248:251], v[50:53]\n"
    "s_waitcnt lgkmcnt(0)\n"
    "v_add_f32 v158, v158, v38\n"
    "v_add_f32 v159, v159, v39\n"
    "v_mfma_f32_16x16x32_bf16 v[50:53], a[92:95], v[252:255], v[50:53]\n"
    "v_cvt_pk_bf16_f32 v156, v156, v157\n"
    "v_cvt_pk_bf16_f32 v157, v158, v159\n"
    "v_mfma_f32_16x16x32_bf16 v[50:53], a[32:35], v[116:119], v[50:53]\n"
    "buffer_store_dwordx2 v[156:157], v27, s[24:27], s55 offen\n"
    "v_mfma_f32_16x16x32_bf16 v[50:53], a[36:39], v[84:87], v[50:53]\n"
    "v_mfma_f32_16x16x32_bf16 v[50:53], a[40:43], v[88:91], v[50:53]\n"
    "v_mfma_f32_16x16x32_bf16 v[50:53], a[44:47], v[92:95], v[50:53]\n"
    "v_mfma_f32_16x16x32_bf16 v[50:53], a[48:51], v[96:99], v[50:53]\n"
    "v_mfma_f32_16x16x32_bf16 v[50:53], a[52:55], v[100:103], v[50:53]\n"
    "v_mfma_f32_16x16x32_bf16 v[50:53], a[56:59], v[104:107], v[50:53]\n"
    "v_mfma_f32_16x16x32_bf16 v[50:53], a[60:63], v[108:111], v[50:53]\n"
    "v_mfma_f32_16x16x32_bf16 v[50:53], a[0:3], v[112:115], v[50:53]\n"
    "v_mfma_f32_16x16x32_bf16 v[50:53], a[4:7], v[124:127], v[50:53]\n"
    "v_mfma_f32_16x16x32_bf16 v[50:53], a[8:11], v[128:131], v[50:53]\n"
    "v_mfma_f32_16x16x32_bf16 v[50:53], a[12:15], v[132:135], v[50:53]\n"
    "v_mfma_f32_16x16x32_bf16 v[50:53], a[16:19], v[136:139], v[50:53]\n"
    "v_mfma_f32_16x16x32_bf16 v[50:53], a[20:23], v[140:143], v[50:53]\n"
    "v_mfma_f32_16x16x32_bf16 v[50:53], a[24:27], v[144:147], v[50:53]\n"
    "v_mfma_f32_16x16x32_bf16 v[50:53], a[28:31], v[148:151], v[50:53]\n"
    "s_nop 7\n"
    "ds_write_b128 v23, v[50:53] offset:3072\n"
    "s_waitcnt lgkmcnt(0)\n"
    "s_barrier\n"
    "ds_read_b128 v[160:163], v186\n"
    "ds_read2st64_b32 v[164:165], v24 offset0:12 offset1:44\n"
    "ds_read2st64_b32 v[166:167], v24 offset0:76 offset1:108\n"
    "s_waitcnt lgkmcnt(2)\n"
    "v_or3_b32 v186, v160, v161, v162\n"
    "v_or_b32 v186, v186, v163\n"
    "s_nop 0\n"
    "v_readfirstlane_b32 s33, v186\n"
    "s_waitcnt lgkmcnt(1)\n"
    "v_add_f32 v164, v164, v40\n"
    "s_cselect_b32 s33, s33, 0\n"
    "s_add_u32 s56, s55, 0x80000\n"
    "v_add_f32 v165, v165, v41\n"
    "s_waitcnt lgkmcnt(0)\n"
    "v_add_f32 v166, v166, v42\n"
    "v_add_f32 v167, v167, v43\n"
    "v_cvt_pk_bf16_f32 v164, v164, v165\n"
    "v_cvt_pk_bf16_f32 v165, v166, v167\n"
    "buffer_store_dwordx2 v[164:165], v27, s[24:27], s56 offen\n"
    "s_cmp_lg_u32 s33, 0\n"
    "s_cbranch_scc1 Lslow2_1_%=\n"
    "s_waitcnt vmcnt(0) lgkmcnt(0)\n"
    "s_endpgm\n"
    "Lslow_4_%=:\n"
    "s_waitcnt vmcnt(0) lgkmcnt(0)\n"
    "s_mov_b32 s28, 1\n"
    "v_and_b32 v189, 7, v8\n"
    "v_lshlrev_b32 v189, 8, v189\n"
    "s_mov_b32 s29, 22784\n"
    "s_mov_b32 s76, 0\n"
    "Lslp_37_%=:\n"
    "buffer_load_dword v190, v189, s[20:23], s29 offen sc1\n"
    "s_waitcnt vmcnt(0)\n"
    "v_cmp_le_u32 vcc, 32, v190\n"
    "s_nop 1\n"
    "s_cmp_eq_u64 vcc, exec\n"
    "s_cbranch_scc1 Lslx_38_%=\n"
    "s_sleep 4\n"
    "s_add_u32 s76, s76, 1\n"
    "s_cmp_lt_u32 s76, 0x10000\n"
    "s_cbranch_scc1 Lslp_37_%=\n"
    "Lslx_38_%=:\n"
    "s_nop 4\n"
    "s_branch Lhead_5_%=\n"
    "Lslow2_1_%=:\n"
    "s_waitcnt vmcnt(0) lgkmcnt(0)\n"
    "s_cmp_eq_u32 s32, 0\n"
    "s_cbranch_scc0 Ls2r_41_%=\n"
    "s_mov_b32 s32, 1\n"
    "v_and_b32 v189, 7, v8\n"
    "v_lshlrev_b32 v189, 8, v189\n"
    "s_mov_b32 s76, 0\n"
    "Ls2p_39_%=:\n"
    "buffer_load_dword v190, v189, s[20:23], s13 offen sc1\n"
    "s_waitcnt vmcnt(0)\n"
    "v_cmp_le_u32 vcc, 32, v190\n"
    "s_nop 1\n"
    "s_cmp_eq_u64 vcc, exec\n"
    "s_cbranch_scc1 Ls2x_40_%=\n"
    "s_sleep 4\n"
    "s_add_u32 s76, s76, 1\n"
    "s_cmp_lt_u32 s76, 0x10000\n"
    "s_cbranch_scc1 Ls2p_39_%=\n"
    "Ls2x_40_%=:\n"
    "s_branch Ls2j_43_%=\n"
    "Ls2r_41_%=:\n"
    "s_mov_b32 s32, 2\n"
    "v_lshrrev_b32 v48, 3, v0\n"
    "v_lshlrev_b32 v50, 16, v48\n"
    "v_and_b32 v48, 7, v0\n"
    "v_lshl_add_u32 v50, v48, 4, v50\n"
    "v_lshlrev_b32 v51, 4, v8\n"
    "s_lshl_b32 s29, s7, 12\n"
    "v_add_u32 v51, s29, v51\n"
    "v_mov_b32 v16, 0\n"
    "v_mov_b32 v17, 0\n"
    "v_mov_b32 v18, 0\n"
    "v_mov_b32 v19, 0\n"
    "v_mov_b32 v20, 0\n"
    "v_mov_b32 v21, 0\n"
    "v_mov_b32 v22, 0\n"
    "v_mov_b32 v23, 0\n"
    "v_mov_b32 v24, 0\n"
    "v_mov_b32 v25, 0\n"
    "v_mov_b32 v26, 0\n"
    "v_mov_b32 v27, 0\n"
    "v_mov_b32 v28, 0\n"
    "v_mov_b32 v29, 0\n"
    "v_mov_b32 v30, 0\n"
    "v_mov_b32 v31, 0\n"
    "s_lshl_b32 s29, s12, 21\n"
    "s_add_u32 s29, s29, 5242880\n"
    "s_mov_b32 s76, 0\n"
    "Ls2l_42_%=:\n"
    "buffer_load_dwordx4 v[32:35], v50, s[20:23], s29 offen offset:0 sc1\n"
    "buffer_load_dwordx4 v[36:39], v50, s[20:23], s29 offen offset:128 sc1\n"
    "buffer_load_dwordx4 v[40:43], v50, s[20:23], s29 offen offset:256 sc1\n"
    "buffer_load_dwordx4 v[44:47], v50, s[20:23], s29 offen offset:384 sc1\n"
    "s_waitcnt vmcnt(0)\n"
    "v_lshlrev_b32 v48, 16, v32\n"
    "v_and_b32 v49, 0xffff0000, v32\n"
    "v_add_f32 v16, v16, v48\n"
    "v_add_f32 v17, v17, v49\n"
    "v_fmac_f32 v24, v48, v48\n"
    "v_fmac_f32 v25, v49, v49\n"
    "v_lshlrev_b32 v48, 16, v33\n"
    "v_and_b32 v49, 0xffff0000, v33\n"
    "v_add_f32 v18, v18, v48\n"
    "v_add_f32 v19, v19, v49\n"
    "v_fmac_f32 v26, v48, v48\n"
    "v_fmac_f32 v27, v49, v49\n"
    "v_lshlrev_b32 v48, 16, v34\n"
    "v_and_b32 v49, 0xffff0000, v34\n"
    "v_add_f32 v20, v20, v48\n"
    "v_add_f32 v21, v21, v49\n"
    "v_fmac_f32 v28, v48, v48\n"
    "v_fmac_f32 v29, v49, v49\n"
    "v_lshlrev_b32 v48, 16, v35\n"
    "v_and_b32 v49, 0xffff0000, v35\n"
    "v_add_f32 v22, v22, v48\n"
    "v_add_f32 v23, v23, v49\n"
    "v_fmac_f32 v30, v48, v48\n"
    "v_fmac_f32 v31, v49, v49\n"
    "v_lshlrev_b32 v48, 16, v36\n"
    "v_and_b32 v49, 0xffff0000, v36\n"
    "v_add_f32 v16, v16, v48\n"
    "v_add_f32 v17, v17, v49\n"
    "v_fmac_f32 v24, v48, v48\n"
    "v_fmac_f32 v25, v49, v49\n"
    "v_lshlrev_b32 v48, 16, v37\n"
    "v_and_b32 v49, 0xffff0000, v37\n"
    "v_add_f32 v18, v18, v48\n"
    "v_add_f32 v19, v19, v49\n"
    "v_fmac_f32 v26, v48, v48\n"
    "v_fmac_f32 v27, v49, v49\n"
    "v_lshlrev_b32 v48, 16, v38\n"
    "v_and_b32 v49, 0xffff0000, v38\n"
    "v_add_f32 v20, v20, v48\n"
    "v_add_f32 v21, v21, v49\n"
    "v_fmac_f32 v28, v48, v48\n"
    "v_fmac_f32 v29, v49, v49\n"
    "v_lshlrev_b32 v48, 16, v39\n"
    "v_and_b32 v49, 0xffff0000, v39\n"
    "v_add_f32 v22, v22, v48\n"
    "v_add_f32 v23, v23, v49\n"
    "v_fmac_f32 v30, v48, v48\n"
    "v_fmac_f32 v31, v49, v49\n"
    "v_lshlrev_b32 v48, 16, v40\n"
    "v_and_b32 v49, 0xffff0000, v40\n"
    "v_add_f32 v16, v16, v48\n"
    "v_add_f32 v17, v17, v49\n"
    "v_fmac_f32 v24, v48, v48\n"
    "v_fmac_f32 v25, v49, v49\n"
    "v_lshlrev_b32 v48, 16, v41\n"
    "v_and_b32 v49, 0xffff0000, v41\n"
    "v_add_f32 v18, v18, v48\n"
    "v_add_f32 v19, v19, v49\n"
    "v_fmac_f32 v26, v48, v48\n"
    "v_fmac_f32 v27, v49, v49\n"
    "v_lshlrev_b32 v48, 16, v42\n"
    "v_and_b32 v49, 0xffff0000, v42\n"
    "v_add_f32 v20, v20, v48\n"
    "v_add_f32 v21, v21, v49\n"
    "v_fmac_f32 v28, v48, v48\n"
    "v_fmac_f32 v29, v49, v49\n"
    "v_lshlrev_b32 v48, 16, v43\n"
    "v_and_b32 v49, 0xffff0000, v43\n"
    "v_add_f32 v22, v22, v48\n"
    "v_add_f32 v23, v23, v49\n"
    "v_fmac_f32 v30, v48, v48\n"
    "v_fmac_f32 v31, v49, v49\n"
    "v_lshlrev_b32 v48, 16, v44\n"
    "v_and_b32 v49, 0xffff0000, v44\n"
    "v_add_f32 v16, v16, v48\n"
    "v_add_f32 v17, v17, v49\n"
    "v_fmac_f32 v24, v48, v48\n"
    "v_fmac_f32 v25, v49, v49\n"
    "v_lshlrev_b32 v48, 16, v45\n"
    "v_and_b32 v49, 0xffff0000, v45\n"
    "v_add_f32 v18, v18, v48\n"
    "v_add_f32 v19, v19, v49\n"
    "v_fmac_f32 v26, v48, v48\n"
    "v_fmac_f32 v27, v49, v49\n"
    "v_lshlrev_b32 v48, 16, v46\n"
    "v_and_b32 v49, 0xffff0000, v46\n"
    "v_add_f32 v20, v20, v48\n"
    "v_add_f32 v21, v21, v49\n"
    "v_fmac_f32 v28, v48, v48\n"
    "v_fmac_f32 v29, v49, v49\n"
    "v_lshlrev_b32 v48, 16, v47\n"
    "v_and_b32 v49, 0xffff0000, v47\n"
    "v_add_f32 v22, v22, v48\n"
    "v_add_f32 v23, v23, v49\n"
    "v_fmac_f32 v30, v48, v48\n"
    "v_fmac_f32 v31, v49, v49\n"
    "s_add_u32 s29, s29, 512\n"
    "s_add_u32 s76, s76, 1\n"
    "s_cmp_lt_u32 s76, 128\n"
    "s_cbranch_scc1 Ls2l_42_%=\n"
    "s_lshl_b32 s29, s6, 15\n"
    "s_add_u32 s29, s29, 16777216\n"
    "v_cvt_f64_f32 v[32:33], v16\n"
    "v_add_f64 v[32:33], v[32:33], -s[38:39]\n"
    "v_cvt_f64_f32 v[34:35], v17\n"
    "v_add_f64 v[34:35], v[34:35], -s[38:39]\n"
    "s_nop 4\n"
    "buffer_store_dwordx4 v[32:35], v51, s[20:23], s29 offen offset:0 sc1\n"
    "s_waitcnt vmcnt(0)\n"
    "v_cvt_f64_f32 v[32:33], v18\n"
    "v_add_f64 v[32:33], v[32:33], -s[38:39]\n"
    "v_cvt_f64_f32 v[34:35], v19\n"
    "v_add_f64 v[34:35], v[34:35], -s[38:39]\n"
    "s_nop 4\n"
    "buffer_store_dwordx4 v[32:35], v51, s[20:23], s29 offen offset:1024 sc1\n"
    "s_waitcnt vmcnt(0)\n"
    "v_cvt_f64_f32 v[32:33], v20\n"
    "v_add_f64 v[32:33], v[32:33], -s[38:39]\n"
    "v_cvt_f64_f32 v[34:35], v21\n"
    "v_add_f64 v[34:35], v[34:35], -s[38:39]\n"
    "s_nop 4\n"
    "buffer_store_dwordx4 v[32:35], v51, s[20:23], s29 offen offset:2048 sc1\n"
    "s_waitcnt vmcnt(0)\n"
    "v_cvt_f64_f32 v[32:33], v22\n"
    "v_add_f64 v[32:33], v[32:33], -s[38:39]\n"
    "v_cvt_f64_f32 v[34:35], v23\n"
    "v_add_f64 v[34:35], v[34:35], -s[38:39]\n"
    "s_nop 4\n"
    "buffer_store_dwordx4 v[32:35], v51, s[20:23], s29 offen offset:3072 sc1\n"
    "s_waitcnt vmcnt(0)\n"
    "s_add_u32 s29, s29, 16384\n"
    "v_cvt_f64_f32 v[32:33], v24\n"
    "v_add_f64 v[32:33], v[32:33], -s[38:39]\n"
    "v_cvt_f64_f32 v[34:35], v25\n"
    "v_add_f64 v[34:35], v[34:35], -s[38:39]\n"
    "s_nop 4\n"
    "buffer_store_dwordx4 v[32:35], v51, s[20:23], s29 offen offset:0 sc1\n"
    "s_waitcnt vmcnt(0)\n"
    "v_cvt_f64_f32 v[32:33], v26\n"
    "v_add_f64 v[32:33], v[32:33], -s[38:39]\n"
    "v_cvt_f64_f32 v[34:35], v27\n"
    "v_add_f64 v[34:35], v[34:35], -s[38:39]\n"
    "s_nop 4\n"
    "buffer_store_dwordx4 v[32:35], v51, s[20:23], s29 offen offset:1024 sc1\n"
    "s_waitcnt vmcnt(0)\n"
    "v_cvt_f64_f32 v[32:33], v28\n"
    "v_add_f64 v[32:33], v[32:33], -s[38:39]\n"
    "v_cvt_f64_f32 v[34:35], v29\n"
    "v_add_f64 v[34:35], v[34:35], -s[38:39]\n"
    "s_nop 4\n"
    "buffer_store_dwordx4 v[32:35], v51, s[20:23], s29 offen offset:2048 sc1\n"
    "s_waitcnt vmcnt(0)\n"
    "v_cvt_f64_f32 v[32:33], v30\n"
    "v_add_f64 v[32:33], v[32:33], -s[38:39]\n"
    "v_cvt_f64_f32 v[34:35], v31\n"
    "v_add_f64 v[34:35], v[34:35], -s[38:39]\n"
    "s_nop 4\n"
    "buffer_store_dwordx4 v[32:35], v51, s[20:23], s29 offen offset:3072 sc1\n"
    "s_waitcnt vmcnt(0)\n"
    "Ls2j_43_%=:\n"
    "s_nop 4\n"
    "s_cmp_eq_u32 s12, 0\n"
    "s_cbranch_scc1 Lcvhead0_2_%=\n"
    "s_branch Lcvhead1_3_%=\n"
    "Lend_%=:\n"
    :
    : "s"(__builtin_amdgcn_kernarg_segment_ptr()), "s"(blockIdx.x), "v"(threadIdx.x), "s"((__attribute__((address_space(3))) char*)smem)
    : "v1", "v2", "v3", "v4", "v5", "v6", "v7", "v8", "v9", "v10", "v11", "v12", "v13", "v14", "v15", "v16", "v17", "v18", "v19", "v20", "v21", "v22", "v23", "v24", "v25", "v26", "v27", "v28", "v29", "v30", "v31", "v32", "v33", "v34", "v35", "v36", "v37", "v38", "v39", "v40", "v41", "v42", "v43", "v44", "v45", "v46", "v47", "v48", "v49", "v50", "v51", "v52", "v53", "v54", "v55", "v56", "v57", "v58", "v59", "v60", "v61", "v62", "v63", "v64", "v65", "v66", "v67", "v68", "v69", "v70", "v71", "v72", "v73", "v74", "v75", "v76", "v77", "v78", "v79", "v80", "v81", "v82", "v83", "v84", "v85", "v86", "v87", "v88", "v89", "v90", "v91", "v92", "v93", "v94", "v95", "v96", "v97", "v98", "v99", "v100", "v101", "v102", "v103", "v104", "v105", "v106", "v107", "v108", "v109", "v110", "v111", "v112", "v113", "v114", "v115", "v116", "v117", "v118", "v119", "v120", "v121", "v122", "v123", "v124", "v125", "v126", "v127", "v128", "v129", "v130", "v131", "v132", "v133", "v134", "v135", "v136", "v137", "v138", "v139", "v140", "v141", "v142", "v143", "v144", "v145", "v146", "v147", "v148", "v149", "v150", "v151", "v152", "v153", "v154", "v155", "v156", "v157", "v158", "v159", "v160", "v161", "v162", "v163", "v164", "v165", "v166", "v167", "v168", "v169", "v170", "v171", "v172", "v173", "v174", "v175", "v176", "v177", "v178", "v179", "v180", "v181", "v182", "v183", "v184", "v185", "v186", "v187", "v188", "v189", "v190", "v191", "v192", "v193", "v194", "v195", "v196", "v197", "v198", "v199", "v200", "v201", "v202", "v203", "v204", "v205", "v206", "v207", "v208", "v209", "v210", "v211", "v212", "v213", "v214", "v215", "v216", "v217", "v218", "v219", "v220", "v221", "v222", "v223", "v224", "v225", "v226", "v227", "v228", "v229", "v230", "v231", "v232", "v233", "v234", "v235", "v236", "v237", "v238", "v239", "v240", "v241", "v242", "v243", "v244", "v245", "v246", "v247", "v248", "v249", "v250", "v251", "v252", "v253", "v254", "v255",
      "a0", "a1", "a2", "a3", "a4", "a5", "a6", "a7", "a8", "a9", "a10", "a11", "a12", "a13", "a14", "a15", "a16", "a17", "a18", "a19", "a20", "a21", "a22", "a23", "a24", "a25", "a26", "a27", "a28", "a29", "a30", "a31", "a32", "a33", "a34", "a35", "a36", "a37", "a38", "a39", "a40", "a41", "a42", "a43", "a44", "a45", "a46", "a47", "a48", "a49", "a50", "a51", "a52", "a53", "a54", "a55", "a56", "a57", "a58", "a59", "a60", "a61", "a62", "a63", "a64", "a65", "a66", "a67", "a68", "a69", "a70", "a71", "a72", "a73", "a74", "a75", "a76", "a77", "a78", "a79", "a80", "a81", "a82", "a83", "a84", "a85", "a86", "a87", "a88", "a89", "a90", "a91", "a92", "a93", "a94", "a95", "a96", "a97", "a98", "a99", "a100", "a101", "a102", "a103", "a104", "a105", "a106", "a107", "a108", "a109", "a110", "a111", "a112", "a113", "a114", "a115", "a116", "a117", "a118", "a119", "a120", "a121", "a122", "a123", "a124", "a125", "a126", "a127", "a128", "a129", "a130", "a131", "a132", "a133", "a134", "a135", "a136", "a137", "a138", "a139", "a140", "a141", "a142", "a143", "a144", "a145", "a146", "a147", "a148", "a149", "a150", "a151", "a152", "a153", "a154", "a155", "a156", "a157", "a158", "a159", "a160", "a161", "a162", "a163", "a164", "a165", "a166", "a167", "a168", "a169", "a170", "a171", "a172", "a173", "a174", "a175", "a176", "a177", "a178", "a179", "a180", "a181", "a182", "a183", "a184", "a185", "a186", "a187", "a188", "a189", "a190", "a191", "a192", "a193", "a194", "a195", "a196", "a197", "a198", "a199", "a200", "a201", "a202", "a203", "a204", "a205", "a206", "a207", "a208", "a209", "a210", "a211", "a212", "a213", "a214", "a215", "a216", "a217", "a218", "a219", "a220", "a221", "a222", "a223", "a224", "a225", "a226", "a227", "a228", "a229", "a230", "a231", "a232", "a233", "a234", "a235", "a236", "a237", "a238", "a239", "a240", "a241", "a242", "a243", "a244", "a245", "a246", "a247", "a248", "a249", "a250", "a251", "a252", "a253", "a254", "a255",
      "s4", "s5", "s6", "s7", "s8", "s9", "s10", "s11", "s12", "s13", "s14", "s15", "s16", "s17", "s18", "s19", "s20", "s21", "s22", "s23", "s24", "s25", "s26", "s27", "s28", "s29", "s30", "s31", "s32", "s33", "s34", "s35", "s36", "s37", "s38", "s39", "s40", "s41", "s42", "s43", "s44", "s45", "s46", "s47", "s48", "s49", "s50", "s51", "s52", "s53", "s54", "s55", "s56", "s57", "s58", "s59", "s60", "s61", "s62", "s63", "s64", "s65", "s66", "s67", "s68", "s69", "s70", "s71", "s72", "s73", "s74", "s75", "s76", "s77", "s78", "s79", "s80", "s81", "s82", "s83", "s84", "s85", "s86", "s87", "s88", "s89", "s90", "s91", "s92", "s93", "s94", "s95", "s96", "s97", "s98", "s99", "s100", "s101", "vcc", "m0", "memory");
}

extern "C" void kernel_launch(void* const* d_in, const int* in_sizes, int n_in, void* d_out, int out_size, void* d_ws, size_t ws_size,
                              hipStream_t stream) {
  interleave_mega<<<256, 256, 0, stream>>>(d_in[0], d_in[1], d_in[2], d_in[4], d_in[5], d_in[6], d_in[7], d_in[8], d_in[9], d_in[10],
                                           d_in[11], d_out, d_ws);
}
```
